```python
import torch

def _softmax(x, axis=-1):
    return torch.softmax(x, dim=axis)
def _gelu(x):
    return torch.nn.functional.gelu(x, approximate="tanh")
def _silu(x):
    return torch.nn.functional.silu(x)
def _sigmoid(x):
    return torch.sigmoid(x)
def _layernorm(x, axis=-1, eps=1e-5):
    m = x.mean(dim=axis, keepdim=True)
    v = x.var(dim=axis, keepdim=True, unbiased=False)
    return (x - m) / torch.sqrt(v + eps)
def _scatter_add(tgt, idx, src, axis=0):
    return tgt.clone().index_add_(axis, idx, src)
def _segment_sum(src, seg, n):
    out = torch.zeros((n,) + src.shape[1:], dtype=src.dtype)
    return out.index_add_(0, seg, src)
def _pad(x, pw):
    flat = tuple(v for pair in reversed(pw) for v in pair)
    return torch.nn.functional.pad(x, flat)

kernel_name = 'standalone__mha_block_cached__near_ridge'

def setup_inputs():

    _w = (lambda a: (lambda t: t.to(torch.bfloat16) if t.is_floating_point() else t)(torch.as_tensor(a)))
    B, Sq, D = 8, 256, 640
    H, Sk, Dh = 32, 1024, 192
    return {
        "x": _w((lambda *s: torch.randn(*s, dtype=torch.bfloat16) * 0.8)(B, Sq, D)),
        "k_cache": _w((lambda *s: torch.randn(*s, dtype=torch.bfloat16) * 0.8)(B, H, Sk, Dh)),
        "v_cache": _w((lambda *s: torch.randn(*s, dtype=torch.bfloat16) * 0.05)(B, H, Sk, Dh)),
        "wq": _w((lambda *s: torch.randn(*s, dtype=torch.bfloat16) * 0.8)(D, H * Dh) * (D ** -0.5)),
        "wo": _w((lambda *s: torch.randn(*s, dtype=torch.bfloat16) * 0.05)(H * Dh, D) * ((H * Dh) ** -0.5)),
    }

def reference(x, k_cache, v_cache, wq, wo):
    B, Sq, D = x.shape
    H, Dh = 32, 192
    q = (lambda x, *d: x.permute(*d))((x @ wq).reshape(B, Sq, H, Dh), 0, 2, 1, 3)
    scores = torch.einsum("bhqd,bhkd->bhqk", q, k_cache) * (Dh ** -0.5)
    p = _softmax(scores, axis=-1)
    o = torch.einsum("bhqk,bhkd->bhqd", p, v_cache)
    return (lambda x, *d: x.permute(*d))(o, 0, 2, 1, 3).reshape(B, Sq, H * Dh) @ wo
```

```cpp
#include <hip/hip_runtime.h>
#include <hip/hip_bf16.h>
typedef __attribute__((ext_vector_type(8))) short bf16x8;
typedef __attribute__((ext_vector_type(16))) float f32x16;
typedef unsigned short u16;
__device__ __forceinline__ float b2f(u16 x){ return __uint_as_float(((unsigned)x)<<16); }
__device__ __forceinline__ u16 f2b(float x){ __hip_bfloat16 b = __float2bfloat16(x); return *(u16*)&b; }
__global__ __launch_bounds__(64) void gemm_naive(const u16* A, const u16* B, u16* C, int M, int N, int K){
  int lane = threadIdx.x, r = lane&31, h = lane>>5;
  int tn = blockIdx.x, tm = blockIdx.y;
  f32x16 acc = {};
  for (int k0=0;k0<K;k0+=16){
    bf16x8 a = *(const bf16x8*)(A + (size_t)(tm*32+r)*K + k0 + 8*h);
    bf16x8 b;
    for (int j=0;j<8;j++) b[j] = (short)B[(size_t)(k0+8*h+j)*N + tn*32 + r];
    acc = __builtin_amdgcn_mfma_f32_32x32x16_bf16(a,b,acc,0,0,0);
  }
  for (int i=0;i<16;i++){ int row=(i&3)+8*(i>>2)+4*h; C[(size_t)(tm*32+row)*N + tn*32 + r] = f2b(acc[i]); }
}
__global__ __launch_bounds__(256) void attn_naive(const u16* Q, const u16* Kc, const u16* Vc, u16* O){
  __shared__ float p[1024]; __shared__ float q[192]; __shared__ float red[256];
  int row = blockIdx.x;
  int h = blockIdx.y; int b = row>>8; int t = threadIdx.x;
  if (t<192) q[t] = b2f(Q[(size_t)row*6144 + h*192 + t]);
  __syncthreads();
  const u16* Kb = Kc + (size_t)(b*32+h)*1024*192; const u16* Vb = Vc + (size_t)(b*32+h)*1024*192;
  float mx=-1e30f;
  for (int k=t;k<1024;k+=256){ float s=0; for(int d=0;d<192;d+=8){ bf16x8 kv=*(const bf16x8*)(Kb+k*192+d); for(int j=0;j<8;j++) s+=q[d+j]*b2f((u16)kv[j]); } s*=0.07216878364870322f; p[k]=s; mx=fmaxf(mx,s);}
  red[t]=mx; __syncthreads();
  for(int o=128;o>0;o>>=1){ if(t<o) red[t]=fmaxf(red[t],red[t+o]); __syncthreads(); }
  mx=red[0]; __syncthreads();
  float sum=0; for(int k=t;k<1024;k+=256){ float e=__expf(p[k]-mx); p[k]=e; sum+=e; }
  red[t]=sum; __syncthreads();
  for(int o=128;o>0;o>>=1){ if(t<o) red[t]+=red[t+o]; __syncthreads(); }
  sum=red[0];
  if (t<192){ float o=0; for(int k=0;k<1024;k++) o+=p[k]*b2f(Vb[k*192+t]); O[(size_t)row*6144+h*192+t]=f2b(o/sum); }
}
extern "C" void kernel_launch(void* const* d_in, const int* in_sizes, int n_in, void* d_out, int out_size, void* d_ws, size_t ws_size, hipStream_t stream){
  const u16* x=(const u16*)d_in[0]; const u16* k=(const u16*)d_in[1]; const u16* v=(const u16*)d_in[2]; const u16* wq=(const u16*)d_in[3]; const u16* wo=(const u16*)d_in[4];
  u16* Q=(u16*)d_ws; u16* O=Q+(size_t)2048*6144;
  gemm_naive<<<dim3(6144/32,2048/32),64,0,stream>>>(x,wq,Q,2048,6144,640);
  attn_naive<<<dim3(2048,32),256,0,stream>>>(Q,k,v,O);
  gemm_naive<<<dim3(640/32,2048/32),64,0,stream>>>(O,wo,(u16*)d_out,2048,640,6144);
}
```

```cpp
#include <hip/hip_runtime.h>
typedef unsigned short u16;

__global__ __launch_bounds__(256, 1) __attribute__((aligned(4096))) void mha_fused(const u16* __restrict__ x, const u16* __restrict__ kc,
                                                    const u16* __restrict__ vc, const u16* __restrict__ wq,
                                                    const u16* __restrict__ wo, char* ws, u16* out) {
  const unsigned bid = blockIdx.x;
  const unsigned w = __builtin_amdgcn_readfirstlane(threadIdx.x >> 6);
  asm volatile(
"s_mov_b32 s63, 0\n"
"v_mov_b32 v251, 0xff800000\n"
"Lstart%=:\n"
"s_load_dwordx8 s[8:15], %[ka], 0x0\n"
"s_load_dwordx4 s[16:19], %[ka], 0x20\n"
"s_load_dwordx2 s[20:21], %[ka], 0x30\n"
"s_lshr_b32 s22, %[bid], 3\n"
"s_and_b32 s25, %[bid], 1\n"
"s_lshl_b32 s25, s25, 2\n"
"s_lshr_b32 s28, %[bid], 6\n"
"s_add_u32 s25, s25, s28\n"
"s_and_b32 s26, %[bid], 6\n"
"s_lshl_b32 s26, s26, 2\n"
"s_and_b32 s28, s22, 7\n"
"s_add_u32 s26, s26, s28\n"
"s_and_b32 s23, s26, 3\n"
"s_lshr_b32 s24, s26, 2\n"
"s_lshl_b32 s28, s25, 2\n"
"s_add_u32 s28, s28, %[w]\n"
"s_mul_i32 s28, s28, 0x14000\n"
"s_mul_i32 s29, s26, 0x180\n"
"s_mov_b32 s38, 0x14000\n"
"s_mov_b32 s39, 0x20000\n"
"s_sub_u32 s42, 0x780000, s29\n"
"s_mov_b32 s43, 0x20000\n"
"v_mbcnt_lo_u32_b32 v0, -1, 0\n"
"v_mbcnt_hi_u32_b32 v0, -1, v0\n"
"s_lshl_b32 s62, %[w], 13\n"
"s_add_u32 s62, s62, 0x18000\n"
"v_lshrrev_b32 v1, 3, v0\n"
"v_bfe_u32 v2, v0, 4, 1\n"
"v_bfe_u32 v3, v0, 5, 1\n"
"v_lshlrev_b32 v2, 2, v2\n"
"v_lshl_or_b32 v2, v3, 1, v2\n"
"v_and_b32 v3, 7, v0\n"
"v_xor_b32 v2, v2, v3\n"
"v_mul_u32_u24 v1, 0x500, v1\n"
"v_lshl_add_u32 v228, v2, 4, v1\n"
"v_xor_b32 v229, 48, v228\n"
"s_mul_i32 s73, %[w], 0x1800\n"
"s_mul_i32 s59, %[w], 0x30000\n"
"s_mov_b32 s80, 0x3000\n"
"s_mov_b32 s81, 0x180\n"
"v_mul_u32_u24 v4, 0xaab, v0\n"
"v_lshrrev_b32 v4, 16, v4\n"
"v_mul_u32_u24 v2, 24, v4\n"
"v_sub_u32 v7, v0, v2\n"
"v_bfe_u32 v2, v4, 1, 1\n"
"v_lshlrev_b32 v2, 2, v2\n"
"v_xor_b32 v7, v7, v2\n"
"v_lshlrev_b32 v2, 4, v7\n"
"v_mad_u32_u24 v222, v4, s80, v2\n"
"v_add_u32 v1, 64, v0\n"
"v_mul_u32_u24 v5, 0xaab, v1\n"
"v_lshrrev_b32 v5, 16, v5\n"
"v_mul_u32_u24 v2, 24, v5\n"
"v_sub_u32 v8, v1, v2\n"
"v_bfe_u32 v2, v5, 1, 1\n"
"v_lshlrev_b32 v2, 2, v2\n"
"v_xor_b32 v8, v8, v2\n"
"v_lshlrev_b32 v2, 4, v8\n"
"v_mad_u32_u24 v223, v5, s80, v2\n"
"v_add_u32 v223, 0xfffffc00, v223\n"
"v_add_u32 v1, 0x80, v0\n"
"v_mul_u32_u24 v6, 0xaab, v1\n"
"v_lshrrev_b32 v6, 16, v6\n"
"v_mul_u32_u24 v2, 24, v6\n"
"v_sub_u32 v9, v1, v2\n"
"v_bfe_u32 v2, v6, 1, 1\n"
"v_lshlrev_b32 v2, 2, v2\n"
"v_xor_b32 v9, v9, v2\n"
"v_lshlrev_b32 v2, 4, v9\n"
"v_mad_u32_u24 v224, v6, s80, v2\n"
"v_add_u32 v224, 0xfffff800, v224\n"
"s_mov_b32 s82, 0x17400\n"
"v_xad_u32 v225, v222, 32, s82\n"
"s_mov_b32 s82, 0x18400\n"
"v_xad_u32 v226, v223, 32, s82\n"
"s_mov_b32 s82, 0x18400\n"
"v_xad_u32 v227, v224, 32, s82\n"
"s_waitcnt lgkmcnt(0)\n"
"s_add_u32 s36, s8, s28\n"
"s_addc_u32 s37, s9, 0\n"
"s_add_u32 s40, s14, s29\n"
"s_addc_u32 s41, s15, 0\n"
"s_memrealtime s[94:95]\n"
"s_add_u32 m0, s62, 0\n"
"s_mov_b32 s58, 0\n"
"buffer_load_dwordx4 v228, s[36:39], s58 offen offset:0 lds\n"
"s_lshl_b32 s28, s25, 5\n"
"s_add_u32 s29, s28, s26\n"
"s_mul_i32 s29, s29, 0x60000\n"
"s_add_u32 s10, s10, s29\n"
"s_addc_u32 s11, s11, 0\n"
"s_add_u32 s12, s12, s29\n"
"s_addc_u32 s13, s13, 0\n"
"s_lshl_b32 s29, s24, 2\n"
"s_add_u32 s28, s28, s29\n"
"s_mul_i32 s29, s28, 0x18000\n"
"s_mov_b32 s58, 0x2400\n"
"buffer_load_dwordx4 v229, s[36:39], s58 offen offset:1024 lds\n"
"s_add_u32 s29, s29, 0x100000\n"
"s_add_u32 s30, s18, s29\n"
"s_addc_u32 s31, s19, 0\n"
"s_mul_i32 s28, s28, 0x2800\n"
"s_mul_i32 s29, s23, 0x140\n"
"s_add_u32 s28, s28, s29\n"
"s_add_u32 s20, s20, s28\n"
"s_addc_u32 s21, s21, 0\n"
"s_mul_i32 s28, s24, 0xf0000\n"
"s_add_u32 s28, s28, s29\n"
"s_mov_b32 s58, 0x4800\n"
"buffer_load_dwordx4 v228, s[36:39], s58 offen offset:2048 lds\n"
"s_sub_u32 s27, 0x780000, s28\n"
"s_add_u32 s16, s16, s28\n"
"s_addc_u32 s17, s17, 0\n"
"s_lshl_b32 s28, s25, 2\n"
"s_add_u32 s28, s28, s23\n"
"s_mul_i32 s28, s28, 0xa0000\n"
"s_add_u32 s28, s28, 0x2000000\n"
"s_add_u32 s34, s18, s28\n"
"s_addc_u32 s35, s19, 0\n"
"s_lshl_b32 s28, s25, 16\n"
"s_mov_b32 s58, 0x6c00\n"
"buffer_load_dwordx4 v229, s[36:39], s58 offen offset:3072 lds\n"
"s_add_u32 s18, s18, s28\n"
"s_addc_u32 s19, s19, 0\n"
"s_mov_b64 s[44:45], s[10:11]\n"
"s_mov_b32 s46, 0x60000\n"
"s_mov_b32 s47, 0x20000\n"
"s_mov_b64 s[48:49], s[12:13]\n"
"s_mov_b32 s50, 0x60000\n"
"s_mov_b32 s51, 0x20000\n"
"s_mov_b32 s65, s73\n"
"s_mov_b32 s64, s65\n"
"s_add_u32 m0, s62, 0x1000\n"
"s_mov_b32 s58, 0xa000\n"
"buffer_load_dwordx4 v228, s[36:39], s58 offen offset:0 lds\n"
"s_add_u32 s64, s64, 0x6000\n"
"v_bfe_u32 v2, v4, 2, 1\n"
"v_mul_u32_u24 v2, 3, v2\n"
"v_xor_b32 v7, v7, v2\n"
"v_lshlrev_b32 v2, 4, v7\n"
"v_mad_u32_u24 v216, v4, s81, v2\n"
"v_bfe_u32 v2, v5, 2, 1\n"
"v_mul_u32_u24 v2, 3, v2\n"
"v_xor_b32 v8, v8, v2\n"
"v_lshlrev_b32 v2, 4, v8\n"
"s_mov_b32 s58, 0xc400\n"
"buffer_load_dwordx4 v229, s[36:39], s58 offen offset:1024 lds\n"
"v_mad_u32_u24 v217, v5, s81, v2\n"
"v_add_u32 v217, 0xfffffc00, v217\n"
"v_bfe_u32 v2, v6, 2, 1\n"
"v_mul_u32_u24 v2, 3, v2\n"
"v_xor_b32 v9, v9, v2\n"
"v_lshlrev_b32 v2, 4, v9\n"
"v_mad_u32_u24 v218, v6, s81, v2\n"
"v_add_u32 v218, 0xfffff800, v218\n"
"s_mov_b32 s82, 0\n"
"v_xad_u32 v219, v216, 16, s82\n"
"s_mov_b32 s58, 0xe800\n"
"buffer_load_dwordx4 v228, s[36:39], s58 offen offset:2048 lds\n"
"s_mov_b32 s82, 0x1000\n"
"v_xad_u32 v220, v217, 16, s82\n"
"s_mov_b32 s82, 0x1000\n"
"v_xad_u32 v221, v218, 16, s82\n"
"v_and_b32 v1, 15, v0\n"
"v_lshrrev_b32 v2, 4, v0\n"
"v_bfe_u32 v3, v0, 2, 2\n"
"v_bfe_u32 v4, v0, 1, 1\n"
"v_and_b32 v5, 1, v0\n"
"v_bfe_u32 v7, v0, 2, 1\n"
"s_mov_b32 s58, 0x10c00\n"
"buffer_load_dwordx4 v229, s[36:39], s58 offen offset:3072 lds\n"
"v_bfe_u32 v8, v0, 3, 1\n"
"v_xor_b32 v9, v7, v8\n"
"v_lshl_or_b32 v10, v9, 1, v8\n"
"v_lshl_or_b32 v10, v4, 2, v10\n"
"v_xor_b32 v10, v10, v2\n"
"v_lshlrev_b32 v10, 4, v10\n"
"v_lshl_add_u32 v10, v1, 7, v10\n"
"v_add_u32 v240, s62, v10\n"
"v_xor_b32 v241, 64, v240\n"
"v_lshl_or_b32 v10, v7, 1, v9\n"
"s_add_u32 m0, s73, 0x12000\n"
"s_nop 0\n"
"buffer_load_dwordx4 v222, s[40:43], s59 offen offset:0 lds\n"
"v_lshl_or_b32 v10, v4, 2, v10\n"
"v_bfe_u32 v11, v0, 4, 1\n"
"v_bfe_u32 v12, v0, 5, 1\n"
"v_lshl_or_b32 v13, v11, 1, v12\n"
"v_xor_b32 v10, v10, v13\n"
"v_lshlrev_b32 v10, 4, v10\n"
"v_mad_u32_u24 v230, v1, s81, v10\n"
"v_xor_b32 v231, 64, v230\n"
"v_lshrrev_b32 v14, 2, v0\n"
"v_xor_b32 v10, v11, v12\n"
"buffer_load_dwordx4 v223, s[40:43], s59 offen offset:1024 lds\n"
"v_lshl_or_b32 v10, v11, 1, v10\n"
"v_lshl_or_b32 v10, v8, 2, v10\n"
"v_xor_b32 v10, v10, v4\n"
"v_lshlrev_b32 v10, 4, v10\n"
"v_lshl_or_b32 v10, v5, 3, v10\n"
"v_mad_u32_u24 v10, v14, s81, v10\n"
"v_add_u32 v232, 0xc000, v10\n"
"v_xor_b32 v233, 32, v232\n"
"v_xor_b32 v234, 64, v232\n"
"v_xor_b32 v235, 0x60, v232\n"
"buffer_load_dwordx4 v224, s[40:43], s59 offen offset:2048 lds\n"
"v_lshl_add_u32 v14, v2, 3, v3\n"
"v_lshlrev_b32 v10, 1, v11\n"
"v_lshl_or_b32 v10, v8, 2, v10\n"
"v_xor_b32 v10, v10, v4\n"
"v_lshlrev_b32 v10, 4, v10\n"
"v_lshl_or_b32 v10, v5, 3, v10\n"
"v_mad_u32_u24 v10, v14, s81, v10\n"
"v_add_u32 v236, 0x6000, v10\n"
"v_xor_b32 v237, 32, v236\n"
"v_xor_b32 v238, 64, v236\n"
"buffer_load_dwordx4 v225, s[40:43], s59 offen offset:3072 lds\n"
"v_xor_b32 v239, 0x60, v236\n"
"s_lshl_b32 s82, %[w], 12\n"
"v_lshl_add_u32 v250, v0, 4, s82\n"
"s_lshr_b32 s71, %[bid], 6\n"
"s_lshl_b32 s71, s71, 2\n"
"s_lshr_b32 s71, 0xccccccc0, s71\n"
"s_and_b32 s71, s71, 15\n"
"s_add_u32 s71, s71, 0x5e\n"
"s_mov_b32 s72, 0x3dd53b95\n"
"v_lshrrev_b32 v1, 2, v0\n"
"s_add_u32 m0, s73, 0x13000\n"
"s_nop 0\n"
"buffer_load_dwordx4 v226, s[40:43], s59 offen offset:0 lds\n"
"v_and_b32 v2, 3, v0\n"
"v_min_u32 v2, 2, v2\n"
"v_lshlrev_b32 v2, 7, v2\n"
"v_mad_u32_u24 v242, v1, s80, v2\n"
"v_lshrrev_b32 v2, 4, v0\n"
"v_lshl_add_u32 v242, v2, 28, v242\n"
"v_mul_u32_u24 v1, 0x500, v0\n"
"v_lshrrev_b32 v2, 3, v0\n"
"v_lshl_add_u32 v243, v2, 28, v1\n"
"s_lshr_b32 s60, s22, 3\n"
"buffer_load_dwordx4 v227, s[40:43], s59 offen offset:1024 lds\n"
"s_lshl_b32 s60, s60, 2\n"
"s_add_u32 s60, s60, %[w]\n"
"s_mul_i32 s60, s60, 0xc000\n"
"s_add_u32 s60, s60, 0x180000\n"
"s_and_b32 s61, s22, 7\n"
"s_mul_i32 s61, s61, 0x2800\n"
"s_add_u32 s61, s61, 0x180\n"
"s_getpc_b64 s[52:53]\n"
"s_mov_b32 s54, 0x10b78\n"
"s_mov_b32 s55, 0x20000\n"
"s_add_u32 s59, s59, 0xc0000\n"
"s_lshl_b32 s57, s22, 2\n"
"s_add_u32 s57, s57, %[w]\n"
"s_mul_i32 s57, s57, 0x280\n"
"v_lshl_add_u32 v1, v0, 7, s57\n"
"v_mov_b32 v20, 0x7ff00000\n"
"v_cmp_gt_u32 vcc, 5, v0\n"
"s_nop 1\n"
"v_cndmask_b32 v1, v20, v1, vcc\n"
"buffer_load_dword v255, v1, s[52:55], 0 offen offset:0\n"
"s_mov_b64 s[96:97], s[18:19]\n"
"s_mov_b32 s98, 0x10000\n"
"s_mov_b32 s99, 0x20000\n"
"buffer_load_dword v245, off, s[96:99], 0 sc1\n"
"s_add_u32 m0, s62, 0x8000\n"
"s_mov_b32 s58, 0x80\n"
"buffer_load_dwordx4 v228, s[36:39], s58 offen offset:0 lds\n"
"s_mov_b32 s58, 0x2480\n"
"buffer_load_dwordx4 v229, s[36:39], s58 offen offset:1024 lds\n"
"s_mov_b32 s58, 0x4880\n"
"buffer_load_dwordx4 v228, s[36:39], s58 offen offset:2048 lds\n"
"s_mov_b32 s58, 0x6c80\n"
"buffer_load_dwordx4 v229, s[36:39], s58 offen offset:3072 lds\n"
"s_add_u32 m0, s62, 0x9000\n"
"s_mov_b32 s58, 0xa080\n"
"buffer_load_dwordx4 v228, s[36:39], s58 offen offset:0 lds\n"
"s_mov_b32 s58, 0xc480\n"
"buffer_load_dwordx4 v229, s[36:39], s58 offen offset:1024 lds\n"
"s_mov_b32 s58, 0xe880\n"
"buffer_load_dwordx4 v228, s[36:39], s58 offen offset:2048 lds\n"
"s_mov_b32 s58, 0x10c80\n"
"buffer_load_dwordx4 v229, s[36:39], s58 offen offset:3072 lds\n"
"s_add_u32 m0, s73, 0x6000\n"
"s_nop 0\n"
"buffer_load_dwordx4 v222, s[40:43], s59 offen offset:0 lds\n"
"buffer_load_dwordx4 v223, s[40:43], s59 offen offset:1024 lds\n"
"buffer_load_dwordx4 v224, s[40:43], s59 offen offset:2048 lds\n"
"buffer_load_dwordx4 v225, s[40:43], s59 offen offset:3072 lds\n"
"s_add_u32 m0, s73, 0x7000\n"
"s_nop 0\n"
"buffer_load_dwordx4 v226, s[40:43], s59 offen offset:0 lds\n"
"buffer_load_dwordx4 v227, s[40:43], s59 offen offset:1024 lds\n"
"s_add_u32 s59, s59, 0xc0000\n"
"s_mov_b64 exec, 0\n"
"s_cmp_eq_u32 %[w], 0\n"
"s_cbranch_scc1 Ltp2%=\n"
"s_cmp_eq_u32 %[w], 1\n"
"s_cbranch_scc1 Ltp4%=\n"
"s_cmp_eq_u32 %[w], 2\n"
"s_cbranch_scc1 Ltp5%=\n"
"s_cmp_eq_u32 %[w], 3\n"
"s_cbranch_scc1 Ltp6%=\n"
"Ltrp%=:\n"
"s_mov_b64 exec, -1\n"
"Ltsp%=:\n"
"s_waitcnt vmcnt(16)\n"
"s_barrier\n"
"ds_read_b128 a[0:3], v240 offset:0\n"
"ds_read_b128 a[8:11], v240 offset:2048\n"
"ds_read_b128 a[16:19], v240 offset:4096\n"
"ds_read_b128 a[24:27], v240 offset:6144\n"
"ds_read_b64_tr_b16 v[196:197], v236 offset:61440\n"
"ds_read_b64_tr_b16 v[198:199], v236 offset:62976\n"
"ds_read_b64_tr_b16 v[192:193], v236 offset:49152\n"
"ds_read_b64_tr_b16 v[194:195], v236 offset:50688\n"
"ds_read_b128 a[4:7], v241 offset:0\n"
"ds_read_b128 a[12:15], v241 offset:2048\n"
"ds_read_b128 a[20:23], v241 offset:4096\n"
"ds_read_b128 a[28:31], v241 offset:6144\n"
"ds_read_b64_tr_b16 v[200:201], v237 offset:49152\n"
"ds_read_b64_tr_b16 v[202:203], v237 offset:50688\n"
"s_waitcnt lgkmcnt(2)\n"
"ds_read_b64_tr_b16 v[204:205], v237 offset:61440\n"
"ds_read_b64_tr_b16 v[206:207], v237 offset:62976\n"
"ds_read_b64_tr_b16 v[212:213], v238 offset:61440\n"
"ds_read_b64_tr_b16 v[214:215], v238 offset:62976\n"
"ds_read_b64_tr_b16 v[208:209], v238 offset:49152\n"
"s_add_u32 m0, s62, 0\n"
"s_mov_b32 s58, 0x100\n"
"buffer_load_dwordx4 v228, s[36:39], s58 offen offset:0 lds\n"
"s_mov_b32 s58, 0x2500\n"
"buffer_load_dwordx4 v229, s[36:39], s58 offen offset:1024 lds\n"
"s_mov_b32 s58, 0x4900\n"
"buffer_load_dwordx4 v228, s[36:39], s58 offen offset:2048 lds\n"
"s_mov_b32 s58, 0x6d00\n"
"buffer_load_dwordx4 v229, s[36:39], s58 offen offset:3072 lds\n"
"s_add_u32 m0, s62, 0x1000\n"
"s_mov_b32 s58, 0xa100\n"
"buffer_load_dwordx4 v228, s[36:39], s58 offen offset:0 lds\n"
"s_mov_b32 s58, 0xc500\n"
"buffer_load_dwordx4 v229, s[36:39], s58 offen offset:1024 lds\n"
"s_mov_b32 s58, 0xe900\n"
"buffer_load_dwordx4 v228, s[36:39], s58 offen offset:2048 lds\n"
"s_mov_b32 s58, 0x10d00\n"
"buffer_load_dwordx4 v229, s[36:39], s58 offen offset:3072 lds\n"
"v_mfma_f32_16x16x32_bf16 v[0:3], v[192:195], a[0:3], 0\n"
"ds_read_b64_tr_b16 v[210:211], v238 offset:50688\n"
"v_mfma_f32_16x16x32_bf16 v[0:3], v[196:199], a[4:7], v[0:3]\n"
"s_add_u32 m0, s73, 0xc000\n"
"v_mfma_f32_16x16x32_bf16 v[8:11], v[196:199], a[12:15], 0\n"
"buffer_load_dwordx4 v222, s[40:43], s59 offen offset:0 lds\n"
"v_mfma_f32_16x16x32_bf16 v[8:11], v[192:195], a[8:11], v[8:11]\n"
"v_mfma_f32_16x16x32_bf16 v[16:19], v[192:195], a[16:19], 0\n"
"buffer_load_dwordx4 v223, s[40:43], s59 offen offset:1024 lds\n"
"v_mfma_f32_16x16x32_bf16 v[16:19], v[196:199], a[20:23], v[16:19]\n"
"v_mfma_f32_16x16x32_bf16 v[24:27], v[196:199], a[28:31], 0\n"
"ds_read_b64_tr_b16 v[196:197], v239 offset:61440\n"
"v_mfma_f32_16x16x32_bf16 v[24:27], v[192:195], a[24:27], v[24:27]\n"
"s_waitcnt lgkmcnt(5)\n"
"ds_read_b64_tr_b16 v[198:199], v239 offset:62976\n"
"ds_read_b64_tr_b16 v[192:193], v239 offset:49152\n"
"v_mfma_f32_16x16x32_bf16 v[4:7], v[200:203], a[0:3], 0\n"
"ds_read_b64_tr_b16 v[194:195], v239 offset:50688\n"
"v_mfma_f32_16x16x32_bf16 v[4:7], v[204:207], a[4:7], v[4:7]\n"
"v_mfma_f32_16x16x32_bf16 v[12:15], v[204:207], a[12:15], 0\n"
"buffer_load_dwordx4 v224, s[40:43], s59 offen offset:2048 lds\n"
"v_mfma_f32_16x16x32_bf16 v[12:15], v[200:203], a[8:11], v[12:15]\n"
"v_mfma_f32_16x16x32_bf16 v[20:23], v[200:203], a[16:19], 0\n"
"buffer_load_dwordx4 v225, s[40:43], s59 offen offset:3072 lds\n"
"v_mfma_f32_16x16x32_bf16 v[20:23], v[204:207], a[20:23], v[20:23]\n"
"v_mfma_f32_16x16x32_bf16 v[28:31], v[204:207], a[28:31], 0\n"
"ds_read_b64_tr_b16 v[204:205], v236 offset:61568\n"
"v_mfma_f32_16x16x32_bf16 v[28:31], v[200:203], a[24:27], v[28:31]\n"
"s_waitcnt lgkmcnt(5)\n"
"ds_read_b64_tr_b16 v[206:207], v236 offset:63104\n"
"ds_read_b64_tr_b16 v[200:201], v236 offset:49280\n"
"v_mfma_f32_16x16x32_bf16 v[32:35], v[208:211], a[0:3], 0\n"
"s_waitcnt vmcnt(18)\n"
"ds_read_b64_tr_b16 v[202:203], v236 offset:50816\n"
"v_mfma_f32_16x16x32_bf16 v[32:35], v[212:215], a[4:7], v[32:35]\n"
"s_add_u32 m0, s73, 0xd000\n"
"ds_read_b128 a[32:35], v240 offset:32768\n"
"v_mfma_f32_16x16x32_bf16 v[40:43], v[212:215], a[12:15], 0\n"
"buffer_load_dwordx4 v226, s[40:43], s59 offen offset:0 lds\n"
"v_mfma_f32_16x16x32_bf16 v[40:43], v[208:211], a[8:11], v[40:43]\n"
"v_mfma_f32_16x16x32_bf16 v[48:51], v[208:211], a[16:19], 0\n"
"buffer_load_dwordx4 v227, s[40:43], s59 offen offset:1024 lds\n"
"v_mfma_f32_16x16x32_bf16 v[48:51], v[212:215], a[20:23], v[48:51]\n"
"ds_read_b128 a[36:39], v241 offset:32768\n"
"v_mfma_f32_16x16x32_bf16 v[56:59], v[212:215], a[28:31], 0\n"
"s_add_u32 s59, s59, 0xc0000\n"
"ds_read_b64_tr_b16 v[212:213], v237 offset:61568\n"
"v_mfma_f32_16x16x32_bf16 v[56:59], v[208:211], a[24:27], v[56:59]\n"
"s_waitcnt lgkmcnt(7)\n"
"ds_read_b64_tr_b16 v[214:215], v237 offset:63104\n"
"ds_read_b64_tr_b16 v[208:209], v237 offset:49280\n"
"v_mfma_f32_16x16x32_bf16 v[36:39], v[192:195], a[0:3], 0\n"
"ds_read_b64_tr_b16 v[210:211], v237 offset:50816\n"
"v_mfma_f32_16x16x32_bf16 v[36:39], v[196:199], a[4:7], v[36:39]\n"
"ds_read_b128 a[40:43], v240 offset:34816\n"
"v_mfma_f32_16x16x32_bf16 v[44:47], v[196:199], a[12:15], 0\n"
"s_add_u32 s60, s60, 0xc0000\n"
"buffer_load_dword v255, v242, s[40:43], s60 offen offset:0\n"
"v_mfma_f32_16x16x32_bf16 v[44:47], v[192:195], a[8:11], v[44:47]\n"
"v_mfma_f32_16x16x32_bf16 v[52:55], v[192:195], a[16:19], 0\n"
"v_mfma_f32_16x16x32_bf16 v[52:55], v[196:199], a[20:23], v[52:55]\n"
"ds_read_b128 a[44:47], v241 offset:34816\n"
"v_mfma_f32_16x16x32_bf16 v[60:63], v[196:199], a[28:31], 0\n"
"ds_read_b64_tr_b16 v[196:197], v238 offset:61568\n"
"v_mfma_f32_16x16x32_bf16 v[60:63], v[192:195], a[24:27], v[60:63]\n"
"s_waitcnt lgkmcnt(9)\n"
"ds_read_b64_tr_b16 v[198:199], v238 offset:63104\n"
"ds_read_b64_tr_b16 v[192:193], v238 offset:49280\n"
"v_mfma_f32_16x16x32_bf16 v[64:67], v[200:203], a[0:3], 0\n"
"ds_read_b64_tr_b16 v[194:195], v238 offset:50816\n"
"v_mfma_f32_16x16x32_bf16 v[64:67], v[204:207], a[4:7], v[64:67]\n"
"s_waitcnt lgkmcnt(10)\n"
"s_add_u32 m0, s62, 0x8000\n"
"ds_read_b128 a[48:51], v240 offset:36864\n"
"v_mfma_f32_16x16x32_bf16 v[72:75], v[204:207], a[12:15], 0\n"
"s_mov_b32 s58, 0x180\n"
"buffer_load_dwordx4 v228, s[36:39], s58 offen offset:0 lds\n"
"v_mfma_f32_16x16x32_bf16 v[72:75], v[200:203], a[8:11], v[72:75]\n"
"v_mfma_f32_16x16x32_bf16 v[80:83], v[200:203], a[16:19], 0\n"
"s_mov_b32 s58, 0x2580\n"
"buffer_load_dwordx4 v229, s[36:39], s58 offen offset:1024 lds\n"
"v_mfma_f32_16x16x32_bf16 v[80:83], v[204:207], a[20:23], v[80:83]\n"
"ds_read_b128 a[52:55], v241 offset:36864\n"
"v_mfma_f32_16x16x32_bf16 v[88:91], v[204:207], a[28:31], 0\n"
"ds_read_b64_tr_b16 v[204:205], v239 offset:61568\n"
"v_mfma_f32_16x16x32_bf16 v[88:91], v[200:203], a[24:27], v[88:91]\n"
"s_waitcnt lgkmcnt(9)\n"
"ds_read_b64_tr_b16 v[206:207], v239 offset:63104\n"
"ds_read_b64_tr_b16 v[200:201], v239 offset:49280\n"
"v_mfma_f32_16x16x32_bf16 v[68:71], v[208:211], a[0:3], 0\n"
"ds_read_b64_tr_b16 v[202:203], v239 offset:50816\n"
"v_mfma_f32_16x16x32_bf16 v[68:71], v[212:215], a[4:7], v[68:71]\n"
"s_waitcnt lgkmcnt(10)\n"
"ds_read_b128 a[56:59], v240 offset:38912\n"
"v_mfma_f32_16x16x32_bf16 v[76:79], v[212:215], a[12:15], 0\n"
"s_mov_b32 s58, 0x4980\n"
"buffer_load_dwordx4 v228, s[36:39], s58 offen offset:2048 lds\n"
"v_mfma_f32_16x16x32_bf16 v[76:79], v[208:211], a[8:11], v[76:79]\n"
"v_mfma_f32_16x16x32_bf16 v[84:87], v[208:211], a[16:19], 0\n"
"s_mov_b32 s58, 0x6d80\n"
"buffer_load_dwordx4 v229, s[36:39], s58 offen offset:3072 lds\n"
"v_mfma_f32_16x16x32_bf16 v[84:87], v[212:215], a[20:23], v[84:87]\n"
"ds_read_b128 a[60:63], v241 offset:38912\n"
"v_mfma_f32_16x16x32_bf16 v[92:95], v[212:215], a[28:31], 0\n"
"ds_read_b64_tr_b16 v[212:213], v236 offset:61696\n"
"v_mfma_f32_16x16x32_bf16 v[92:95], v[208:211], a[24:27], v[92:95]\n"
"s_waitcnt lgkmcnt(9)\n"
"ds_read_b64_tr_b16 v[214:215], v236 offset:63232\n"
"ds_read_b64_tr_b16 v[208:209], v236 offset:49408\n"
"v_mfma_f32_16x16x32_bf16 v[96:99], v[192:195], a[0:3], 0\n"
"ds_read_b64_tr_b16 v[210:211], v236 offset:50944\n"
"v_mfma_f32_16x16x32_bf16 v[96:99], v[196:199], a[4:7], v[96:99]\n"
"s_waitcnt lgkmcnt(10)\n"
"s_add_u32 m0, s62, 0x9000\n"
"v_mfma_f32_16x16x32_bf16 v[104:107], v[196:199], a[12:15], 0\n"
"s_mov_b32 s58, 0xa180\n"
"buffer_load_dwordx4 v228, s[36:39], s58 offen offset:0 lds\n"
"v_mfma_f32_16x16x32_bf16 v[104:107], v[192:195], a[8:11], v[104:107]\n"
"v_mfma_f32_16x16x32_bf16 v[112:115], v[192:195], a[16:19], 0\n"
"s_mov_b32 s58, 0xc580\n"
"buffer_load_dwordx4 v229, s[36:39], s58 offen offset:1024 lds\n"
"v_mfma_f32_16x16x32_bf16 v[112:115], v[196:199], a[20:23], v[112:115]\n"
"v_mfma_f32_16x16x32_bf16 v[120:123], v[196:199], a[28:31], 0\n"
"ds_read_b64_tr_b16 v[196:197], v237 offset:61696\n"
"v_mfma_f32_16x16x32_bf16 v[120:123], v[192:195], a[24:27], v[120:123]\n"
"s_waitcnt lgkmcnt(7)\n"
"ds_read_b64_tr_b16 v[198:199], v237 offset:63232\n"
"ds_read_b64_tr_b16 v[192:193], v237 offset:49408\n"
"v_mfma_f32_16x16x32_bf16 v[100:103], v[200:203], a[0:3], 0\n"
"ds_read_b64_tr_b16 v[194:195], v237 offset:50944\n"
"v_mfma_f32_16x16x32_bf16 v[100:103], v[204:207], a[4:7], v[100:103]\n"
"s_waitcnt lgkmcnt(8)\n"
"v_mfma_f32_16x16x32_bf16 v[108:111], v[204:207], a[12:15], 0\n"
"s_mov_b32 s58, 0xe980\n"
"buffer_load_dwordx4 v228, s[36:39], s58 offen offset:2048 lds\n"
"v_mfma_f32_16x16x32_bf16 v[108:111], v[200:203], a[8:11], v[108:111]\n"
"v_mfma_f32_16x16x32_bf16 v[116:119], v[200:203], a[16:19], 0\n"
"s_mov_b32 s58, 0x10d80\n"
"buffer_load_dwordx4 v229, s[36:39], s58 offen offset:3072 lds\n"
"v_mfma_f32_16x16x32_bf16 v[116:119], v[204:207], a[20:23], v[116:119]\n"
"v_mfma_f32_16x16x32_bf16 v[124:127], v[204:207], a[28:31], 0\n"
"ds_read_b64_tr_b16 v[204:205], v238 offset:61696\n"
"v_mfma_f32_16x16x32_bf16 v[124:127], v[200:203], a[24:27], v[124:127]\n"
"s_waitcnt lgkmcnt(5)\n"
"ds_read_b64_tr_b16 v[206:207], v238 offset:63232\n"
"ds_read_b64_tr_b16 v[200:201], v238 offset:49408\n"
"v_mfma_f32_16x16x32_bf16 v[128:131], v[208:211], a[0:3], 0\n"
"s_mov_b64 s[52:53], s[16:17]\n"
"ds_read_b64_tr_b16 v[202:203], v238 offset:50944\n"
"v_mfma_f32_16x16x32_bf16 v[128:131], v[212:215], a[4:7], v[128:131]\n"
"s_mul_i32 s66, s23, 0x140\n"
"v_mfma_f32_16x16x32_bf16 v[136:139], v[212:215], a[12:15], 0\n"
"s_sub_u32 s52, s52, s66\n"
"v_mfma_f32_16x16x32_bf16 v[136:139], v[208:211], a[8:11], v[136:139]\n"
"v_mfma_f32_16x16x32_bf16 v[144:147], v[208:211], a[16:19], 0\n"
"s_subb_u32 s53, s53, 0\n"
"v_mfma_f32_16x16x32_bf16 v[144:147], v[212:215], a[20:23], v[144:147]\n"
"s_mov_b32 s54, 0xf0000\n"
"v_mfma_f32_16x16x32_bf16 v[152:155], v[212:215], a[28:31], 0\n"
"s_lshr_b32 s70, s22, 3\n"
"ds_read_b64_tr_b16 v[212:213], v239 offset:61696\n"
"v_mfma_f32_16x16x32_bf16 v[152:155], v[208:211], a[24:27], v[152:155]\n"
"s_waitcnt lgkmcnt(5)\n"
"ds_read_b64_tr_b16 v[214:215], v239 offset:63232\n"
"ds_read_b64_tr_b16 v[208:209], v239 offset:49408\n"
"v_mfma_f32_16x16x32_bf16 v[132:135], v[192:195], a[0:3], 0\n"
"s_lshl_b32 s70, s70, 2\n"
"ds_read_b64_tr_b16 v[210:211], v239 offset:50944\n"
"v_mfma_f32_16x16x32_bf16 v[132:135], v[196:199], a[4:7], v[132:135]\n"
"s_add_u32 s70, s70, s23\n"
"v_mfma_f32_16x16x32_bf16 v[140:143], v[196:199], a[12:15], 0\n"
"s_lshl_b32 s70, s70, 2\n"
"v_mfma_f32_16x16x32_bf16 v[140:143], v[192:195], a[8:11], v[140:143]\n"
"v_mfma_f32_16x16x32_bf16 v[148:151], v[192:195], a[16:19], 0\n"
"s_add_u32 s70, s70, %[w]\n"
"v_mfma_f32_16x16x32_bf16 v[148:151], v[196:199], a[20:23], v[148:151]\n"
"s_mul_i32 s70, s70, 0x3c00\n"
"v_mfma_f32_16x16x32_bf16 v[156:159], v[196:199], a[28:31], 0\n"
"s_waitcnt vmcnt(23) lgkmcnt(0)\n"
"s_barrier\n"
"s_memrealtime s[6:7]\n"
"ds_read_b64_tr_b16 v[196:197], v236 offset:12288\n"
"v_mfma_f32_16x16x32_bf16 v[156:159], v[192:195], a[24:27], v[156:159]\n"
"ds_read_b64_tr_b16 v[198:199], v236 offset:13824\n"
"ds_read_b64_tr_b16 v[192:193], v236 offset:0\n"
"v_mfma_f32_16x16x32_bf16 v[160:163], v[200:203], a[0:3], 0\n"
"ds_read_b64_tr_b16 v[194:195], v236 offset:1536\n"
"v_mfma_f32_16x16x32_bf16 v[160:163], v[204:207], a[4:7], v[160:163]\n"
"v_mfma_f32_16x16x32_bf16 v[168:171], v[204:207], a[12:15], 0\n"
"v_mfma_f32_16x16x32_bf16 v[168:171], v[200:203], a[8:11], v[168:171]\n"
"v_mfma_f32_16x16x32_bf16 v[176:179], v[200:203], a[16:19], 0\n"
"v_mfma_f32_16x16x32_bf16 v[176:179], v[204:207], a[20:23], v[176:179]\n"
"v_mfma_f32_16x16x32_bf16 v[184:187], v[204:207], a[28:31], 0\n"
"ds_read_b64_tr_b16 v[204:205], v237 offset:12288\n"
"v_mfma_f32_16x16x32_bf16 v[184:187], v[200:203], a[24:27], v[184:187]\n"
"ds_read_b64_tr_b16 v[206:207], v237 offset:13824\n"
"ds_read_b64_tr_b16 v[200:201], v237 offset:0\n"
"v_mfma_f32_16x16x32_bf16 v[164:167], v[208:211], a[0:3], 0\n"
"ds_read_b64_tr_b16 v[202:203], v237 offset:1536\n"
"v_mfma_f32_16x16x32_bf16 v[164:167], v[212:215], a[4:7], v[164:167]\n"
"v_mfma_f32_16x16x32_bf16 v[172:175], v[212:215], a[12:15], 0\n"
"v_mfma_f32_16x16x32_bf16 v[172:175], v[208:211], a[8:11], v[172:175]\n"
"v_mfma_f32_16x16x32_bf16 v[180:183], v[208:211], a[16:19], 0\n"
"v_mfma_f32_16x16x32_bf16 v[180:183], v[212:215], a[20:23], v[180:183]\n"
"v_mfma_f32_16x16x32_bf16 v[188:191], v[212:215], a[28:31], 0\n"
"ds_read_b64_tr_b16 v[212:213], v238 offset:12288\n"
"v_mfma_f32_16x16x32_bf16 v[188:191], v[208:211], a[24:27], v[188:191]\n"
"s_waitcnt lgkmcnt(5)\n"
"ds_read_b64_tr_b16 v[214:215], v238 offset:13824\n"
"ds_read_b64_tr_b16 v[208:209], v238 offset:0\n"
"v_mfma_f32_16x16x32_bf16 v[0:3], v[192:195], a[32:35], v[0:3]\n"
"ds_read_b64_tr_b16 v[210:211], v238 offset:1536\n"
"v_mfma_f32_16x16x32_bf16 v[0:3], v[196:199], a[36:39], v[0:3]\n"
"s_add_u32 m0, s73, 0x12000\n"
"v_mfma_f32_16x16x32_bf16 v[8:11], v[196:199], a[44:47], v[8:11]\n"
"buffer_load_dwordx4 v222, s[40:43], s59 offen offset:0 lds\n"
"v_mfma_f32_16x16x32_bf16 v[8:11], v[192:195], a[40:43], v[8:11]\n"
"v_mfma_f32_16x16x32_bf16 v[16:19], v[192:195], a[48:51], v[16:19]\n"
"buffer_load_dwordx4 v223, s[40:43], s59 offen offset:1024 lds\n"
"v_mfma_f32_16x16x32_bf16 v[16:19], v[196:199], a[52:55], v[16:19]\n"
"v_mfma_f32_16x16x32_bf16 v[24:27], v[196:199], a[60:63], v[24:27]\n"
"ds_read_b64_tr_b16 v[196:197], v239 offset:12288\n"
"v_mfma_f32_16x16x32_bf16 v[24:27], v[192:195], a[56:59], v[24:27]\n"
"s_waitcnt lgkmcnt(5)\n"
"ds_read_b64_tr_b16 v[198:199], v239 offset:13824\n"
"ds_read_b64_tr_b16 v[192:193], v239 offset:0\n"
"v_mfma_f32_16x16x32_bf16 v[4:7], v[200:203], a[32:35], v[4:7]\n"
"ds_read_b64_tr_b16 v[194:195], v239 offset:1536\n"
"v_mfma_f32_16x16x32_bf16 v[4:7], v[204:207], a[36:39], v[4:7]\n"
"v_mfma_f32_16x16x32_bf16 v[12:15], v[204:207], a[44:47], v[12:15]\n"
"buffer_load_dwordx4 v224, s[40:43], s59 offen offset:2048 lds\n"
"v_mfma_f32_16x16x32_bf16 v[12:15], v[200:203], a[40:43], v[12:15]\n"
"v_mfma_f32_16x16x32_bf16 v[20:23], v[200:203], a[48:51], v[20:23]\n"
"buffer_load_dwordx4 v225, s[40:43], s59 offen offset:3072 lds\n"
"v_mfma_f32_16x16x32_bf16 v[20:23], v[204:207], a[52:55], v[20:23]\n"
"v_mfma_f32_16x16x32_bf16 v[28:31], v[204:207], a[60:63], v[28:31]\n"
"ds_read_b64_tr_b16 v[204:205], v236 offset:12416\n"
"v_mfma_f32_16x16x32_bf16 v[28:31], v[200:203], a[56:59], v[28:31]\n"
"s_waitcnt lgkmcnt(5)\n"
"ds_read_b64_tr_b16 v[206:207], v236 offset:13952\n"
"ds_read_b64_tr_b16 v[200:201], v236 offset:128\n"
"v_mfma_f32_16x16x32_bf16 v[32:35], v[208:211], a[32:35], v[32:35]\n"
"s_waitcnt vmcnt(19)\n"
"ds_read_b64_tr_b16 v[202:203], v236 offset:1664\n"
"v_mfma_f32_16x16x32_bf16 v[32:35], v[212:215], a[36:39], v[32:35]\n"
"s_add_u32 m0, s73, 0x13000\n"
"ds_read_b128 a[0:3], v240 offset:0\n"
"v_mfma_f32_16x16x32_bf16 v[40:43], v[212:215], a[44:47], v[40:43]\n"
"buffer_load_dwordx4 v226, s[40:43], s59 offen offset:0 lds\n"
"v_mfma_f32_16x16x32_bf16 v[40:43], v[208:211], a[40:43], v[40:43]\n"
"v_mfma_f32_16x16x32_bf16 v[48:51], v[208:211], a[48:51], v[48:51]\n"
"Ltp1%=:\n"
"s_cbranch_execz Ltrp%=\n"
"buffer_load_dwordx4 v227, s[40:43], s59 offen offset:1024 lds\n"
"v_mfma_f32_16x16x32_bf16 v[48:51], v[212:215], a[52:55], v[48:51]\n"
"ds_read_b128 a[4:7], v241 offset:0\n"
"v_mfma_f32_16x16x32_bf16 v[56:59], v[212:215], a[60:63], v[56:59]\n"
"s_add_u32 s59, s59, 0xc0000\n"
"ds_read_b64_tr_b16 v[212:213], v237 offset:12416\n"
"v_mfma_f32_16x16x32_bf16 v[56:59], v[208:211], a[56:59], v[56:59]\n"
"s_waitcnt lgkmcnt(7)\n"
"ds_read_b64_tr_b16 v[214:215], v237 offset:13952\n"
"ds_read_b64_tr_b16 v[208:209], v237 offset:128\n"
"v_mfma_f32_16x16x32_bf16 v[36:39], v[192:195], a[32:35], v[36:39]\n"
"ds_read_b64_tr_b16 v[210:211], v237 offset:1664\n"
"v_mfma_f32_16x16x32_bf16 v[36:39], v[196:199], a[36:39], v[36:39]\n"
"ds_read_b128 a[8:11], v240 offset:2048\n"
"v_mfma_f32_16x16x32_bf16 v[44:47], v[196:199], a[44:47], v[44:47]\n"
"s_add_u32 s60, s60, 0xc0000\n"
"buffer_load_dword v255, v242, s[40:43], s60 offen offset:0\n"
"v_mfma_f32_16x16x32_bf16 v[44:47], v[192:195], a[40:43], v[44:47]\n"
"v_mfma_f32_16x16x32_bf16 v[52:55], v[192:195], a[48:51], v[52:55]\n"
"v_mfma_f32_16x16x32_bf16 v[52:55], v[196:199], a[52:55], v[52:55]\n"
"ds_read_b128 a[12:15], v241 offset:2048\n"
"v_mfma_f32_16x16x32_bf16 v[60:63], v[196:199], a[60:63], v[60:63]\n"
"ds_read_b64_tr_b16 v[196:197], v238 offset:12416\n"
"v_mfma_f32_16x16x32_bf16 v[60:63], v[192:195], a[56:59], v[60:63]\n"
"s_waitcnt lgkmcnt(9)\n"
"ds_read_b64_tr_b16 v[198:199], v238 offset:13952\n"
"ds_read_b64_tr_b16 v[192:193], v238 offset:128\n"
"v_mfma_f32_16x16x32_bf16 v[64:67], v[200:203], a[32:35], v[64:67]\n"
"ds_read_b64_tr_b16 v[194:195], v238 offset:1664\n"
"v_mfma_f32_16x16x32_bf16 v[64:67], v[204:207], a[36:39], v[64:67]\n"
"s_waitcnt lgkmcnt(10)\n"
"s_add_u32 m0, s62, 0\n"
"ds_read_b128 a[16:19], v240 offset:4096\n"
"v_mfma_f32_16x16x32_bf16 v[72:75], v[204:207], a[44:47], v[72:75]\n"
"s_mov_b32 s58, 0x200\n"
"buffer_load_dwordx4 v228, s[36:39], s58 offen offset:0 lds\n"
"v_mfma_f32_16x16x32_bf16 v[72:75], v[200:203], a[40:43], v[72:75]\n"
"v_mfma_f32_16x16x32_bf16 v[80:83], v[200:203], a[48:51], v[80:83]\n"
"s_mov_b32 s58, 0x2600\n"
"buffer_load_dwordx4 v229, s[36:39], s58 offen offset:1024 lds\n"
"v_mfma_f32_16x16x32_bf16 v[80:83], v[204:207], a[52:55], v[80:83]\n"
"ds_read_b128 a[20:23], v241 offset:4096\n"
"v_mfma_f32_16x16x32_bf16 v[88:91], v[204:207], a[60:63], v[88:91]\n"
"ds_read_b64_tr_b16 v[204:205], v239 offset:12416\n"
"v_mfma_f32_16x16x32_bf16 v[88:91], v[200:203], a[56:59], v[88:91]\n"
"s_waitcnt lgkmcnt(9)\n"
"ds_read_b64_tr_b16 v[206:207], v239 offset:13952\n"
"ds_read_b64_tr_b16 v[200:201], v239 offset:128\n"
"v_mfma_f32_16x16x32_bf16 v[68:71], v[208:211], a[32:35], v[68:71]\n"
"ds_read_b64_tr_b16 v[202:203], v239 offset:1664\n"
"v_mfma_f32_16x16x32_bf16 v[68:71], v[212:215], a[36:39], v[68:71]\n"
"s_waitcnt lgkmcnt(10)\n"
"ds_read_b128 a[24:27], v240 offset:6144\n"
"v_mfma_f32_16x16x32_bf16 v[76:79], v[212:215], a[44:47], v[76:79]\n"
"s_mov_b32 s58, 0x4a00\n"
"buffer_load_dwordx4 v228, s[36:39], s58 offen offset:2048 lds\n"
"v_mfma_f32_16x16x32_bf16 v[76:79], v[208:211], a[40:43], v[76:79]\n"
"v_mfma_f32_16x16x32_bf16 v[84:87], v[208:211], a[48:51], v[84:87]\n"
"s_mov_b32 s58, 0x6e00\n"
"buffer_load_dwordx4 v229, s[36:39], s58 offen offset:3072 lds\n"
"v_mfma_f32_16x16x32_bf16 v[84:87], v[212:215], a[52:55], v[84:87]\n"
"ds_read_b128 a[28:31], v241 offset:6144\n"
"v_mfma_f32_16x16x32_bf16 v[92:95], v[212:215], a[60:63], v[92:95]\n"
"ds_read_b64_tr_b16 v[212:213], v236 offset:12544\n"
"v_mfma_f32_16x16x32_bf16 v[92:95], v[208:211], a[56:59], v[92:95]\n"
"s_waitcnt lgkmcnt(9)\n"
"ds_read_b64_tr_b16 v[214:215], v236 offset:14080\n"
"ds_read_b64_tr_b16 v[208:209], v236 offset:256\n"
"v_mfma_f32_16x16x32_bf16 v[96:99], v[192:195], a[32:35], v[96:99]\n"
"ds_read_b64_tr_b16 v[210:211], v236 offset:1792\n"
"v_mfma_f32_16x16x32_bf16 v[96:99], v[196:199], a[36:39], v[96:99]\n"
"s_waitcnt lgkmcnt(10)\n"
"s_add_u32 m0, s62, 0x1000\n"
"v_mfma_f32_16x16x32_bf16 v[104:107], v[196:199], a[44:47], v[104:107]\n"
"s_mov_b32 s58, 0xa200\n"
"buffer_load_dwordx4 v228, s[36:39], s58 offen offset:0 lds\n"
"v_mfma_f32_16x16x32_bf16 v[104:107], v[192:195], a[40:43], v[104:107]\n"
"v_mfma_f32_16x16x32_bf16 v[112:115], v[192:195], a[48:51], v[112:115]\n"
"s_mov_b32 s58, 0xc600\n"
"buffer_load_dwordx4 v229, s[36:39], s58 offen offset:1024 lds\n"
"v_mfma_f32_16x16x32_bf16 v[112:115], v[196:199], a[52:55], v[112:115]\n"
"v_mfma_f32_16x16x32_bf16 v[120:123], v[196:199], a[60:63], v[120:123]\n"
"ds_read_b64_tr_b16 v[196:197], v237 offset:12544\n"
"v_mfma_f32_16x16x32_bf16 v[120:123], v[192:195], a[56:59], v[120:123]\n"
"s_waitcnt lgkmcnt(7)\n"
"ds_read_b64_tr_b16 v[198:199], v237 offset:14080\n"
"ds_read_b64_tr_b16 v[192:193], v237 offset:256\n"
"v_mfma_f32_16x16x32_bf16 v[100:103], v[200:203], a[32:35], v[100:103]\n"
"ds_read_b64_tr_b16 v[194:195], v237 offset:1792\n"
"v_mfma_f32_16x16x32_bf16 v[100:103], v[204:207], a[36:39], v[100:103]\n"
"s_waitcnt lgkmcnt(8)\n"
"v_mfma_f32_16x16x32_bf16 v[108:111], v[204:207], a[44:47], v[108:111]\n"
"s_mov_b32 s58, 0xea00\n"
"buffer_load_dwordx4 v228, s[36:39], s58 offen offset:2048 lds\n"
"v_mfma_f32_16x16x32_bf16 v[108:111], v[200:203], a[40:43], v[108:111]\n"
"v_mfma_f32_16x16x32_bf16 v[116:119], v[200:203], a[48:51], v[116:119]\n"
"s_mov_b32 s58, 0x10e00\n"
"buffer_load_dwordx4 v229, s[36:39], s58 offen offset:3072 lds\n"
"v_mfma_f32_16x16x32_bf16 v[116:119], v[204:207], a[52:55], v[116:119]\n"
"v_mfma_f32_16x16x32_bf16 v[124:127], v[204:207], a[60:63], v[124:127]\n"
"ds_read_b64_tr_b16 v[204:205], v238 offset:12544\n"
"v_mfma_f32_16x16x32_bf16 v[124:127], v[200:203], a[56:59], v[124:127]\n"
"s_waitcnt lgkmcnt(5)\n"
"ds_read_b64_tr_b16 v[206:207], v238 offset:14080\n"
"ds_read_b64_tr_b16 v[200:201], v238 offset:256\n"
"v_mfma_f32_16x16x32_bf16 v[128:131], v[208:211], a[32:35], v[128:131]\n"
"ds_read_b64_tr_b16 v[202:203], v238 offset:1792\n"
"v_mfma_f32_16x16x32_bf16 v[128:131], v[212:215], a[36:39], v[128:131]\n"
"v_mfma_f32_16x16x32_bf16 v[136:139], v[212:215], a[44:47], v[136:139]\n"
"v_mfma_f32_16x16x32_bf16 v[136:139], v[208:211], a[40:43], v[136:139]\n"
"v_mfma_f32_16x16x32_bf16 v[144:147], v[208:211], a[48:51], v[144:147]\n"
"v_mfma_f32_16x16x32_bf16 v[144:147], v[212:215], a[52:55], v[144:147]\n"
"v_mfma_f32_16x16x32_bf16 v[152:155], v[212:215], a[60:63], v[152:155]\n"
"ds_read_b64_tr_b16 v[212:213], v239 offset:12544\n"
"v_mfma_f32_16x16x32_bf16 v[152:155], v[208:211], a[56:59], v[152:155]\n"
"s_waitcnt lgkmcnt(5)\n"
"ds_read_b64_tr_b16 v[214:215], v239 offset:14080\n"
"ds_read_b64_tr_b16 v[208:209], v239 offset:256\n"
"v_mfma_f32_16x16x32_bf16 v[132:135], v[192:195], a[32:35], v[132:135]\n"
"ds_read_b64_tr_b16 v[210:211], v239 offset:1792\n"
"v_mfma_f32_16x16x32_bf16 v[132:135], v[196:199], a[36:39], v[132:135]\n"
"v_mfma_f32_16x16x32_bf16 v[140:143], v[196:199], a[44:47], v[140:143]\n"
"v_mfma_f32_16x16x32_bf16 v[140:143], v[192:195], a[40:43], v[140:143]\n"
"v_mfma_f32_16x16x32_bf16 v[148:151], v[192:195], a[48:51], v[148:151]\n"
"v_mfma_f32_16x16x32_bf16 v[148:151], v[196:199], a[52:55], v[148:151]\n"
"v_mfma_f32_16x16x32_bf16 v[156:159], v[196:199], a[60:63], v[156:159]\n"
"s_waitcnt vmcnt(24) lgkmcnt(0)\n"
"s_barrier\n"
"ds_read_b64_tr_b16 v[196:197], v236 offset:36864\n"
"v_mfma_f32_16x16x32_bf16 v[156:159], v[192:195], a[56:59], v[156:159]\n"
"ds_read_b64_tr_b16 v[198:199], v236 offset:38400\n"
"ds_read_b64_tr_b16 v[192:193], v236 offset:24576\n"
"v_mfma_f32_16x16x32_bf16 v[160:163], v[200:203], a[32:35], v[160:163]\n"
"ds_read_b64_tr_b16 v[194:195], v236 offset:26112\n"
"v_mfma_f32_16x16x32_bf16 v[160:163], v[204:207], a[36:39], v[160:163]\n"
"v_mfma_f32_16x16x32_bf16 v[168:171], v[204:207], a[44:47], v[168:171]\n"
"v_mfma_f32_16x16x32_bf16 v[168:171], v[200:203], a[40:43], v[168:171]\n"
"v_mfma_f32_16x16x32_bf16 v[176:179], v[200:203], a[48:51], v[176:179]\n"
"v_mfma_f32_16x16x32_bf16 v[176:179], v[204:207], a[52:55], v[176:179]\n"
"v_mfma_f32_16x16x32_bf16 v[184:187], v[204:207], a[60:63], v[184:187]\n"
"ds_read_b64_tr_b16 v[204:205], v237 offset:36864\n"
"v_mfma_f32_16x16x32_bf16 v[184:187], v[200:203], a[56:59], v[184:187]\n"
"ds_read_b64_tr_b16 v[206:207], v237 offset:38400\n"
"ds_read_b64_tr_b16 v[200:201], v237 offset:24576\n"
"v_mfma_f32_16x16x32_bf16 v[164:167], v[208:211], a[32:35], v[164:167]\n"
"ds_read_b64_tr_b16 v[202:203], v237 offset:26112\n"
"v_mfma_f32_16x16x32_bf16 v[164:167], v[212:215], a[36:39], v[164:167]\n"
"v_mfma_f32_16x16x32_bf16 v[172:175], v[212:215], a[44:47], v[172:175]\n"
"v_mfma_f32_16x16x32_bf16 v[172:175], v[208:211], a[40:43], v[172:175]\n"
"v_mfma_f32_16x16x32_bf16 v[180:183], v[208:211], a[48:51], v[180:183]\n"
"v_mfma_f32_16x16x32_bf16 v[180:183], v[212:215], a[52:55], v[180:183]\n"
"v_mfma_f32_16x16x32_bf16 v[188:191], v[212:215], a[60:63], v[188:191]\n"
"ds_read_b64_tr_b16 v[212:213], v238 offset:36864\n"
"v_mfma_f32_16x16x32_bf16 v[188:191], v[208:211], a[56:59], v[188:191]\n"
"s_waitcnt lgkmcnt(5)\n"
"ds_read_b64_tr_b16 v[214:215], v238 offset:38400\n"
"ds_read_b64_tr_b16 v[208:209], v238 offset:24576\n"
"v_mfma_f32_16x16x32_bf16 v[0:3], v[192:195], a[0:3], v[0:3]\n"
"ds_read_b64_tr_b16 v[210:211], v238 offset:26112\n"
"v_mfma_f32_16x16x32_bf16 v[0:3], v[196:199], a[4:7], v[0:3]\n"
"s_add_u32 m0, s73, 0x6000\n"
"v_mfma_f32_16x16x32_bf16 v[8:11], v[196:199], a[12:15], v[8:11]\n"
"buffer_load_dwordx4 v222, s[40:43], s59 offen offset:0 lds\n"
"v_mfma_f32_16x16x32_bf16 v[8:11], v[192:195], a[8:11], v[8:11]\n"
"v_mfma_f32_16x16x32_bf16 v[16:19], v[192:195], a[16:19], v[16:19]\n"
"buffer_load_dwordx4 v223, s[40:43], s59 offen offset:1024 lds\n"
"v_mfma_f32_16x16x32_bf16 v[16:19], v[196:199], a[20:23], v[16:19]\n"
"v_mfma_f32_16x16x32_bf16 v[24:27], v[196:199], a[28:31], v[24:27]\n"
"ds_read_b64_tr_b16 v[196:197], v239 offset:36864\n"
"v_mfma_f32_16x16x32_bf16 v[24:27], v[192:195], a[24:27], v[24:27]\n"
"s_waitcnt lgkmcnt(5)\n"
"ds_read_b64_tr_b16 v[198:199], v239 offset:38400\n"
"ds_read_b64_tr_b16 v[192:193], v239 offset:24576\n"
"v_mfma_f32_16x16x32_bf16 v[4:7], v[200:203], a[0:3], v[4:7]\n"
"ds_read_b64_tr_b16 v[194:195], v239 offset:26112\n"
"v_mfma_f32_16x16x32_bf16 v[4:7], v[204:207], a[4:7], v[4:7]\n"
"v_mfma_f32_16x16x32_bf16 v[12:15], v[204:207], a[12:15], v[12:15]\n"
"buffer_load_dwordx4 v224, s[40:43], s59 offen offset:2048 lds\n"
"v_mfma_f32_16x16x32_bf16 v[12:15], v[200:203], a[8:11], v[12:15]\n"
"v_mfma_f32_16x16x32_bf16 v[20:23], v[200:203], a[16:19], v[20:23]\n"
"buffer_load_dwordx4 v225, s[40:43], s59 offen offset:3072 lds\n"
"v_mfma_f32_16x16x32_bf16 v[20:23], v[204:207], a[20:23], v[20:23]\n"
"v_mfma_f32_16x16x32_bf16 v[28:31], v[204:207], a[28:31], v[28:31]\n"
"ds_read_b64_tr_b16 v[204:205], v236 offset:36992\n"
"v_mfma_f32_16x16x32_bf16 v[28:31], v[200:203], a[24:27], v[28:31]\n"
"s_waitcnt lgkmcnt(5)\n"
"ds_read_b64_tr_b16 v[206:207], v236 offset:38528\n"
"ds_read_b64_tr_b16 v[200:201], v236 offset:24704\n"
"v_mfma_f32_16x16x32_bf16 v[32:35], v[208:211], a[0:3], v[32:35]\n"
"s_waitcnt vmcnt(19)\n"
"ds_read_b64_tr_b16 v[202:203], v236 offset:26240\n"
"v_mfma_f32_16x16x32_bf16 v[32:35], v[212:215], a[4:7], v[32:35]\n"
"s_add_u32 m0, s73, 0x7000\n"
"ds_read_b128 a[32:35], v240 offset:32768\n"
"v_mfma_f32_16x16x32_bf16 v[40:43], v[212:215], a[12:15], v[40:43]\n"
"buffer_load_dwordx4 v226, s[40:43], s59 offen offset:0 lds\n"
"v_mfma_f32_16x16x32_bf16 v[40:43], v[208:211], a[8:11], v[40:43]\n"
"v_mfma_f32_16x16x32_bf16 v[48:51], v[208:211], a[16:19], v[48:51]\n"
"buffer_load_dwordx4 v227, s[40:43], s59 offen offset:1024 lds\n"
"v_mfma_f32_16x16x32_bf16 v[48:51], v[212:215], a[20:23], v[48:51]\n"
"ds_read_b128 a[36:39], v241 offset:32768\n"
"v_mfma_f32_16x16x32_bf16 v[56:59], v[212:215], a[28:31], v[56:59]\n"
"s_add_u32 s59, s59, 0xc0000\n"
"ds_read_b64_tr_b16 v[212:213], v237 offset:36992\n"
"v_mfma_f32_16x16x32_bf16 v[56:59], v[208:211], a[24:27], v[56:59]\n"
"s_waitcnt lgkmcnt(7)\n"
"ds_read_b64_tr_b16 v[214:215], v237 offset:38528\n"
"ds_read_b64_tr_b16 v[208:209], v237 offset:24704\n"
"v_mfma_f32_16x16x32_bf16 v[36:39], v[192:195], a[0:3], v[36:39]\n"
"ds_read_b64_tr_b16 v[210:211], v237 offset:26240\n"
"v_mfma_f32_16x16x32_bf16 v[36:39], v[196:199], a[4:7], v[36:39]\n"
"ds_read_b128 a[40:43], v240 offset:34816\n"
"v_mfma_f32_16x16x32_bf16 v[44:47], v[196:199], a[12:15], v[44:47]\n"
"s_add_u32 s60, s60, 0xc0000\n"
"buffer_load_dword v255, v242, s[40:43], s60 offen offset:0\n"
"v_mfma_f32_16x16x32_bf16 v[44:47], v[192:195], a[8:11], v[44:47]\n"
"v_mfma_f32_16x16x32_bf16 v[52:55], v[192:195], a[16:19], v[52:55]\n"
"v_mfma_f32_16x16x32_bf16 v[52:55], v[196:199], a[20:23], v[52:55]\n"
"ds_read_b128 a[44:47], v241 offset:34816\n"
"v_mfma_f32_16x16x32_bf16 v[60:63], v[196:199], a[28:31], v[60:63]\n"
"ds_read_b64_tr_b16 v[196:197], v238 offset:36992\n"
"v_mfma_f32_16x16x32_bf16 v[60:63], v[192:195], a[24:27], v[60:63]\n"
"s_waitcnt lgkmcnt(9)\n"
"ds_read_b64_tr_b16 v[198:199], v238 offset:38528\n"
"ds_read_b64_tr_b16 v[192:193], v238 offset:24704\n"
"v_mfma_f32_16x16x32_bf16 v[64:67], v[200:203], a[0:3], v[64:67]\n"
"ds_read_b64_tr_b16 v[194:195], v238 offset:26240\n"
"v_mfma_f32_16x16x32_bf16 v[64:67], v[204:207], a[4:7], v[64:67]\n"
"s_waitcnt lgkmcnt(10)\n"
"s_add_u32 m0, s62, 0x8000\n"
"ds_read_b128 a[48:51], v240 offset:36864\n"
"v_mfma_f32_16x16x32_bf16 v[72:75], v[204:207], a[12:15], v[72:75]\n"
"s_mov_b32 s58, 0x280\n"
"buffer_load_dwordx4 v228, s[36:39], s58 offen offset:0 lds\n"
"v_mfma_f32_16x16x32_bf16 v[72:75], v[200:203], a[8:11], v[72:75]\n"
"v_mfma_f32_16x16x32_bf16 v[80:83], v[200:203], a[16:19], v[80:83]\n"
"s_mov_b32 s58, 0x2680\n"
"buffer_load_dwordx4 v229, s[36:39], s58 offen offset:1024 lds\n"
"v_mfma_f32_16x16x32_bf16 v[80:83], v[204:207], a[20:23], v[80:83]\n"
"ds_read_b128 a[52:55], v241 offset:36864\n"
"v_mfma_f32_16x16x32_bf16 v[88:91], v[204:207], a[28:31], v[88:91]\n"
"ds_read_b64_tr_b16 v[204:205], v239 offset:36992\n"
"v_mfma_f32_16x16x32_bf16 v[88:91], v[200:203], a[24:27], v[88:91]\n"
"s_waitcnt lgkmcnt(9)\n"
"ds_read_b64_tr_b16 v[206:207], v239 offset:38528\n"
"ds_read_b64_tr_b16 v[200:201], v239 offset:24704\n"
"v_mfma_f32_16x16x32_bf16 v[68:71], v[208:211], a[0:3], v[68:71]\n"
"ds_read_b64_tr_b16 v[202:203], v239 offset:26240\n"
"v_mfma_f32_16x16x32_bf16 v[68:71], v[212:215], a[4:7], v[68:71]\n"
"s_waitcnt lgkmcnt(10)\n"
"ds_read_b128 a[56:59], v240 offset:38912\n"
"v_mfma_f32_16x16x32_bf16 v[76:79], v[212:215], a[12:15], v[76:79]\n"
"s_mov_b32 s58, 0x4a80\n"
"buffer_load_dwordx4 v228, s[36:39], s58 offen offset:2048 lds\n"
"v_mfma_f32_16x16x32_bf16 v[76:79], v[208:211], a[8:11], v[76:79]\n"
"v_mfma_f32_16x16x32_bf16 v[84:87], v[208:211], a[16:19], v[84:87]\n"
"s_mov_b32 s58, 0x6e80\n"
"buffer_load_dwordx4 v229, s[36:39], s58 offen offset:3072 lds\n"
"v_mfma_f32_16x16x32_bf16 v[84:87], v[212:215], a[20:23], v[84:87]\n"
"ds_read_b128 a[60:63], v241 offset:38912\n"
"v_mfma_f32_16x16x32_bf16 v[92:95], v[212:215], a[28:31], v[92:95]\n"
"ds_read_b64_tr_b16 v[212:213], v236 offset:37120\n"
"v_mfma_f32_16x16x32_bf16 v[92:95], v[208:211], a[24:27], v[92:95]\n"
"s_waitcnt lgkmcnt(9)\n"
"ds_read_b64_tr_b16 v[214:215], v236 offset:38656\n"
"ds_read_b64_tr_b16 v[208:209], v236 offset:24832\n"
"v_mfma_f32_16x16x32_bf16 v[96:99], v[192:195], a[0:3], v[96:99]\n"
"ds_read_b64_tr_b16 v[210:211], v236 offset:26368\n"
"v_mfma_f32_16x16x32_bf16 v[96:99], v[196:199], a[4:7], v[96:99]\n"
"s_waitcnt lgkmcnt(10)\n"
"s_add_u32 m0, s62, 0x9000\n"
"v_mfma_f32_16x16x32_bf16 v[104:107], v[196:199], a[12:15], v[104:107]\n"
"s_mov_b32 s58, 0xa280\n"
"buffer_load_dwordx4 v228, s[36:39], s58 offen offset:0 lds\n"
"v_mfma_f32_16x16x32_bf16 v[104:107], v[192:195], a[8:11], v[104:107]\n"
"v_mfma_f32_16x16x32_bf16 v[112:115], v[192:195], a[16:19], v[112:115]\n"
"s_mov_b32 s58, 0xc680\n"
"buffer_load_dwordx4 v229, s[36:39], s58 offen offset:1024 lds\n"
"v_mfma_f32_16x16x32_bf16 v[112:115], v[196:199], a[20:23], v[112:115]\n"
"v_mfma_f32_16x16x32_bf16 v[120:123], v[196:199], a[28:31], v[120:123]\n"
"ds_read_b64_tr_b16 v[196:197], v237 offset:37120\n"
"v_mfma_f32_16x16x32_bf16 v[120:123], v[192:195], a[24:27], v[120:123]\n"
"s_waitcnt lgkmcnt(7)\n"
"ds_read_b64_tr_b16 v[198:199], v237 offset:38656\n"
"ds_read_b64_tr_b16 v[192:193], v237 offset:24832\n"
"v_mfma_f32_16x16x32_bf16 v[100:103], v[200:203], a[0:3], v[100:103]\n"
"ds_read_b64_tr_b16 v[194:195], v237 offset:26368\n"
"v_mfma_f32_16x16x32_bf16 v[100:103], v[204:207], a[4:7], v[100:103]\n"
"s_waitcnt lgkmcnt(8)\n"
"v_mfma_f32_16x16x32_bf16 v[108:111], v[204:207], a[12:15], v[108:111]\n"
"s_mov_b32 s58, 0xea80\n"
"buffer_load_dwordx4 v228, s[36:39], s58 offen offset:2048 lds\n"
"v_mfma_f32_16x16x32_bf16 v[108:111], v[200:203], a[8:11], v[108:111]\n"
"v_mfma_f32_16x16x32_bf16 v[116:119], v[200:203], a[16:19], v[116:119]\n"
"s_mov_b32 s58, 0x10e80\n"
"buffer_load_dwordx4 v229, s[36:39], s58 offen offset:3072 lds\n"
"v_mfma_f32_16x16x32_bf16 v[116:119], v[204:207], a[20:23], v[116:119]\n"
"v_mfma_f32_16x16x32_bf16 v[124:127], v[204:207], a[28:31], v[124:127]\n"
"ds_read_b64_tr_b16 v[204:205], v238 offset:37120\n"
"v_mfma_f32_16x16x32_bf16 v[124:127], v[200:203], a[24:27], v[124:127]\n"
"s_waitcnt lgkmcnt(5)\n"
"ds_read_b64_tr_b16 v[206:207], v238 offset:38656\n"
"ds_read_b64_tr_b16 v[200:201], v238 offset:24832\n"
"v_mfma_f32_16x16x32_bf16 v[128:131], v[208:211], a[0:3], v[128:131]\n"
"ds_read_b64_tr_b16 v[202:203], v238 offset:26368\n"
"v_mfma_f32_16x16x32_bf16 v[128:131], v[212:215], a[4:7], v[128:131]\n"
"v_mfma_f32_16x16x32_bf16 v[136:139], v[212:215], a[12:15], v[136:139]\n"
"v_mfma_f32_16x16x32_bf16 v[136:139], v[208:211], a[8:11], v[136:139]\n"
"v_mfma_f32_16x16x32_bf16 v[144:147], v[208:211], a[16:19], v[144:147]\n"
"v_mfma_f32_16x16x32_bf16 v[144:147], v[212:215], a[20:23], v[144:147]\n"
"v_mfma_f32_16x16x32_bf16 v[152:155], v[212:215], a[28:31], v[152:155]\n"
"ds_read_b64_tr_b16 v[212:213], v239 offset:37120\n"
"v_mfma_f32_16x16x32_bf16 v[152:155], v[208:211], a[24:27], v[152:155]\n"
"s_waitcnt lgkmcnt(5)\n"
"ds_read_b64_tr_b16 v[214:215], v239 offset:38656\n"
"ds_read_b64_tr_b16 v[208:209], v239 offset:24832\n"
"v_mfma_f32_16x16x32_bf16 v[132:135], v[192:195], a[0:3], v[132:135]\n"
"ds_read_b64_tr_b16 v[210:211], v239 offset:26368\n"
"v_mfma_f32_16x16x32_bf16 v[132:135], v[196:199], a[4:7], v[132:135]\n"
"v_mfma_f32_16x16x32_bf16 v[140:143], v[196:199], a[12:15], v[140:143]\n"
"v_mfma_f32_16x16x32_bf16 v[140:143], v[192:195], a[8:11], v[140:143]\n"
"v_mfma_f32_16x16x32_bf16 v[148:151], v[192:195], a[16:19], v[148:151]\n"
"v_mfma_f32_16x16x32_bf16 v[148:151], v[196:199], a[20:23], v[148:151]\n"
"v_mfma_f32_16x16x32_bf16 v[156:159], v[196:199], a[28:31], v[156:159]\n"
"s_waitcnt vmcnt(24) lgkmcnt(0)\n"
"s_barrier\n"
"ds_read_b64_tr_b16 v[196:197], v236 offset:61440\n"
"v_mfma_f32_16x16x32_bf16 v[156:159], v[192:195], a[24:27], v[156:159]\n"
"ds_read_b64_tr_b16 v[198:199], v236 offset:62976\n"
"ds_read_b64_tr_b16 v[192:193], v236 offset:49152\n"
"v_mfma_f32_16x16x32_bf16 v[160:163], v[200:203], a[0:3], v[160:163]\n"
"ds_read_b64_tr_b16 v[194:195], v236 offset:50688\n"
"v_mfma_f32_16x16x32_bf16 v[160:163], v[204:207], a[4:7], v[160:163]\n"
"v_mfma_f32_16x16x32_bf16 v[168:171], v[204:207], a[12:15], v[168:171]\n"
"v_mfma_f32_16x16x32_bf16 v[168:171], v[200:203], a[8:11], v[168:171]\n"
"v_mfma_f32_16x16x32_bf16 v[176:179], v[200:203], a[16:19], v[176:179]\n"
"v_mfma_f32_16x16x32_bf16 v[176:179], v[204:207], a[20:23], v[176:179]\n"
"v_mfma_f32_16x16x32_bf16 v[184:187], v[204:207], a[28:31], v[184:187]\n"
"ds_read_b64_tr_b16 v[204:205], v237 offset:61440\n"
"v_mfma_f32_16x16x32_bf16 v[184:187], v[200:203], a[24:27], v[184:187]\n"
"ds_read_b64_tr_b16 v[206:207], v237 offset:62976\n"
"ds_read_b64_tr_b16 v[200:201], v237 offset:49152\n"
"v_mfma_f32_16x16x32_bf16 v[164:167], v[208:211], a[0:3], v[164:167]\n"
"ds_read_b64_tr_b16 v[202:203], v237 offset:50688\n"
"v_mfma_f32_16x16x32_bf16 v[164:167], v[212:215], a[4:7], v[164:167]\n"
"v_mfma_f32_16x16x32_bf16 v[172:175], v[212:215], a[12:15], v[172:175]\n"
"v_mfma_f32_16x16x32_bf16 v[172:175], v[208:211], a[8:11], v[172:175]\n"
"v_mfma_f32_16x16x32_bf16 v[180:183], v[208:211], a[16:19], v[180:183]\n"
"v_mfma_f32_16x16x32_bf16 v[180:183], v[212:215], a[20:23], v[180:183]\n"
"v_mfma_f32_16x16x32_bf16 v[188:191], v[212:215], a[28:31], v[188:191]\n"
"ds_read_b64_tr_b16 v[212:213], v238 offset:61440\n"
"v_mfma_f32_16x16x32_bf16 v[188:191], v[208:211], a[24:27], v[188:191]\n"
"s_waitcnt lgkmcnt(5)\n"
"ds_read_b64_tr_b16 v[214:215], v238 offset:62976\n"
"ds_read_b64_tr_b16 v[208:209], v238 offset:49152\n"
"v_mfma_f32_16x16x32_bf16 v[0:3], v[192:195], a[32:35], v[0:3]\n"
"ds_read_b64_tr_b16 v[210:211], v238 offset:50688\n"
"v_mfma_f32_16x16x32_bf16 v[0:3], v[196:199], a[36:39], v[0:3]\n"
"s_add_u32 m0, s73, 0xc000\n"
"v_mfma_f32_16x16x32_bf16 v[8:11], v[196:199], a[44:47], v[8:11]\n"
"buffer_load_dwordx4 v222, s[40:43], s59 offen offset:0 lds\n"
"v_mfma_f32_16x16x32_bf16 v[8:11], v[192:195], a[40:43], v[8:11]\n"
"v_mfma_f32_16x16x32_bf16 v[16:19], v[192:195], a[48:51], v[16:19]\n"
"buffer_load_dwordx4 v223, s[40:43], s59 offen offset:1024 lds\n"
"v_mfma_f32_16x16x32_bf16 v[16:19], v[196:199], a[52:55], v[16:19]\n"
"v_mfma_f32_16x16x32_bf16 v[24:27], v[196:199], a[60:63], v[24:27]\n"
"ds_read_b64_tr_b16 v[196:197], v239 offset:61440\n"
"v_mfma_f32_16x16x32_bf16 v[24:27], v[192:195], a[56:59], v[24:27]\n"
"s_waitcnt lgkmcnt(5)\n"
"ds_read_b64_tr_b16 v[198:199], v239 offset:62976\n"
"ds_read_b64_tr_b16 v[192:193], v239 offset:49152\n"
"v_mfma_f32_16x16x32_bf16 v[4:7], v[200:203], a[32:35], v[4:7]\n"
"ds_read_b64_tr_b16 v[194:195], v239 offset:50688\n"
"v_mfma_f32_16x16x32_bf16 v[4:7], v[204:207], a[36:39], v[4:7]\n"
"v_mfma_f32_16x16x32_bf16 v[12:15], v[204:207], a[44:47], v[12:15]\n"
"buffer_load_dwordx4 v224, s[40:43], s59 offen offset:2048 lds\n"
"v_mfma_f32_16x16x32_bf16 v[12:15], v[200:203], a[40:43], v[12:15]\n"
"v_mfma_f32_16x16x32_bf16 v[20:23], v[200:203], a[48:51], v[20:23]\n"
"buffer_load_dwordx4 v225, s[40:43], s59 offen offset:3072 lds\n"
"v_mfma_f32_16x16x32_bf16 v[20:23], v[204:207], a[52:55], v[20:23]\n"
"v_mfma_f32_16x16x32_bf16 v[28:31], v[204:207], a[60:63], v[28:31]\n"
"ds_read_b64_tr_b16 v[204:205], v236 offset:61568\n"
"v_mfma_f32_16x16x32_bf16 v[28:31], v[200:203], a[56:59], v[28:31]\n"
"s_waitcnt lgkmcnt(5)\n"
"ds_read_b64_tr_b16 v[206:207], v236 offset:63104\n"
"ds_read_b64_tr_b16 v[200:201], v236 offset:49280\n"
"v_mfma_f32_16x16x32_bf16 v[32:35], v[208:211], a[32:35], v[32:35]\n"
"s_waitcnt vmcnt(19)\n"
"ds_read_b64_tr_b16 v[202:203], v236 offset:50816\n"
"v_mfma_f32_16x16x32_bf16 v[32:35], v[212:215], a[36:39], v[32:35]\n"
"s_add_u32 m0, s73, 0xd000\n"
"ds_read_b128 a[0:3], v240 offset:0\n"
"v_mfma_f32_16x16x32_bf16 v[40:43], v[212:215], a[44:47], v[40:43]\n"
"buffer_load_dwordx4 v226, s[40:43], s59 offen offset:0 lds\n"
"v_mfma_f32_16x16x32_bf16 v[40:43], v[208:211], a[40:43], v[40:43]\n"
"v_mfma_f32_16x16x32_bf16 v[48:51], v[208:211], a[48:51], v[48:51]\n"
"buffer_load_dwordx4 v227, s[40:43], s59 offen offset:1024 lds\n"
"v_mfma_f32_16x16x32_bf16 v[48:51], v[212:215], a[52:55], v[48:51]\n"
"ds_read_b128 a[4:7], v241 offset:0\n"
"v_mfma_f32_16x16x32_bf16 v[56:59], v[212:215], a[60:63], v[56:59]\n"
"s_add_u32 s59, s59, 0xc0000\n"
"ds_read_b64_tr_b16 v[212:213], v237 offset:61568\n"
"v_mfma_f32_16x16x32_bf16 v[56:59], v[208:211], a[56:59], v[56:59]\n"
"s_waitcnt lgkmcnt(7)\n"
"ds_read_b64_tr_b16 v[214:215], v237 offset:63104\n"
"ds_read_b64_tr_b16 v[208:209], v237 offset:49280\n"
"v_mfma_f32_16x16x32_bf16 v[36:39], v[192:195], a[32:35], v[36:39]\n"
"ds_read_b64_tr_b16 v[210:211], v237 offset:50816\n"
"v_mfma_f32_16x16x32_bf16 v[36:39], v[196:199], a[36:39], v[36:39]\n"
"ds_read_b128 a[8:11], v240 offset:2048\n"
"v_mfma_f32_16x16x32_bf16 v[44:47], v[196:199], a[44:47], v[44:47]\n"
"s_add_u32 s60, s60, 0xc0000\n"
"buffer_load_dword v255, v242, s[40:43], s60 offen offset:0\n"
"v_mfma_f32_16x16x32_bf16 v[44:47], v[192:195], a[40:43], v[44:47]\n"
"v_mfma_f32_16x16x32_bf16 v[52:55], v[192:195], a[48:51], v[52:55]\n"
"v_mfma_f32_16x16x32_bf16 v[52:55], v[196:199], a[52:55], v[52:55]\n"
"ds_read_b128 a[12:15], v241 offset:2048\n"
"v_mfma_f32_16x16x32_bf16 v[60:63], v[196:199], a[60:63], v[60:63]\n"
"ds_read_b64_tr_b16 v[196:197], v238 offset:61568\n"
"v_mfma_f32_16x16x32_bf16 v[60:63], v[192:195], a[56:59], v[60:63]\n"
"s_waitcnt lgkmcnt(9)\n"
"ds_read_b64_tr_b16 v[198:199], v238 offset:63104\n"
"ds_read_b64_tr_b16 v[192:193], v238 offset:49280\n"
"v_mfma_f32_16x16x32_bf16 v[64:67], v[200:203], a[32:35], v[64:67]\n"
"ds_read_b64_tr_b16 v[194:195], v238 offset:50816\n"
"v_mfma_f32_16x16x32_bf16 v[64:67], v[204:207], a[36:39], v[64:67]\n"
"s_waitcnt lgkmcnt(10)\n"
"s_add_u32 m0, s62, 0\n"
"ds_read_b128 a[16:19], v240 offset:4096\n"
"v_mfma_f32_16x16x32_bf16 v[72:75], v[204:207], a[44:47], v[72:75]\n"
"s_mov_b32 s58, 0x300\n"
"buffer_load_dwordx4 v228, s[36:39], s58 offen offset:0 lds\n"
"v_mfma_f32_16x16x32_bf16 v[72:75], v[200:203], a[40:43], v[72:75]\n"
"v_mfma_f32_16x16x32_bf16 v[80:83], v[200:203], a[48:51], v[80:83]\n"
"s_mov_b32 s58, 0x2700\n"
"buffer_load_dwordx4 v229, s[36:39], s58 offen offset:1024 lds\n"
"v_mfma_f32_16x16x32_bf16 v[80:83], v[204:207], a[52:55], v[80:83]\n"
"ds_read_b128 a[20:23], v241 offset:4096\n"
"v_mfma_f32_16x16x32_bf16 v[88:91], v[204:207], a[60:63], v[88:91]\n"
"ds_read_b64_tr_b16 v[204:205], v239 offset:61568\n"
"v_mfma_f32_16x16x32_bf16 v[88:91], v[200:203], a[56:59], v[88:91]\n"
"s_waitcnt lgkmcnt(9)\n"
"ds_read_b64_tr_b16 v[206:207], v239 offset:63104\n"
"ds_read_b64_tr_b16 v[200:201], v239 offset:49280\n"
"v_mfma_f32_16x16x32_bf16 v[68:71], v[208:211], a[32:35], v[68:71]\n"
"ds_read_b64_tr_b16 v[202:203], v239 offset:50816\n"
"v_mfma_f32_16x16x32_bf16 v[68:71], v[212:215], a[36:39], v[68:71]\n"
"s_waitcnt lgkmcnt(10)\n"
"ds_read_b128 a[24:27], v240 offset:6144\n"
"v_mfma_f32_16x16x32_bf16 v[76:79], v[212:215], a[44:47], v[76:79]\n"
"s_mov_b32 s58, 0x4b00\n"
"buffer_load_dwordx4 v228, s[36:39], s58 offen offset:2048 lds\n"
"v_mfma_f32_16x16x32_bf16 v[76:79], v[208:211], a[40:43], v[76:79]\n"
"v_mfma_f32_16x16x32_bf16 v[84:87], v[208:211], a[48:51], v[84:87]\n"
"s_mov_b32 s58, 0x6f00\n"
"buffer_load_dwordx4 v229, s[36:39], s58 offen offset:3072 lds\n"
"v_mfma_f32_16x16x32_bf16 v[84:87], v[212:215], a[52:55], v[84:87]\n"
"ds_read_b128 a[28:31], v241 offset:6144\n"
"v_mfma_f32_16x16x32_bf16 v[92:95], v[212:215], a[60:63], v[92:95]\n"
"ds_read_b64_tr_b16 v[212:213], v236 offset:61696\n"
"v_mfma_f32_16x16x32_bf16 v[92:95], v[208:211], a[56:59], v[92:95]\n"
"s_waitcnt lgkmcnt(9)\n"
"ds_read_b64_tr_b16 v[214:215], v236 offset:63232\n"
"ds_read_b64_tr_b16 v[208:209], v236 offset:49408\n"
"v_mfma_f32_16x16x32_bf16 v[96:99], v[192:195], a[32:35], v[96:99]\n"
"ds_read_b64_tr_b16 v[210:211], v236 offset:50944\n"
"v_mfma_f32_16x16x32_bf16 v[96:99], v[196:199], a[36:39], v[96:99]\n"
"s_waitcnt lgkmcnt(10)\n"
"s_add_u32 m0, s62, 0x1000\n"
"v_mfma_f32_16x16x32_bf16 v[104:107], v[196:199], a[44:47], v[104:107]\n"
"s_mov_b32 s58, 0xa300\n"
"buffer_load_dwordx4 v228, s[36:39], s58 offen offset:0 lds\n"
"v_mfma_f32_16x16x32_bf16 v[104:107], v[192:195], a[40:43], v[104:107]\n"
"v_mfma_f32_16x16x32_bf16 v[112:115], v[192:195], a[48:51], v[112:115]\n"
"s_mov_b32 s58, 0xc700\n"
"buffer_load_dwordx4 v229, s[36:39], s58 offen offset:1024 lds\n"
"v_mfma_f32_16x16x32_bf16 v[112:115], v[196:199], a[52:55], v[112:115]\n"
"v_mfma_f32_16x16x32_bf16 v[120:123], v[196:199], a[60:63], v[120:123]\n"
"ds_read_b64_tr_b16 v[196:197], v237 offset:61696\n"
"v_mfma_f32_16x16x32_bf16 v[120:123], v[192:195], a[56:59], v[120:123]\n"
"s_waitcnt lgkmcnt(7)\n"
"ds_read_b64_tr_b16 v[198:199], v237 offset:63232\n"
"ds_read_b64_tr_b16 v[192:193], v237 offset:49408\n"
"v_mfma_f32_16x16x32_bf16 v[100:103], v[200:203], a[32:35], v[100:103]\n"
"ds_read_b64_tr_b16 v[194:195], v237 offset:50944\n"
"v_mfma_f32_16x16x32_bf16 v[100:103], v[204:207], a[36:39], v[100:103]\n"
"s_waitcnt lgkmcnt(8)\n"
"v_mfma_f32_16x16x32_bf16 v[108:111], v[204:207], a[44:47], v[108:111]\n"
"s_mov_b32 s58, 0xeb00\n"
"buffer_load_dwordx4 v228, s[36:39], s58 offen offset:2048 lds\n"
"v_mfma_f32_16x16x32_bf16 v[108:111], v[200:203], a[40:43], v[108:111]\n"
"v_mfma_f32_16x16x32_bf16 v[116:119], v[200:203], a[48:51], v[116:119]\n"
"s_mov_b32 s58, 0x10f00\n"
"buffer_load_dwordx4 v229, s[36:39], s58 offen offset:3072 lds\n"
"v_mfma_f32_16x16x32_bf16 v[116:119], v[204:207], a[52:55], v[116:119]\n"
"v_mfma_f32_16x16x32_bf16 v[124:127], v[204:207], a[60:63], v[124:127]\n"
"ds_read_b64_tr_b16 v[204:205], v238 offset:61696\n"
"v_mfma_f32_16x16x32_bf16 v[124:127], v[200:203], a[56:59], v[124:127]\n"
"s_waitcnt lgkmcnt(5)\n"
"ds_read_b64_tr_b16 v[206:207], v238 offset:63232\n"
"ds_read_b64_tr_b16 v[200:201], v238 offset:49408\n"
"v_mfma_f32_16x16x32_bf16 v[128:131], v[208:211], a[32:35], v[128:131]\n"
"ds_read_b64_tr_b16 v[202:203], v238 offset:50944\n"
"v_mfma_f32_16x16x32_bf16 v[128:131], v[212:215], a[36:39], v[128:131]\n"
"v_mfma_f32_16x16x32_bf16 v[136:139], v[212:215], a[44:47], v[136:139]\n"
"v_mfma_f32_16x16x32_bf16 v[136:139], v[208:211], a[40:43], v[136:139]\n"
"v_mfma_f32_16x16x32_bf16 v[144:147], v[208:211], a[48:51], v[144:147]\n"
"v_mfma_f32_16x16x32_bf16 v[144:147], v[212:215], a[52:55], v[144:147]\n"
"v_mfma_f32_16x16x32_bf16 v[152:155], v[212:215], a[60:63], v[152:155]\n"
"ds_read_b64_tr_b16 v[212:213], v239 offset:61696\n"
"v_mfma_f32_16x16x32_bf16 v[152:155], v[208:211], a[56:59], v[152:155]\n"
"s_waitcnt lgkmcnt(5)\n"
"ds_read_b64_tr_b16 v[214:215], v239 offset:63232\n"
"ds_read_b64_tr_b16 v[208:209], v239 offset:49408\n"
"v_mfma_f32_16x16x32_bf16 v[132:135], v[192:195], a[32:35], v[132:135]\n"
"ds_read_b64_tr_b16 v[210:211], v239 offset:50944\n"
"v_mfma_f32_16x16x32_bf16 v[132:135], v[196:199], a[36:39], v[132:135]\n"
"v_mfma_f32_16x16x32_bf16 v[140:143], v[196:199], a[44:47], v[140:143]\n"
"v_mfma_f32_16x16x32_bf16 v[140:143], v[192:195], a[40:43], v[140:143]\n"
"v_mfma_f32_16x16x32_bf16 v[148:151], v[192:195], a[48:51], v[148:151]\n"
"v_mfma_f32_16x16x32_bf16 v[148:151], v[196:199], a[52:55], v[148:151]\n"
"v_mfma_f32_16x16x32_bf16 v[156:159], v[196:199], a[60:63], v[156:159]\n"
"s_waitcnt vmcnt(24) lgkmcnt(0)\n"
"s_barrier\n"
"s_memrealtime s[28:29]\n"
"ds_read_b64_tr_b16 v[196:197], v236 offset:12288\n"
"v_mfma_f32_16x16x32_bf16 v[156:159], v[192:195], a[56:59], v[156:159]\n"
"ds_read_b64_tr_b16 v[198:199], v236 offset:13824\n"
"ds_read_b64_tr_b16 v[192:193], v236 offset:0\n"
"v_mfma_f32_16x16x32_bf16 v[160:163], v[200:203], a[32:35], v[160:163]\n"
"ds_read_b64_tr_b16 v[194:195], v236 offset:1536\n"
"v_mfma_f32_16x16x32_bf16 v[160:163], v[204:207], a[36:39], v[160:163]\n"
"v_mfma_f32_16x16x32_bf16 v[168:171], v[204:207], a[44:47], v[168:171]\n"
"v_mfma_f32_16x16x32_bf16 v[168:171], v[200:203], a[40:43], v[168:171]\n"
"v_mfma_f32_16x16x32_bf16 v[176:179], v[200:203], a[48:51], v[176:179]\n"
"v_mfma_f32_16x16x32_bf16 v[176:179], v[204:207], a[52:55], v[176:179]\n"
"Ltp2%=:\n"
"s_cbranch_execz Ltp1%=\n"
"v_mfma_f32_16x16x32_bf16 v[184:187], v[204:207], a[60:63], v[184:187]\n"
"ds_read_b64_tr_b16 v[204:205], v237 offset:12288\n"
"v_mfma_f32_16x16x32_bf16 v[184:187], v[200:203], a[56:59], v[184:187]\n"
"ds_read_b64_tr_b16 v[206:207], v237 offset:13824\n"
"ds_read_b64_tr_b16 v[200:201], v237 offset:0\n"
"v_mfma_f32_16x16x32_bf16 v[164:167], v[208:211], a[32:35], v[164:167]\n"
"ds_read_b64_tr_b16 v[202:203], v237 offset:1536\n"
"v_mfma_f32_16x16x32_bf16 v[164:167], v[212:215], a[36:39], v[164:167]\n"
"v_mfma_f32_16x16x32_bf16 v[172:175], v[212:215], a[44:47], v[172:175]\n"
"v_mfma_f32_16x16x32_bf16 v[172:175], v[208:211], a[40:43], v[172:175]\n"
"v_mfma_f32_16x16x32_bf16 v[180:183], v[208:211], a[48:51], v[180:183]\n"
"v_mfma_f32_16x16x32_bf16 v[180:183], v[212:215], a[52:55], v[180:183]\n"
"v_mfma_f32_16x16x32_bf16 v[188:191], v[212:215], a[60:63], v[188:191]\n"
"ds_read_b64_tr_b16 v[212:213], v238 offset:12288\n"
"v_mfma_f32_16x16x32_bf16 v[188:191], v[208:211], a[56:59], v[188:191]\n"
"s_waitcnt lgkmcnt(5)\n"
"ds_read_b64_tr_b16 v[214:215], v238 offset:13824\n"
"ds_read_b64_tr_b16 v[208:209], v238 offset:0\n"
"v_mfma_f32_16x16x32_bf16 v[0:3], v[192:195], a[0:3], v[0:3]\n"
"ds_read_b64_tr_b16 v[210:211], v238 offset:1536\n"
"v_mfma_f32_16x16x32_bf16 v[0:3], v[196:199], a[4:7], v[0:3]\n"
"s_add_u32 m0, s73, 0x12000\n"
"v_mfma_f32_16x16x32_bf16 v[8:11], v[196:199], a[12:15], v[8:11]\n"
"buffer_load_dwordx4 v222, s[40:43], s59 offen offset:0 lds\n"
"v_mfma_f32_16x16x32_bf16 v[8:11], v[192:195], a[8:11], v[8:11]\n"
"v_mfma_f32_16x16x32_bf16 v[16:19], v[192:195], a[16:19], v[16:19]\n"
"buffer_load_dwordx4 v223, s[40:43], s59 offen offset:1024 lds\n"
"v_mfma_f32_16x16x32_bf16 v[16:19], v[196:199], a[20:23], v[16:19]\n"
"v_mfma_f32_16x16x32_bf16 v[24:27], v[196:199], a[28:31], v[24:27]\n"
"ds_read_b64_tr_b16 v[196:197], v239 offset:12288\n"
"v_mfma_f32_16x16x32_bf16 v[24:27], v[192:195], a[24:27], v[24:27]\n"
"s_waitcnt lgkmcnt(5)\n"
"ds_read_b64_tr_b16 v[198:199], v239 offset:13824\n"
"ds_read_b64_tr_b16 v[192:193], v239 offset:0\n"
"v_mfma_f32_16x16x32_bf16 v[4:7], v[200:203], a[0:3], v[4:7]\n"
"ds_read_b64_tr_b16 v[194:195], v239 offset:1536\n"
"v_mfma_f32_16x16x32_bf16 v[4:7], v[204:207], a[4:7], v[4:7]\n"
"v_mfma_f32_16x16x32_bf16 v[12:15], v[204:207], a[12:15], v[12:15]\n"
"buffer_load_dwordx4 v224, s[40:43], s59 offen offset:2048 lds\n"
"v_mfma_f32_16x16x32_bf16 v[12:15], v[200:203], a[8:11], v[12:15]\n"
"v_mfma_f32_16x16x32_bf16 v[20:23], v[200:203], a[16:19], v[20:23]\n"
"buffer_load_dwordx4 v225, s[40:43], s59 offen offset:3072 lds\n"
"v_mfma_f32_16x16x32_bf16 v[20:23], v[204:207], a[20:23], v[20:23]\n"
"v_mfma_f32_16x16x32_bf16 v[28:31], v[204:207], a[28:31], v[28:31]\n"
"ds_read_b64_tr_b16 v[204:205], v236 offset:12416\n"
"v_mfma_f32_16x16x32_bf16 v[28:31], v[200:203], a[24:27], v[28:31]\n"
"s_waitcnt lgkmcnt(5)\n"
"ds_read_b64_tr_b16 v[206:207], v236 offset:13952\n"
"ds_read_b64_tr_b16 v[200:201], v236 offset:128\n"
"v_mfma_f32_16x16x32_bf16 v[32:35], v[208:211], a[0:3], v[32:35]\n"
"s_waitcnt vmcnt(19)\n"
"ds_read_b64_tr_b16 v[202:203], v236 offset:1664\n"
"v_mfma_f32_16x16x32_bf16 v[32:35], v[212:215], a[4:7], v[32:35]\n"
"s_add_u32 m0, s73, 0x13000\n"
"ds_read_b128 a[32:35], v240 offset:32768\n"
"v_mfma_f32_16x16x32_bf16 v[40:43], v[212:215], a[12:15], v[40:43]\n"
"buffer_load_dwordx4 v226, s[40:43], s59 offen offset:0 lds\n"
"v_mfma_f32_16x16x32_bf16 v[40:43], v[208:211], a[8:11], v[40:43]\n"
"v_mfma_f32_16x16x32_bf16 v[48:51], v[208:211], a[16:19], v[48:51]\n"
"buffer_load_dwordx4 v227, s[40:43], s59 offen offset:1024 lds\n"
"v_mfma_f32_16x16x32_bf16 v[48:51], v[212:215], a[20:23], v[48:51]\n"
"ds_read_b128 a[36:39], v241 offset:32768\n"
"v_mfma_f32_16x16x32_bf16 v[56:59], v[212:215], a[28:31], v[56:59]\n"
"s_add_u32 s59, s59, 0xc0000\n"
"ds_read_b64_tr_b16 v[212:213], v237 offset:12416\n"
"v_mfma_f32_16x16x32_bf16 v[56:59], v[208:211], a[24:27], v[56:59]\n"
"s_waitcnt lgkmcnt(7)\n"
"ds_read_b64_tr_b16 v[214:215], v237 offset:13952\n"
"ds_read_b64_tr_b16 v[208:209], v237 offset:128\n"
"v_mfma_f32_16x16x32_bf16 v[36:39], v[192:195], a[0:3], v[36:39]\n"
"ds_read_b64_tr_b16 v[210:211], v237 offset:1664\n"
"v_mfma_f32_16x16x32_bf16 v[36:39], v[196:199], a[4:7], v[36:39]\n"
"ds_read_b128 a[40:43], v240 offset:34816\n"
"v_mfma_f32_16x16x32_bf16 v[44:47], v[196:199], a[12:15], v[44:47]\n"
"s_add_u32 s60, s60, 0xc0000\n"
"buffer_load_dword v255, v242, s[40:43], s60 offen offset:0\n"
"v_mfma_f32_16x16x32_bf16 v[44:47], v[192:195], a[8:11], v[44:47]\n"
"v_mfma_f32_16x16x32_bf16 v[52:55], v[192:195], a[16:19], v[52:55]\n"
"v_mfma_f32_16x16x32_bf16 v[52:55], v[196:199], a[20:23], v[52:55]\n"
"ds_read_b128 a[44:47], v241 offset:34816\n"
"v_mfma_f32_16x16x32_bf16 v[60:63], v[196:199], a[28:31], v[60:63]\n"
"ds_read_b64_tr_b16 v[196:197], v238 offset:12416\n"
"v_mfma_f32_16x16x32_bf16 v[60:63], v[192:195], a[24:27], v[60:63]\n"
"s_waitcnt lgkmcnt(9)\n"
"ds_read_b64_tr_b16 v[198:199], v238 offset:13952\n"
"ds_read_b64_tr_b16 v[192:193], v238 offset:128\n"
"v_mfma_f32_16x16x32_bf16 v[64:67], v[200:203], a[0:3], v[64:67]\n"
"ds_read_b64_tr_b16 v[194:195], v238 offset:1664\n"
"v_mfma_f32_16x16x32_bf16 v[64:67], v[204:207], a[4:7], v[64:67]\n"
"s_waitcnt lgkmcnt(10)\n"
"s_add_u32 m0, s62, 0x8000\n"
"ds_read_b128 a[48:51], v240 offset:36864\n"
"v_mfma_f32_16x16x32_bf16 v[72:75], v[204:207], a[12:15], v[72:75]\n"
"s_mov_b32 s58, 0x380\n"
"buffer_load_dwordx4 v228, s[36:39], s58 offen offset:0 lds\n"
"v_mfma_f32_16x16x32_bf16 v[72:75], v[200:203], a[8:11], v[72:75]\n"
"v_mfma_f32_16x16x32_bf16 v[80:83], v[200:203], a[16:19], v[80:83]\n"
"s_mov_b32 s58, 0x2780\n"
"buffer_load_dwordx4 v229, s[36:39], s58 offen offset:1024 lds\n"
"v_mfma_f32_16x16x32_bf16 v[80:83], v[204:207], a[20:23], v[80:83]\n"
"ds_read_b128 a[52:55], v241 offset:36864\n"
"v_mfma_f32_16x16x32_bf16 v[88:91], v[204:207], a[28:31], v[88:91]\n"
"ds_read_b64_tr_b16 v[204:205], v239 offset:12416\n"
"v_mfma_f32_16x16x32_bf16 v[88:91], v[200:203], a[24:27], v[88:91]\n"
"s_waitcnt lgkmcnt(9)\n"
"ds_read_b64_tr_b16 v[206:207], v239 offset:13952\n"
"ds_read_b64_tr_b16 v[200:201], v239 offset:128\n"
"v_mfma_f32_16x16x32_bf16 v[68:71], v[208:211], a[0:3], v[68:71]\n"
"ds_read_b64_tr_b16 v[202:203], v239 offset:1664\n"
"v_mfma_f32_16x16x32_bf16 v[68:71], v[212:215], a[4:7], v[68:71]\n"
"s_waitcnt lgkmcnt(10)\n"
"ds_read_b128 a[56:59], v240 offset:38912\n"
"v_mfma_f32_16x16x32_bf16 v[76:79], v[212:215], a[12:15], v[76:79]\n"
"s_mov_b32 s58, 0x4b80\n"
"buffer_load_dwordx4 v228, s[36:39], s58 offen offset:2048 lds\n"
"v_mfma_f32_16x16x32_bf16 v[76:79], v[208:211], a[8:11], v[76:79]\n"
"v_mfma_f32_16x16x32_bf16 v[84:87], v[208:211], a[16:19], v[84:87]\n"
"s_mov_b32 s58, 0x6f80\n"
"buffer_load_dwordx4 v229, s[36:39], s58 offen offset:3072 lds\n"
"v_mfma_f32_16x16x32_bf16 v[84:87], v[212:215], a[20:23], v[84:87]\n"
"ds_read_b128 a[60:63], v241 offset:38912\n"
"v_mfma_f32_16x16x32_bf16 v[92:95], v[212:215], a[28:31], v[92:95]\n"
"ds_read_b64_tr_b16 v[212:213], v236 offset:12544\n"
"v_mfma_f32_16x16x32_bf16 v[92:95], v[208:211], a[24:27], v[92:95]\n"
"s_waitcnt lgkmcnt(9)\n"
"ds_read_b64_tr_b16 v[214:215], v236 offset:14080\n"
"ds_read_b64_tr_b16 v[208:209], v236 offset:256\n"
"v_mfma_f32_16x16x32_bf16 v[96:99], v[192:195], a[0:3], v[96:99]\n"
"ds_read_b64_tr_b16 v[210:211], v236 offset:1792\n"
"v_mfma_f32_16x16x32_bf16 v[96:99], v[196:199], a[4:7], v[96:99]\n"
"s_waitcnt lgkmcnt(10)\n"
"s_add_u32 m0, s62, 0x9000\n"
"v_mfma_f32_16x16x32_bf16 v[104:107], v[196:199], a[12:15], v[104:107]\n"
"s_mov_b32 s58, 0xa380\n"
"buffer_load_dwordx4 v228, s[36:39], s58 offen offset:0 lds\n"
"v_mfma_f32_16x16x32_bf16 v[104:107], v[192:195], a[8:11], v[104:107]\n"
"v_mfma_f32_16x16x32_bf16 v[112:115], v[192:195], a[16:19], v[112:115]\n"
"s_mov_b32 s58, 0xc780\n"
"buffer_load_dwordx4 v229, s[36:39], s58 offen offset:1024 lds\n"
"v_mfma_f32_16x16x32_bf16 v[112:115], v[196:199], a[20:23], v[112:115]\n"
"v_mfma_f32_16x16x32_bf16 v[120:123], v[196:199], a[28:31], v[120:123]\n"
"ds_read_b64_tr_b16 v[196:197], v237 offset:12544\n"
"v_mfma_f32_16x16x32_bf16 v[120:123], v[192:195], a[24:27], v[120:123]\n"
"s_waitcnt lgkmcnt(7)\n"
"ds_read_b64_tr_b16 v[198:199], v237 offset:14080\n"
"ds_read_b64_tr_b16 v[192:193], v237 offset:256\n"
"v_mfma_f32_16x16x32_bf16 v[100:103], v[200:203], a[0:3], v[100:103]\n"
"ds_read_b64_tr_b16 v[194:195], v237 offset:1792\n"
"v_mfma_f32_16x16x32_bf16 v[100:103], v[204:207], a[4:7], v[100:103]\n"
"s_waitcnt lgkmcnt(8)\n"
"v_mfma_f32_16x16x32_bf16 v[108:111], v[204:207], a[12:15], v[108:111]\n"
"s_mov_b32 s58, 0xeb80\n"
"buffer_load_dwordx4 v228, s[36:39], s58 offen offset:2048 lds\n"
"v_mfma_f32_16x16x32_bf16 v[108:111], v[200:203], a[8:11], v[108:111]\n"
"v_mfma_f32_16x16x32_bf16 v[116:119], v[200:203], a[16:19], v[116:119]\n"
"s_mov_b32 s58, 0x10f80\n"
"buffer_load_dwordx4 v229, s[36:39], s58 offen offset:3072 lds\n"
"v_mfma_f32_16x16x32_bf16 v[116:119], v[204:207], a[20:23], v[116:119]\n"
"v_mfma_f32_16x16x32_bf16 v[124:127], v[204:207], a[28:31], v[124:127]\n"
"ds_read_b64_tr_b16 v[204:205], v238 offset:12544\n"
"v_mfma_f32_16x16x32_bf16 v[124:127], v[200:203], a[24:27], v[124:127]\n"
"s_waitcnt lgkmcnt(5)\n"
"ds_read_b64_tr_b16 v[206:207], v238 offset:14080\n"
"ds_read_b64_tr_b16 v[200:201], v238 offset:256\n"
"v_mfma_f32_16x16x32_bf16 v[128:131], v[208:211], a[0:3], v[128:131]\n"
"s_sub_u32 s95, s6, s94\n"
"ds_read_b64_tr_b16 v[202:203], v238 offset:1792\n"
"v_mfma_f32_16x16x32_bf16 v[128:131], v[212:215], a[4:7], v[128:131]\n"
"v_mfma_f32_16x16x32_bf16 v[136:139], v[212:215], a[12:15], v[136:139]\n"
"v_mfma_f32_16x16x32_bf16 v[136:139], v[208:211], a[8:11], v[136:139]\n"
"v_mfma_f32_16x16x32_bf16 v[144:147], v[208:211], a[16:19], v[144:147]\n"
"v_mfma_f32_16x16x32_bf16 v[144:147], v[212:215], a[20:23], v[144:147]\n"
"v_mfma_f32_16x16x32_bf16 v[152:155], v[212:215], a[28:31], v[152:155]\n"
"ds_read_b64_tr_b16 v[212:213], v239 offset:12544\n"
"v_mfma_f32_16x16x32_bf16 v[152:155], v[208:211], a[24:27], v[152:155]\n"
"s_waitcnt lgkmcnt(5)\n"
"ds_read_b64_tr_b16 v[214:215], v239 offset:14080\n"
"ds_read_b64_tr_b16 v[208:209], v239 offset:256\n"
"v_mfma_f32_16x16x32_bf16 v[132:135], v[192:195], a[0:3], v[132:135]\n"
"ds_read_b64_tr_b16 v[210:211], v239 offset:1792\n"
"v_mfma_f32_16x16x32_bf16 v[132:135], v[196:199], a[4:7], v[132:135]\n"
"v_mfma_f32_16x16x32_bf16 v[140:143], v[196:199], a[12:15], v[140:143]\n"
"v_mfma_f32_16x16x32_bf16 v[140:143], v[192:195], a[8:11], v[140:143]\n"
"v_mfma_f32_16x16x32_bf16 v[148:151], v[192:195], a[16:19], v[148:151]\n"
"v_mfma_f32_16x16x32_bf16 v[148:151], v[196:199], a[20:23], v[148:151]\n"
"v_mfma_f32_16x16x32_bf16 v[156:159], v[196:199], a[28:31], v[156:159]\n"
"s_waitcnt vmcnt(24) lgkmcnt(0)\n"
"s_barrier\n"
"ds_read_b64_tr_b16 v[196:197], v236 offset:36864\n"
"v_mfma_f32_16x16x32_bf16 v[156:159], v[192:195], a[24:27], v[156:159]\n"
"ds_read_b64_tr_b16 v[198:199], v236 offset:38400\n"
"ds_read_b64_tr_b16 v[192:193], v236 offset:24576\n"
"v_mfma_f32_16x16x32_bf16 v[160:163], v[200:203], a[0:3], v[160:163]\n"
"ds_read_b64_tr_b16 v[194:195], v236 offset:26112\n"
"v_mfma_f32_16x16x32_bf16 v[160:163], v[204:207], a[4:7], v[160:163]\n"
"v_mfma_f32_16x16x32_bf16 v[168:171], v[204:207], a[12:15], v[168:171]\n"
"v_mfma_f32_16x16x32_bf16 v[168:171], v[200:203], a[8:11], v[168:171]\n"
"v_mfma_f32_16x16x32_bf16 v[176:179], v[200:203], a[16:19], v[176:179]\n"
"v_mfma_f32_16x16x32_bf16 v[176:179], v[204:207], a[20:23], v[176:179]\n"
"v_mfma_f32_16x16x32_bf16 v[184:187], v[204:207], a[28:31], v[184:187]\n"
"ds_read_b64_tr_b16 v[204:205], v237 offset:36864\n"
"v_mfma_f32_16x16x32_bf16 v[184:187], v[200:203], a[24:27], v[184:187]\n"
"ds_read_b64_tr_b16 v[206:207], v237 offset:38400\n"
"ds_read_b64_tr_b16 v[200:201], v237 offset:24576\n"
"v_mfma_f32_16x16x32_bf16 v[164:167], v[208:211], a[0:3], v[164:167]\n"
"ds_read_b64_tr_b16 v[202:203], v237 offset:26112\n"
"v_mfma_f32_16x16x32_bf16 v[164:167], v[212:215], a[4:7], v[164:167]\n"
"v_mfma_f32_16x16x32_bf16 v[172:175], v[212:215], a[12:15], v[172:175]\n"
"v_mfma_f32_16x16x32_bf16 v[172:175], v[208:211], a[8:11], v[172:175]\n"
"v_mfma_f32_16x16x32_bf16 v[180:183], v[208:211], a[16:19], v[180:183]\n"
"v_mfma_f32_16x16x32_bf16 v[180:183], v[212:215], a[20:23], v[180:183]\n"
"v_mfma_f32_16x16x32_bf16 v[188:191], v[212:215], a[28:31], v[188:191]\n"
"ds_read_b64_tr_b16 v[212:213], v238 offset:36864\n"
"v_mfma_f32_16x16x32_bf16 v[188:191], v[208:211], a[24:27], v[188:191]\n"
"s_waitcnt lgkmcnt(5)\n"
"ds_read_b64_tr_b16 v[214:215], v238 offset:38400\n"
"ds_read_b64_tr_b16 v[208:209], v238 offset:24576\n"
"v_mfma_f32_16x16x32_bf16 v[0:3], v[192:195], a[32:35], v[0:3]\n"
"ds_read_b64_tr_b16 v[210:211], v238 offset:26112\n"
"v_mfma_f32_16x16x32_bf16 v[0:3], v[196:199], a[36:39], v[0:3]\n"
"s_add_u32 m0, s73, 0x6000\n"
"v_mfma_f32_16x16x32_bf16 v[8:11], v[196:199], a[44:47], v[8:11]\n"
"buffer_load_dwordx4 v222, s[40:43], s59 offen offset:0 lds\n"
"v_mfma_f32_16x16x32_bf16 v[8:11], v[192:195], a[40:43], v[8:11]\n"
"v_mfma_f32_16x16x32_bf16 v[16:19], v[192:195], a[48:51], v[16:19]\n"
"buffer_load_dwordx4 v223, s[40:43], s59 offen offset:1024 lds\n"
"v_mfma_f32_16x16x32_bf16 v[16:19], v[196:199], a[52:55], v[16:19]\n"
"v_mfma_f32_16x16x32_bf16 v[24:27], v[196:199], a[60:63], v[24:27]\n"
"ds_read_b64_tr_b16 v[196:197], v239 offset:36864\n"
"v_mfma_f32_16x16x32_bf16 v[24:27], v[192:195], a[56:59], v[24:27]\n"
"s_waitcnt lgkmcnt(5)\n"
"ds_read_b64_tr_b16 v[198:199], v239 offset:38400\n"
"ds_read_b64_tr_b16 v[192:193], v239 offset:24576\n"
"v_mfma_f32_16x16x32_bf16 v[4:7], v[200:203], a[32:35], v[4:7]\n"
"ds_read_b64_tr_b16 v[194:195], v239 offset:26112\n"
"v_mfma_f32_16x16x32_bf16 v[4:7], v[204:207], a[36:39], v[4:7]\n"
"v_mfma_f32_16x16x32_bf16 v[12:15], v[204:207], a[44:47], v[12:15]\n"
"buffer_load_dwordx4 v224, s[40:43], s59 offen offset:2048 lds\n"
"v_mfma_f32_16x16x32_bf16 v[12:15], v[200:203], a[40:43], v[12:15]\n"
"v_mfma_f32_16x16x32_bf16 v[20:23], v[200:203], a[48:51], v[20:23]\n"
"buffer_load_dwordx4 v225, s[40:43], s59 offen offset:3072 lds\n"
"v_mfma_f32_16x16x32_bf16 v[20:23], v[204:207], a[52:55], v[20:23]\n"
"v_mfma_f32_16x16x32_bf16 v[28:31], v[204:207], a[60:63], v[28:31]\n"
"ds_read_b64_tr_b16 v[204:205], v236 offset:36992\n"
"v_mfma_f32_16x16x32_bf16 v[28:31], v[200:203], a[56:59], v[28:31]\n"
"s_waitcnt lgkmcnt(5)\n"
"ds_read_b64_tr_b16 v[206:207], v236 offset:38528\n"
"ds_read_b64_tr_b16 v[200:201], v236 offset:24704\n"
"v_mfma_f32_16x16x32_bf16 v[32:35], v[208:211], a[32:35], v[32:35]\n"
"s_waitcnt vmcnt(19)\n"
"ds_read_b64_tr_b16 v[202:203], v236 offset:26240\n"
"v_mfma_f32_16x16x32_bf16 v[32:35], v[212:215], a[36:39], v[32:35]\n"
"s_add_u32 m0, s73, 0x7000\n"
"ds_read_b128 a[0:3], v240 offset:0\n"
"v_mfma_f32_16x16x32_bf16 v[40:43], v[212:215], a[44:47], v[40:43]\n"
"buffer_load_dwordx4 v226, s[40:43], s59 offen offset:0 lds\n"
"v_mfma_f32_16x16x32_bf16 v[40:43], v[208:211], a[40:43], v[40:43]\n"
"v_mfma_f32_16x16x32_bf16 v[48:51], v[208:211], a[48:51], v[48:51]\n"
"buffer_load_dwordx4 v227, s[40:43], s59 offen offset:1024 lds\n"
"v_mfma_f32_16x16x32_bf16 v[48:51], v[212:215], a[52:55], v[48:51]\n"
"ds_read_b128 a[4:7], v241 offset:0\n"
"v_mfma_f32_16x16x32_bf16 v[56:59], v[212:215], a[60:63], v[56:59]\n"
"s_add_u32 s59, s59, 0xc0000\n"
"ds_read_b64_tr_b16 v[212:213], v237 offset:36992\n"
"v_mfma_f32_16x16x32_bf16 v[56:59], v[208:211], a[56:59], v[56:59]\n"
"s_waitcnt lgkmcnt(7)\n"
"ds_read_b64_tr_b16 v[214:215], v237 offset:38528\n"
"ds_read_b64_tr_b16 v[208:209], v237 offset:24704\n"
"v_mfma_f32_16x16x32_bf16 v[36:39], v[192:195], a[32:35], v[36:39]\n"
"ds_read_b64_tr_b16 v[210:211], v237 offset:26240\n"
"v_mfma_f32_16x16x32_bf16 v[36:39], v[196:199], a[36:39], v[36:39]\n"
"ds_read_b128 a[8:11], v240 offset:2048\n"
"v_mfma_f32_16x16x32_bf16 v[44:47], v[196:199], a[44:47], v[44:47]\n"
"s_add_u32 s60, s60, 0xc0000\n"
"buffer_load_dword v255, v242, s[40:43], s60 offen offset:0\n"
"v_mfma_f32_16x16x32_bf16 v[44:47], v[192:195], a[40:43], v[44:47]\n"
"v_mfma_f32_16x16x32_bf16 v[52:55], v[192:195], a[48:51], v[52:55]\n"
"v_mfma_f32_16x16x32_bf16 v[52:55], v[196:199], a[52:55], v[52:55]\n"
"ds_read_b128 a[12:15], v241 offset:2048\n"
"v_mfma_f32_16x16x32_bf16 v[60:63], v[196:199], a[60:63], v[60:63]\n"
"ds_read_b64_tr_b16 v[196:197], v238 offset:36992\n"
"v_mfma_f32_16x16x32_bf16 v[60:63], v[192:195], a[56:59], v[60:63]\n"
"s_waitcnt lgkmcnt(9)\n"
"ds_read_b64_tr_b16 v[198:199], v238 offset:38528\n"
"ds_read_b64_tr_b16 v[192:193], v238 offset:24704\n"
"v_mfma_f32_16x16x32_bf16 v[64:67], v[200:203], a[32:35], v[64:67]\n"
"ds_read_b64_tr_b16 v[194:195], v238 offset:26240\n"
"v_mfma_f32_16x16x32_bf16 v[64:67], v[204:207], a[36:39], v[64:67]\n"
"s_waitcnt lgkmcnt(10)\n"
"s_add_u32 m0, s62, 0\n"
"ds_read_b128 a[16:19], v240 offset:4096\n"
"v_mfma_f32_16x16x32_bf16 v[72:75], v[204:207], a[44:47], v[72:75]\n"
"s_mov_b32 s58, 0x400\n"
"buffer_load_dwordx4 v228, s[36:39], s58 offen offset:0 lds\n"
"v_mfma_f32_16x16x32_bf16 v[72:75], v[200:203], a[40:43], v[72:75]\n"
"v_mfma_f32_16x16x32_bf16 v[80:83], v[200:203], a[48:51], v[80:83]\n"
"s_mov_b32 s58, 0x2800\n"
"buffer_load_dwordx4 v229, s[36:39], s58 offen offset:1024 lds\n"
"v_mfma_f32_16x16x32_bf16 v[80:83], v[204:207], a[52:55], v[80:83]\n"
"ds_read_b128 a[20:23], v241 offset:4096\n"
"v_mfma_f32_16x16x32_bf16 v[88:91], v[204:207], a[60:63], v[88:91]\n"
"ds_read_b64_tr_b16 v[204:205], v239 offset:36992\n"
"v_mfma_f32_16x16x32_bf16 v[88:91], v[200:203], a[56:59], v[88:91]\n"
"s_waitcnt lgkmcnt(9)\n"
"ds_read_b64_tr_b16 v[206:207], v239 offset:38528\n"
"ds_read_b64_tr_b16 v[200:201], v239 offset:24704\n"
"v_mfma_f32_16x16x32_bf16 v[68:71], v[208:211], a[32:35], v[68:71]\n"
"ds_read_b64_tr_b16 v[202:203], v239 offset:26240\n"
"v_mfma_f32_16x16x32_bf16 v[68:71], v[212:215], a[36:39], v[68:71]\n"
"s_waitcnt lgkmcnt(10)\n"
"ds_read_b128 a[24:27], v240 offset:6144\n"
"v_mfma_f32_16x16x32_bf16 v[76:79], v[212:215], a[44:47], v[76:79]\n"
"s_mov_b32 s58, 0x4c00\n"
"buffer_load_dwordx4 v228, s[36:39], s58 offen offset:2048 lds\n"
"v_mfma_f32_16x16x32_bf16 v[76:79], v[208:211], a[40:43], v[76:79]\n"
"v_mfma_f32_16x16x32_bf16 v[84:87], v[208:211], a[48:51], v[84:87]\n"
"s_mov_b32 s58, 0x7000\n"
"buffer_load_dwordx4 v229, s[36:39], s58 offen offset:3072 lds\n"
"v_mfma_f32_16x16x32_bf16 v[84:87], v[212:215], a[52:55], v[84:87]\n"
"ds_read_b128 a[28:31], v241 offset:6144\n"
"v_mfma_f32_16x16x32_bf16 v[92:95], v[212:215], a[60:63], v[92:95]\n"
"ds_read_b64_tr_b16 v[212:213], v236 offset:37120\n"
"v_mfma_f32_16x16x32_bf16 v[92:95], v[208:211], a[56:59], v[92:95]\n"
"s_waitcnt lgkmcnt(9)\n"
"ds_read_b64_tr_b16 v[214:215], v236 offset:38656\n"
"ds_read_b64_tr_b16 v[208:209], v236 offset:24832\n"
"v_mfma_f32_16x16x32_bf16 v[96:99], v[192:195], a[32:35], v[96:99]\n"
"ds_read_b64_tr_b16 v[210:211], v236 offset:26368\n"
"v_mfma_f32_16x16x32_bf16 v[96:99], v[196:199], a[36:39], v[96:99]\n"
"s_waitcnt lgkmcnt(10)\n"
"s_add_u32 m0, s62, 0x1000\n"
"v_mfma_f32_16x16x32_bf16 v[104:107], v[196:199], a[44:47], v[104:107]\n"
"s_mov_b32 s58, 0xa400\n"
"buffer_load_dwordx4 v228, s[36:39], s58 offen offset:0 lds\n"
"v_mfma_f32_16x16x32_bf16 v[104:107], v[192:195], a[40:43], v[104:107]\n"
"v_mfma_f32_16x16x32_bf16 v[112:115], v[192:195], a[48:51], v[112:115]\n"
"s_mov_b32 s58, 0xc800\n"
"buffer_load_dwordx4 v229, s[36:39], s58 offen offset:1024 lds\n"
"v_mfma_f32_16x16x32_bf16 v[112:115], v[196:199], a[52:55], v[112:115]\n"
"v_mfma_f32_16x16x32_bf16 v[120:123], v[196:199], a[60:63], v[120:123]\n"
"ds_read_b64_tr_b16 v[196:197], v237 offset:37120\n"
"v_mfma_f32_16x16x32_bf16 v[120:123], v[192:195], a[56:59], v[120:123]\n"
"s_waitcnt lgkmcnt(7)\n"
"ds_read_b64_tr_b16 v[198:199], v237 offset:38656\n"
"ds_read_b64_tr_b16 v[192:193], v237 offset:24832\n"
"v_mfma_f32_16x16x32_bf16 v[100:103], v[200:203], a[32:35], v[100:103]\n"
"ds_read_b64_tr_b16 v[194:195], v237 offset:26368\n"
"v_mfma_f32_16x16x32_bf16 v[100:103], v[204:207], a[36:39], v[100:103]\n"
"s_waitcnt lgkmcnt(8)\n"
"v_mfma_f32_16x16x32_bf16 v[108:111], v[204:207], a[44:47], v[108:111]\n"
"s_mov_b32 s58, 0xec00\n"
"buffer_load_dwordx4 v228, s[36:39], s58 offen offset:2048 lds\n"
"v_mfma_f32_16x16x32_bf16 v[108:111], v[200:203], a[40:43], v[108:111]\n"
"v_mfma_f32_16x16x32_bf16 v[116:119], v[200:203], a[48:51], v[116:119]\n"
"s_mov_b32 s58, 0x11000\n"
"buffer_load_dwordx4 v229, s[36:39], s58 offen offset:3072 lds\n"
"v_mfma_f32_16x16x32_bf16 v[116:119], v[204:207], a[52:55], v[116:119]\n"
"v_mfma_f32_16x16x32_bf16 v[124:127], v[204:207], a[60:63], v[124:127]\n"
"ds_read_b64_tr_b16 v[204:205], v238 offset:37120\n"
"v_mfma_f32_16x16x32_bf16 v[124:127], v[200:203], a[56:59], v[124:127]\n"
"s_waitcnt lgkmcnt(5)\n"
"ds_read_b64_tr_b16 v[206:207], v238 offset:38656\n"
"ds_read_b64_tr_b16 v[200:201], v238 offset:24832\n"
"v_mfma_f32_16x16x32_bf16 v[128:131], v[208:211], a[32:35], v[128:131]\n"
"s_sub_u32 s28, s28, s6\n"
"ds_read_b64_tr_b16 v[202:203], v238 offset:26368\n"
"v_mfma_f32_16x16x32_bf16 v[128:131], v[212:215], a[36:39], v[128:131]\n"
"s_mul_i32 s7, s28, 27\n"
"v_mfma_f32_16x16x32_bf16 v[136:139], v[212:215], a[44:47], v[136:139]\n"
"s_lshr_b32 s7, s7, 5\n"
"v_mfma_f32_16x16x32_bf16 v[136:139], v[208:211], a[40:43], v[136:139]\n"
"v_mfma_f32_16x16x32_bf16 v[144:147], v[208:211], a[48:51], v[144:147]\n"
"s_max_u32 s7, s7, 0x108\n"
"v_mfma_f32_16x16x32_bf16 v[144:147], v[212:215], a[52:55], v[144:147]\n"
"s_sub_u32 s95, s95, s7\n"
"v_mfma_f32_16x16x32_bf16 v[152:155], v[212:215], a[60:63], v[152:155]\n"
"s_sub_u32 s28, s28, 0x140\n"
"ds_read_b64_tr_b16 v[212:213], v239 offset:37120\n"
"v_mfma_f32_16x16x32_bf16 v[152:155], v[208:211], a[56:59], v[152:155]\n"
"s_waitcnt lgkmcnt(5)\n"
"ds_read_b64_tr_b16 v[214:215], v239 offset:38656\n"
"ds_read_b64_tr_b16 v[208:209], v239 offset:24832\n"
"v_mfma_f32_16x16x32_bf16 v[132:135], v[192:195], a[32:35], v[132:135]\n"
"s_max_i32 s28, s28, 0\n"
"ds_read_b64_tr_b16 v[210:211], v239 offset:26368\n"
"v_mfma_f32_16x16x32_bf16 v[132:135], v[196:199], a[36:39], v[132:135]\n"
"s_lshr_b32 s28, s28, 1\n"
"v_mfma_f32_16x16x32_bf16 v[140:143], v[196:199], a[44:47], v[140:143]\n"
"s_min_u32 s28, s28, 5\n"
"v_mfma_f32_16x16x32_bf16 v[140:143], v[192:195], a[40:43], v[140:143]\n"
"v_mfma_f32_16x16x32_bf16 v[148:151], v[192:195], a[48:51], v[148:151]\n"
"s_sub_u32 s71, s71, s28\n"
"v_mfma_f32_16x16x32_bf16 v[148:151], v[196:199], a[52:55], v[148:151]\n"
"s_mov_b32 s29, s71\n"
"v_mfma_f32_16x16x32_bf16 v[156:159], v[196:199], a[60:63], v[156:159]\n"
"s_waitcnt vmcnt(24) lgkmcnt(0)\n"
"s_barrier\n"
"ds_read_b64_tr_b16 v[196:197], v236 offset:61440\n"
"v_mfma_f32_16x16x32_bf16 v[156:159], v[192:195], a[56:59], v[156:159]\n"
"ds_read_b64_tr_b16 v[198:199], v236 offset:62976\n"
"ds_read_b64_tr_b16 v[192:193], v236 offset:49152\n"
"v_mfma_f32_16x16x32_bf16 v[160:163], v[200:203], a[32:35], v[160:163]\n"
"ds_read_b64_tr_b16 v[194:195], v236 offset:50688\n"
"v_mfma_f32_16x16x32_bf16 v[160:163], v[204:207], a[36:39], v[160:163]\n"
"v_mfma_f32_16x16x32_bf16 v[168:171], v[204:207], a[44:47], v[168:171]\n"
"v_mfma_f32_16x16x32_bf16 v[168:171], v[200:203], a[40:43], v[168:171]\n"
"v_mfma_f32_16x16x32_bf16 v[176:179], v[200:203], a[48:51], v[176:179]\n"
"v_mfma_f32_16x16x32_bf16 v[176:179], v[204:207], a[52:55], v[176:179]\n"
"v_mfma_f32_16x16x32_bf16 v[184:187], v[204:207], a[60:63], v[184:187]\n"
"ds_read_b64_tr_b16 v[204:205], v237 offset:61440\n"
"v_mfma_f32_16x16x32_bf16 v[184:187], v[200:203], a[56:59], v[184:187]\n"
"ds_read_b64_tr_b16 v[206:207], v237 offset:62976\n"
"ds_read_b64_tr_b16 v[200:201], v237 offset:49152\n"
"v_mfma_f32_16x16x32_bf16 v[164:167], v[208:211], a[32:35], v[164:167]\n"
"ds_read_b64_tr_b16 v[202:203], v237 offset:50688\n"
"v_mfma_f32_16x16x32_bf16 v[164:167], v[212:215], a[36:39], v[164:167]\n"
"v_mfma_f32_16x16x32_bf16 v[172:175], v[212:215], a[44:47], v[172:175]\n"
"v_mfma_f32_16x16x32_bf16 v[172:175], v[208:211], a[40:43], v[172:175]\n"
"v_mfma_f32_16x16x32_bf16 v[180:183], v[208:211], a[48:51], v[180:183]\n"
"v_mfma_f32_16x16x32_bf16 v[180:183], v[212:215], a[52:55], v[180:183]\n"
"v_mfma_f32_16x16x32_bf16 v[188:191], v[212:215], a[60:63], v[188:191]\n"
"ds_read_b64_tr_b16 v[212:213], v238 offset:61440\n"
"v_mfma_f32_16x16x32_bf16 v[188:191], v[208:211], a[56:59], v[188:191]\n"
"s_waitcnt lgkmcnt(5)\n"
"ds_read_b64_tr_b16 v[214:215], v238 offset:62976\n"
"ds_read_b64_tr_b16 v[208:209], v238 offset:49152\n"
"v_mfma_f32_16x16x32_bf16 v[0:3], v[192:195], a[0:3], v[0:3]\n"
"ds_read_b64_tr_b16 v[210:211], v238 offset:50688\n"
"v_mfma_f32_16x16x32_bf16 v[0:3], v[196:199], a[4:7], v[0:3]\n"
"s_add_u32 m0, s73, 0xc000\n"
"v_mfma_f32_16x16x32_bf16 v[8:11], v[196:199], a[12:15], v[8:11]\n"
"buffer_load_dwordx4 v222, s[40:43], s59 offen offset:0 lds\n"
"v_mfma_f32_16x16x32_bf16 v[8:11], v[192:195], a[8:11], v[8:11]\n"
"v_mfma_f32_16x16x32_bf16 v[16:19], v[192:195], a[16:19], v[16:19]\n"
"buffer_load_dwordx4 v223, s[40:43], s59 offen offset:1024 lds\n"
"v_mfma_f32_16x16x32_bf16 v[16:19], v[196:199], a[20:23], v[16:19]\n"
"v_mfma_f32_16x16x32_bf16 v[24:27], v[196:199], a[28:31], v[24:27]\n"
"ds_read_b64_tr_b16 v[196:197], v239 offset:61440\n"
"v_mfma_f32_16x16x32_bf16 v[24:27], v[192:195], a[24:27], v[24:27]\n"
"s_waitcnt lgkmcnt(5)\n"
"ds_read_b64_tr_b16 v[198:199], v239 offset:62976\n"
"ds_read_b64_tr_b16 v[192:193], v239 offset:49152\n"
"v_mfma_f32_16x16x32_bf16 v[4:7], v[200:203], a[0:3], v[4:7]\n"
"ds_read_b64_tr_b16 v[194:195], v239 offset:50688\n"
"v_mfma_f32_16x16x32_bf16 v[4:7], v[204:207], a[4:7], v[4:7]\n"
"v_mfma_f32_16x16x32_bf16 v[12:15], v[204:207], a[12:15], v[12:15]\n"
"buffer_load_dwordx4 v224, s[40:43], s59 offen offset:2048 lds\n"
"v_mfma_f32_16x16x32_bf16 v[12:15], v[200:203], a[8:11], v[12:15]\n"
"v_mfma_f32_16x16x32_bf16 v[20:23], v[200:203], a[16:19], v[20:23]\n"
"buffer_load_dwordx4 v225, s[40:43], s59 offen offset:3072 lds\n"
"v_mfma_f32_16x16x32_bf16 v[20:23], v[204:207], a[20:23], v[20:23]\n"
"v_mfma_f32_16x16x32_bf16 v[28:31], v[204:207], a[28:31], v[28:31]\n"
"ds_read_b64_tr_b16 v[204:205], v236 offset:61568\n"
"v_mfma_f32_16x16x32_bf16 v[28:31], v[200:203], a[24:27], v[28:31]\n"
"s_waitcnt lgkmcnt(5)\n"
"ds_read_b64_tr_b16 v[206:207], v236 offset:63104\n"
"ds_read_b64_tr_b16 v[200:201], v236 offset:49280\n"
"v_mfma_f32_16x16x32_bf16 v[32:35], v[208:211], a[0:3], v[32:35]\n"
"s_waitcnt vmcnt(19)\n"
"ds_read_b64_tr_b16 v[202:203], v236 offset:50816\n"
"v_mfma_f32_16x16x32_bf16 v[32:35], v[212:215], a[4:7], v[32:35]\n"
"s_add_u32 m0, s73, 0xd000\n"
"ds_read_b128 a[32:35], v240 offset:32768\n"
"v_mfma_f32_16x16x32_bf16 v[40:43], v[212:215], a[12:15], v[40:43]\n"
"buffer_load_dwordx4 v226, s[40:43], s59 offen offset:0 lds\n"
"v_mfma_f32_16x16x32_bf16 v[40:43], v[208:211], a[8:11], v[40:43]\n"
"v_mfma_f32_16x16x32_bf16 v[48:51], v[208:211], a[16:19], v[48:51]\n"
"buffer_load_dwordx4 v227, s[40:43], s59 offen offset:1024 lds\n"
"v_mfma_f32_16x16x32_bf16 v[48:51], v[212:215], a[20:23], v[48:51]\n"
"ds_read_b128 a[36:39], v241 offset:32768\n"
"v_mfma_f32_16x16x32_bf16 v[56:59], v[212:215], a[28:31], v[56:59]\n"
"s_add_u32 s59, s59, 0xc0000\n"
"ds_read_b64_tr_b16 v[212:213], v237 offset:61568\n"
"v_mfma_f32_16x16x32_bf16 v[56:59], v[208:211], a[24:27], v[56:59]\n"
"s_waitcnt lgkmcnt(7)\n"
"ds_read_b64_tr_b16 v[214:215], v237 offset:63104\n"
"ds_read_b64_tr_b16 v[208:209], v237 offset:49280\n"
"v_mfma_f32_16x16x32_bf16 v[36:39], v[192:195], a[0:3], v[36:39]\n"
"ds_read_b64_tr_b16 v[210:211], v237 offset:50816\n"
"v_mfma_f32_16x16x32_bf16 v[36:39], v[196:199], a[4:7], v[36:39]\n"
"ds_read_b128 a[40:43], v240 offset:34816\n"
"v_mfma_f32_16x16x32_bf16 v[44:47], v[196:199], a[12:15], v[44:47]\n"
"s_add_u32 s60, s60, 0xc0000\n"
"buffer_load_dword v255, v242, s[40:43], s60 offen offset:0\n"
"v_mfma_f32_16x16x32_bf16 v[44:47], v[192:195], a[8:11], v[44:47]\n"
"v_mbcnt_lo_u32_b32 v242, -1, 0\n"
"v_mbcnt_hi_u32_b32 v242, -1, v242\n"
"v_mfma_f32_16x16x32_bf16 v[52:55], v[192:195], a[16:19], v[52:55]\n"
"v_mul_u32_u24 v248, 0x199a, v242\n"
"v_lshrrev_b32 v248, 16, v248\n"
"v_mfma_f32_16x16x32_bf16 v[52:55], v[196:199], a[20:23], v[52:55]\n"
"ds_read_b128 a[44:47], v241 offset:34816\n"
"v_add_u32 v247, 64, v242\n"
"v_mfma_f32_16x16x32_bf16 v[60:63], v[196:199], a[28:31], v[60:63]\n"
"ds_read_b64_tr_b16 v[196:197], v238 offset:61568\n"
"v_mfma_f32_16x16x32_bf16 v[60:63], v[192:195], a[24:27], v[60:63]\n"
"s_waitcnt lgkmcnt(9)\n"
"ds_read_b64_tr_b16 v[198:199], v238 offset:63104\n"
"ds_read_b64_tr_b16 v[192:193], v238 offset:49280\n"
"v_mfma_f32_16x16x32_bf16 v[64:67], v[200:203], a[0:3], v[64:67]\n"
"ds_read_b64_tr_b16 v[194:195], v238 offset:50816\n"
"v_mfma_f32_16x16x32_bf16 v[64:67], v[204:207], a[4:7], v[64:67]\n"
"s_waitcnt lgkmcnt(10)\n"
"s_add_u32 m0, s62, 0x8000\n"
"ds_read_b128 a[48:51], v240 offset:36864\n"
"v_mfma_f32_16x16x32_bf16 v[72:75], v[204:207], a[12:15], v[72:75]\n"
"s_mov_b32 s58, 0x480\n"
"buffer_load_dwordx4 v228, s[36:39], s58 offen offset:0 lds\n"
"v_mfma_f32_16x16x32_bf16 v[72:75], v[200:203], a[8:11], v[72:75]\n"
"v_mfma_f32_16x16x32_bf16 v[80:83], v[200:203], a[16:19], v[80:83]\n"
"s_mov_b32 s58, 0x2880\n"
"buffer_load_dwordx4 v229, s[36:39], s58 offen offset:1024 lds\n"
"v_mfma_f32_16x16x32_bf16 v[80:83], v[204:207], a[20:23], v[80:83]\n"
"ds_read_b128 a[52:55], v241 offset:36864\n"
"v_mfma_f32_16x16x32_bf16 v[88:91], v[204:207], a[28:31], v[88:91]\n"
"ds_read_b64_tr_b16 v[204:205], v239 offset:61568\n"
"v_mfma_f32_16x16x32_bf16 v[88:91], v[200:203], a[24:27], v[88:91]\n"
"s_waitcnt lgkmcnt(9)\n"
"ds_read_b64_tr_b16 v[206:207], v239 offset:63104\n"
"ds_read_b64_tr_b16 v[200:201], v239 offset:49280\n"
"v_mfma_f32_16x16x32_bf16 v[68:71], v[208:211], a[0:3], v[68:71]\n"
"ds_read_b64_tr_b16 v[202:203], v239 offset:50816\n"
"v_mfma_f32_16x16x32_bf16 v[68:71], v[212:215], a[4:7], v[68:71]\n"
"s_waitcnt lgkmcnt(10)\n"
"ds_read_b128 a[56:59], v240 offset:38912\n"
"v_mfma_f32_16x16x32_bf16 v[76:79], v[212:215], a[12:15], v[76:79]\n"
"s_mov_b32 s58, 0x4c80\n"
"buffer_load_dwordx4 v228, s[36:39], s58 offen offset:2048 lds\n"
"v_mfma_f32_16x16x32_bf16 v[76:79], v[208:211], a[8:11], v[76:79]\n"
"v_mfma_f32_16x16x32_bf16 v[84:87], v[208:211], a[16:19], v[84:87]\n"
"s_mov_b32 s58, 0x7080\n"
"buffer_load_dwordx4 v229, s[36:39], s58 offen offset:3072 lds\n"
"v_mfma_f32_16x16x32_bf16 v[84:87], v[212:215], a[20:23], v[84:87]\n"
"ds_read_b128 a[60:63], v241 offset:38912\n"
"v_mfma_f32_16x16x32_bf16 v[92:95], v[212:215], a[28:31], v[92:95]\n"
"ds_read_b64_tr_b16 v[212:213], v236 offset:61696\n"
"v_mfma_f32_16x16x32_bf16 v[92:95], v[208:211], a[24:27], v[92:95]\n"
"s_waitcnt lgkmcnt(9)\n"
"ds_read_b64_tr_b16 v[214:215], v236 offset:63232\n"
"ds_read_b64_tr_b16 v[208:209], v236 offset:49408\n"
"Ltp3%=:\n"
"s_cbranch_execz Ltrp%=\n"
"v_mfma_f32_16x16x32_bf16 v[96:99], v[192:195], a[0:3], v[96:99]\n"
"ds_read_b64_tr_b16 v[210:211], v236 offset:50944\n"
"v_mfma_f32_16x16x32_bf16 v[96:99], v[196:199], a[4:7], v[96:99]\n"
"s_waitcnt lgkmcnt(10)\n"
"s_add_u32 m0, s62, 0x9000\n"
"v_mfma_f32_16x16x32_bf16 v[104:107], v[196:199], a[12:15], v[104:107]\n"
"s_mov_b32 s58, 0xa480\n"
"buffer_load_dwordx4 v228, s[36:39], s58 offen offset:0 lds\n"
"v_mfma_f32_16x16x32_bf16 v[104:107], v[192:195], a[8:11], v[104:107]\n"
"v_mfma_f32_16x16x32_bf16 v[112:115], v[192:195], a[16:19], v[112:115]\n"
"s_mov_b32 s58, 0xc880\n"
"buffer_load_dwordx4 v229, s[36:39], s58 offen offset:1024 lds\n"
"v_mfma_f32_16x16x32_bf16 v[112:115], v[196:199], a[20:23], v[112:115]\n"
"v_mfma_f32_16x16x32_bf16 v[120:123], v[196:199], a[28:31], v[120:123]\n"
"ds_read_b64_tr_b16 v[196:197], v237 offset:61696\n"
"v_mfma_f32_16x16x32_bf16 v[120:123], v[192:195], a[24:27], v[120:123]\n"
"s_waitcnt lgkmcnt(7)\n"
"ds_read_b64_tr_b16 v[198:199], v237 offset:63232\n"
"ds_read_b64_tr_b16 v[192:193], v237 offset:49408\n"
"v_mfma_f32_16x16x32_bf16 v[100:103], v[200:203], a[0:3], v[100:103]\n"
"ds_read_b64_tr_b16 v[194:195], v237 offset:50944\n"
"v_mfma_f32_16x16x32_bf16 v[100:103], v[204:207], a[4:7], v[100:103]\n"
"s_waitcnt lgkmcnt(8)\n"
"v_mfma_f32_16x16x32_bf16 v[108:111], v[204:207], a[12:15], v[108:111]\n"
"s_mov_b32 s58, 0xec80\n"
"buffer_load_dwordx4 v228, s[36:39], s58 offen offset:2048 lds\n"
"v_mfma_f32_16x16x32_bf16 v[108:111], v[200:203], a[8:11], v[108:111]\n"
"v_mfma_f32_16x16x32_bf16 v[116:119], v[200:203], a[16:19], v[116:119]\n"
"s_mov_b32 s58, 0x11080\n"
"buffer_load_dwordx4 v229, s[36:39], s58 offen offset:3072 lds\n"
"v_mfma_f32_16x16x32_bf16 v[116:119], v[204:207], a[20:23], v[116:119]\n"
"v_mfma_f32_16x16x32_bf16 v[124:127], v[204:207], a[28:31], v[124:127]\n"
"ds_read_b64_tr_b16 v[204:205], v238 offset:61696\n"
"v_mfma_f32_16x16x32_bf16 v[124:127], v[200:203], a[24:27], v[124:127]\n"
"s_waitcnt lgkmcnt(5)\n"
"ds_read_b64_tr_b16 v[206:207], v238 offset:63232\n"
"ds_read_b64_tr_b16 v[200:201], v238 offset:49408\n"
"v_mfma_f32_16x16x32_bf16 v[128:131], v[208:211], a[0:3], v[128:131]\n"
"ds_read_b64_tr_b16 v[202:203], v238 offset:50944\n"
"v_mfma_f32_16x16x32_bf16 v[128:131], v[212:215], a[4:7], v[128:131]\n"
"v_mfma_f32_16x16x32_bf16 v[136:139], v[212:215], a[12:15], v[136:139]\n"
"v_mfma_f32_16x16x32_bf16 v[136:139], v[208:211], a[8:11], v[136:139]\n"
"v_mfma_f32_16x16x32_bf16 v[144:147], v[208:211], a[16:19], v[144:147]\n"
"v_mfma_f32_16x16x32_bf16 v[144:147], v[212:215], a[20:23], v[144:147]\n"
"v_mfma_f32_16x16x32_bf16 v[152:155], v[212:215], a[28:31], v[152:155]\n"
"ds_read_b64_tr_b16 v[212:213], v239 offset:61696\n"
"v_mfma_f32_16x16x32_bf16 v[152:155], v[208:211], a[24:27], v[152:155]\n"
"s_waitcnt lgkmcnt(5)\n"
"ds_read_b64_tr_b16 v[214:215], v239 offset:63232\n"
"ds_read_b64_tr_b16 v[208:209], v239 offset:49408\n"
"v_mfma_f32_16x16x32_bf16 v[132:135], v[192:195], a[0:3], v[132:135]\n"
"ds_read_b64_tr_b16 v[210:211], v239 offset:50944\n"
"v_mfma_f32_16x16x32_bf16 v[132:135], v[196:199], a[4:7], v[132:135]\n"
"v_mfma_f32_16x16x32_bf16 v[140:143], v[196:199], a[12:15], v[140:143]\n"
"v_mfma_f32_16x16x32_bf16 v[140:143], v[192:195], a[8:11], v[140:143]\n"
"v_mfma_f32_16x16x32_bf16 v[148:151], v[192:195], a[16:19], v[148:151]\n"
"v_mfma_f32_16x16x32_bf16 v[148:151], v[196:199], a[20:23], v[148:151]\n"
"v_mfma_f32_16x16x32_bf16 v[156:159], v[196:199], a[28:31], v[156:159]\n"
"s_waitcnt vmcnt(24) lgkmcnt(0)\n"
"s_barrier\n"
"ds_read_b64_tr_b16 v[196:197], v236 offset:12288\n"
"v_mfma_f32_16x16x32_bf16 v[156:159], v[192:195], a[24:27], v[156:159]\n"
"ds_read_b64_tr_b16 v[198:199], v236 offset:13824\n"
"ds_read_b64_tr_b16 v[192:193], v236 offset:0\n"
"v_mfma_f32_16x16x32_bf16 v[160:163], v[200:203], a[0:3], v[160:163]\n"
"ds_read_b64_tr_b16 v[194:195], v236 offset:1536\n"
"v_mfma_f32_16x16x32_bf16 v[160:163], v[204:207], a[4:7], v[160:163]\n"
"v_mfma_f32_16x16x32_bf16 v[168:171], v[204:207], a[12:15], v[168:171]\n"
"v_mfma_f32_16x16x32_bf16 v[168:171], v[200:203], a[8:11], v[168:171]\n"
"v_mfma_f32_16x16x32_bf16 v[176:179], v[200:203], a[16:19], v[176:179]\n"
"v_mfma_f32_16x16x32_bf16 v[176:179], v[204:207], a[20:23], v[176:179]\n"
"v_mfma_f32_16x16x32_bf16 v[184:187], v[204:207], a[28:31], v[184:187]\n"
"ds_read_b64_tr_b16 v[204:205], v237 offset:12288\n"
"v_mfma_f32_16x16x32_bf16 v[184:187], v[200:203], a[24:27], v[184:187]\n"
"ds_read_b64_tr_b16 v[206:207], v237 offset:13824\n"
"ds_read_b64_tr_b16 v[200:201], v237 offset:0\n"
"v_mfma_f32_16x16x32_bf16 v[164:167], v[208:211], a[0:3], v[164:167]\n"
"ds_read_b64_tr_b16 v[202:203], v237 offset:1536\n"
"v_mfma_f32_16x16x32_bf16 v[164:167], v[212:215], a[4:7], v[164:167]\n"
"v_mfma_f32_16x16x32_bf16 v[172:175], v[212:215], a[12:15], v[172:175]\n"
"v_mfma_f32_16x16x32_bf16 v[172:175], v[208:211], a[8:11], v[172:175]\n"
"v_mfma_f32_16x16x32_bf16 v[180:183], v[208:211], a[16:19], v[180:183]\n"
"v_mfma_f32_16x16x32_bf16 v[180:183], v[212:215], a[20:23], v[180:183]\n"
"v_mfma_f32_16x16x32_bf16 v[188:191], v[212:215], a[28:31], v[188:191]\n"
"ds_read_b64_tr_b16 v[212:213], v238 offset:12288\n"
"v_mfma_f32_16x16x32_bf16 v[188:191], v[208:211], a[24:27], v[188:191]\n"
"s_waitcnt lgkmcnt(5)\n"
"ds_read_b64_tr_b16 v[214:215], v238 offset:13824\n"
"ds_read_b64_tr_b16 v[208:209], v238 offset:0\n"
"v_mfma_f32_16x16x32_bf16 v[0:3], v[192:195], a[32:35], v[0:3]\n"
"ds_read_b64_tr_b16 v[210:211], v238 offset:1536\n"
"v_mfma_f32_16x16x32_bf16 v[0:3], v[196:199], a[36:39], v[0:3]\n"
"s_add_u32 m0, s73, 0x12000\n"
"v_mfma_f32_16x16x32_bf16 v[8:11], v[196:199], a[44:47], v[8:11]\n"
"buffer_load_dwordx4 v222, s[40:43], s59 offen offset:0 lds\n"
"v_mfma_f32_16x16x32_bf16 v[8:11], v[192:195], a[40:43], v[8:11]\n"
"v_mfma_f32_16x16x32_bf16 v[16:19], v[192:195], a[48:51], v[16:19]\n"
"buffer_load_dwordx4 v223, s[40:43], s59 offen offset:1024 lds\n"
"v_mfma_f32_16x16x32_bf16 v[16:19], v[196:199], a[52:55], v[16:19]\n"
"v_mfma_f32_16x16x32_bf16 v[24:27], v[196:199], a[60:63], v[24:27]\n"
"ds_read_b64_tr_b16 v[196:197], v239 offset:12288\n"
"v_mfma_f32_16x16x32_bf16 v[24:27], v[192:195], a[56:59], v[24:27]\n"
"s_waitcnt lgkmcnt(5)\n"
"ds_read_b64_tr_b16 v[198:199], v239 offset:13824\n"
"ds_read_b64_tr_b16 v[192:193], v239 offset:0\n"
"v_mfma_f32_16x16x32_bf16 v[4:7], v[200:203], a[32:35], v[4:7]\n"
"ds_read_b64_tr_b16 v[194:195], v239 offset:1536\n"
"v_mfma_f32_16x16x32_bf16 v[4:7], v[204:207], a[36:39], v[4:7]\n"
"v_mfma_f32_16x16x32_bf16 v[12:15], v[204:207], a[44:47], v[12:15]\n"
"buffer_load_dwordx4 v224, s[40:43], s59 offen offset:2048 lds\n"
"v_mfma_f32_16x16x32_bf16 v[12:15], v[200:203], a[40:43], v[12:15]\n"
"v_mfma_f32_16x16x32_bf16 v[20:23], v[200:203], a[48:51], v[20:23]\n"
"buffer_load_dwordx4 v225, s[40:43], s59 offen offset:3072 lds\n"
"v_mfma_f32_16x16x32_bf16 v[20:23], v[204:207], a[52:55], v[20:23]\n"
"v_mfma_f32_16x16x32_bf16 v[28:31], v[204:207], a[60:63], v[28:31]\n"
"ds_read_b64_tr_b16 v[204:205], v236 offset:12416\n"
"v_mfma_f32_16x16x32_bf16 v[28:31], v[200:203], a[56:59], v[28:31]\n"
"s_waitcnt lgkmcnt(5)\n"
"ds_read_b64_tr_b16 v[206:207], v236 offset:13952\n"
"ds_read_b64_tr_b16 v[200:201], v236 offset:128\n"
"v_mfma_f32_16x16x32_bf16 v[32:35], v[208:211], a[32:35], v[32:35]\n"
"s_waitcnt vmcnt(19)\n"
"ds_read_b64_tr_b16 v[202:203], v236 offset:1664\n"
"v_mfma_f32_16x16x32_bf16 v[32:35], v[212:215], a[36:39], v[32:35]\n"
"s_add_u32 m0, s73, 0x13000\n"
"ds_read_b128 a[0:3], v240 offset:0\n"
"v_mfma_f32_16x16x32_bf16 v[40:43], v[212:215], a[44:47], v[40:43]\n"
"buffer_load_dwordx4 v226, s[40:43], s59 offen offset:0 lds\n"
"v_mfma_f32_16x16x32_bf16 v[40:43], v[208:211], a[40:43], v[40:43]\n"
"v_mfma_f32_16x16x32_bf16 v[48:51], v[208:211], a[48:51], v[48:51]\n"
"buffer_load_dwordx4 v227, s[40:43], s59 offen offset:1024 lds\n"
"v_mfma_f32_16x16x32_bf16 v[48:51], v[212:215], a[52:55], v[48:51]\n"
"ds_read_b128 a[4:7], v241 offset:0\n"
"v_mfma_f32_16x16x32_bf16 v[56:59], v[212:215], a[60:63], v[56:59]\n"
"s_add_u32 s59, s59, 0xc0000\n"
"ds_read_b64_tr_b16 v[212:213], v237 offset:12416\n"
"v_mfma_f32_16x16x32_bf16 v[56:59], v[208:211], a[56:59], v[56:59]\n"
"s_waitcnt lgkmcnt(7)\n"
"ds_read_b64_tr_b16 v[214:215], v237 offset:13952\n"
"ds_read_b64_tr_b16 v[208:209], v237 offset:128\n"
"v_mfma_f32_16x16x32_bf16 v[36:39], v[192:195], a[32:35], v[36:39]\n"
"ds_read_b64_tr_b16 v[210:211], v237 offset:1664\n"
"v_mfma_f32_16x16x32_bf16 v[36:39], v[196:199], a[36:39], v[36:39]\n"
"s_add_u32 m0, s73, 0\n"
"ds_read_b128 a[8:11], v240 offset:2048\n"
"v_mfma_f32_16x16x32_bf16 v[44:47], v[196:199], a[44:47], v[44:47]\n"
"buffer_load_dwordx4 v216, s[44:47], s65 offen offset:0 nt lds\n"
"v_mfma_f32_16x16x32_bf16 v[44:47], v[192:195], a[40:43], v[44:47]\n"
"s_add_u32 m0, s73, 0\n"
"v_mfma_f32_16x16x32_bf16 v[52:55], v[192:195], a[48:51], v[52:55]\n"
"buffer_load_dwordx4 v217, s[44:47], s65 offen offset:1024 nt lds\n"
"v_mfma_f32_16x16x32_bf16 v[52:55], v[196:199], a[52:55], v[52:55]\n"
"ds_read_b128 a[12:15], v241 offset:2048\n"
"v_mfma_f32_16x16x32_bf16 v[60:63], v[196:199], a[60:63], v[60:63]\n"
"ds_read_b64_tr_b16 v[196:197], v238 offset:12416\n"
"v_mfma_f32_16x16x32_bf16 v[60:63], v[192:195], a[56:59], v[60:63]\n"
"s_waitcnt lgkmcnt(9)\n"
"ds_read_b64_tr_b16 v[198:199], v238 offset:13952\n"
"ds_read_b64_tr_b16 v[192:193], v238 offset:128\n"
"v_mfma_f32_16x16x32_bf16 v[64:67], v[200:203], a[32:35], v[64:67]\n"
"ds_read_b64_tr_b16 v[194:195], v238 offset:1664\n"
"v_mfma_f32_16x16x32_bf16 v[64:67], v[204:207], a[36:39], v[64:67]\n"
"s_add_u32 m0, s73, 0\n"
"ds_read_b128 a[16:19], v240 offset:4096\n"
"v_mfma_f32_16x16x32_bf16 v[72:75], v[204:207], a[44:47], v[72:75]\n"
"buffer_load_dwordx4 v218, s[44:47], s65 offen offset:2048 nt lds\n"
"v_mfma_f32_16x16x32_bf16 v[72:75], v[200:203], a[40:43], v[72:75]\n"
"s_add_u32 m0, s73, 0\n"
"v_mfma_f32_16x16x32_bf16 v[80:83], v[200:203], a[48:51], v[80:83]\n"
"buffer_load_dwordx4 v219, s[44:47], s65 offen offset:3072 nt lds\n"
"v_mfma_f32_16x16x32_bf16 v[80:83], v[204:207], a[52:55], v[80:83]\n"
"ds_read_b128 a[20:23], v241 offset:4096\n"
"v_mfma_f32_16x16x32_bf16 v[88:91], v[204:207], a[60:63], v[88:91]\n"
"ds_read_b64_tr_b16 v[204:205], v239 offset:12416\n"
"v_mfma_f32_16x16x32_bf16 v[88:91], v[200:203], a[56:59], v[88:91]\n"
"s_waitcnt lgkmcnt(9)\n"
"ds_read_b64_tr_b16 v[206:207], v239 offset:13952\n"
"ds_read_b64_tr_b16 v[200:201], v239 offset:128\n"
"v_mfma_f32_16x16x32_bf16 v[68:71], v[208:211], a[32:35], v[68:71]\n"
"ds_read_b64_tr_b16 v[202:203], v239 offset:1664\n"
"v_mfma_f32_16x16x32_bf16 v[68:71], v[212:215], a[36:39], v[68:71]\n"
"s_add_u32 m0, s73, 0x1000\n"
"ds_read_b128 a[24:27], v240 offset:6144\n"
"v_mfma_f32_16x16x32_bf16 v[76:79], v[212:215], a[44:47], v[76:79]\n"
"buffer_load_dwordx4 v220, s[44:47], s65 offen offset:0 nt lds\n"
"v_mfma_f32_16x16x32_bf16 v[76:79], v[208:211], a[40:43], v[76:79]\n"
"s_add_u32 m0, s73, 0x1000\n"
"v_mfma_f32_16x16x32_bf16 v[84:87], v[208:211], a[48:51], v[84:87]\n"
"buffer_load_dwordx4 v221, s[44:47], s65 offen offset:1024 nt lds\n"
"v_mfma_f32_16x16x32_bf16 v[84:87], v[212:215], a[52:55], v[84:87]\n"
"ds_read_b128 a[28:31], v241 offset:6144\n"
"v_mfma_f32_16x16x32_bf16 v[92:95], v[212:215], a[60:63], v[92:95]\n"
"ds_read_b64_tr_b16 v[212:213], v236 offset:12544\n"
"v_mfma_f32_16x16x32_bf16 v[92:95], v[208:211], a[56:59], v[92:95]\n"
"s_waitcnt lgkmcnt(9)\n"
"ds_read_b64_tr_b16 v[214:215], v236 offset:14080\n"
"ds_read_b64_tr_b16 v[208:209], v236 offset:256\n"
"v_mfma_f32_16x16x32_bf16 v[96:99], v[192:195], a[32:35], v[96:99]\n"
"ds_read_b64_tr_b16 v[210:211], v236 offset:1792\n"
"v_mfma_f32_16x16x32_bf16 v[96:99], v[196:199], a[36:39], v[96:99]\n"
"v_mfma_f32_16x16x32_bf16 v[104:107], v[196:199], a[44:47], v[104:107]\n"
"v_mfma_f32_16x16x32_bf16 v[104:107], v[192:195], a[40:43], v[104:107]\n"
"v_mfma_f32_16x16x32_bf16 v[112:115], v[192:195], a[48:51], v[112:115]\n"
"v_mfma_f32_16x16x32_bf16 v[112:115], v[196:199], a[52:55], v[112:115]\n"
"v_mfma_f32_16x16x32_bf16 v[120:123], v[196:199], a[60:63], v[120:123]\n"
"ds_read_b64_tr_b16 v[196:197], v237 offset:12544\n"
"v_mfma_f32_16x16x32_bf16 v[120:123], v[192:195], a[56:59], v[120:123]\n"
"s_waitcnt lgkmcnt(7)\n"
"ds_read_b64_tr_b16 v[198:199], v237 offset:14080\n"
"ds_read_b64_tr_b16 v[192:193], v237 offset:256\n"
"v_mfma_f32_16x16x32_bf16 v[100:103], v[200:203], a[32:35], v[100:103]\n"
"ds_read_b64_tr_b16 v[194:195], v237 offset:1792\n"
"v_mfma_f32_16x16x32_bf16 v[100:103], v[204:207], a[36:39], v[100:103]\n"
"v_mfma_f32_16x16x32_bf16 v[108:111], v[204:207], a[44:47], v[108:111]\n"
"v_mfma_f32_16x16x32_bf16 v[108:111], v[200:203], a[40:43], v[108:111]\n"
"v_mfma_f32_16x16x32_bf16 v[116:119], v[200:203], a[48:51], v[116:119]\n"
"v_mfma_f32_16x16x32_bf16 v[116:119], v[204:207], a[52:55], v[116:119]\n"
"v_mfma_f32_16x16x32_bf16 v[124:127], v[204:207], a[60:63], v[124:127]\n"
"ds_read_b64_tr_b16 v[204:205], v238 offset:12544\n"
"v_mfma_f32_16x16x32_bf16 v[124:127], v[200:203], a[56:59], v[124:127]\n"
"s_waitcnt lgkmcnt(5)\n"
"ds_read_b64_tr_b16 v[206:207], v238 offset:14080\n"
"ds_read_b64_tr_b16 v[200:201], v238 offset:256\n"
"v_mfma_f32_16x16x32_bf16 v[128:131], v[208:211], a[32:35], v[128:131]\n"
"ds_read_b64_tr_b16 v[202:203], v238 offset:1792\n"
"v_mfma_f32_16x16x32_bf16 v[128:131], v[212:215], a[36:39], v[128:131]\n"
"v_mfma_f32_16x16x32_bf16 v[136:139], v[212:215], a[44:47], v[136:139]\n"
"v_mfma_f32_16x16x32_bf16 v[136:139], v[208:211], a[40:43], v[136:139]\n"
"v_mfma_f32_16x16x32_bf16 v[144:147], v[208:211], a[48:51], v[144:147]\n"
"v_mfma_f32_16x16x32_bf16 v[144:147], v[212:215], a[52:55], v[144:147]\n"
"v_mfma_f32_16x16x32_bf16 v[152:155], v[212:215], a[60:63], v[152:155]\n"
"ds_read_b64_tr_b16 v[212:213], v239 offset:12544\n"
"v_mfma_f32_16x16x32_bf16 v[152:155], v[208:211], a[56:59], v[152:155]\n"
"s_waitcnt lgkmcnt(5)\n"
"ds_read_b64_tr_b16 v[214:215], v239 offset:14080\n"
"ds_read_b64_tr_b16 v[208:209], v239 offset:256\n"
"v_mfma_f32_16x16x32_bf16 v[132:135], v[192:195], a[32:35], v[132:135]\n"
"ds_read_b64_tr_b16 v[210:211], v239 offset:1792\n"
"v_mfma_f32_16x16x32_bf16 v[132:135], v[196:199], a[36:39], v[132:135]\n"
"v_mfma_f32_16x16x32_bf16 v[140:143], v[196:199], a[44:47], v[140:143]\n"
"v_mfma_f32_16x16x32_bf16 v[140:143], v[192:195], a[40:43], v[140:143]\n"
"v_mfma_f32_16x16x32_bf16 v[148:151], v[192:195], a[48:51], v[148:151]\n"
"v_mfma_f32_16x16x32_bf16 v[148:151], v[196:199], a[52:55], v[148:151]\n"
"v_mfma_f32_16x16x32_bf16 v[156:159], v[196:199], a[60:63], v[156:159]\n"
"s_waitcnt vmcnt(21) lgkmcnt(0)\n"
"s_barrier\n"
"ds_read_b64_tr_b16 v[196:197], v236 offset:36864\n"
"v_mfma_f32_16x16x32_bf16 v[156:159], v[192:195], a[56:59], v[156:159]\n"
"ds_read_b64_tr_b16 v[198:199], v236 offset:38400\n"
"ds_read_b64_tr_b16 v[192:193], v236 offset:24576\n"
"v_mfma_f32_16x16x32_bf16 v[160:163], v[200:203], a[32:35], v[160:163]\n"
"ds_read_b64_tr_b16 v[194:195], v236 offset:26112\n"
"v_mfma_f32_16x16x32_bf16 v[160:163], v[204:207], a[36:39], v[160:163]\n"
"v_mfma_f32_16x16x32_bf16 v[168:171], v[204:207], a[44:47], v[168:171]\n"
"v_mfma_f32_16x16x32_bf16 v[168:171], v[200:203], a[40:43], v[168:171]\n"
"v_mfma_f32_16x16x32_bf16 v[176:179], v[200:203], a[48:51], v[176:179]\n"
"v_mfma_f32_16x16x32_bf16 v[176:179], v[204:207], a[52:55], v[176:179]\n"
"v_mfma_f32_16x16x32_bf16 v[184:187], v[204:207], a[60:63], v[184:187]\n"
"ds_read_b64_tr_b16 v[204:205], v237 offset:36864\n"
"v_mfma_f32_16x16x32_bf16 v[184:187], v[200:203], a[56:59], v[184:187]\n"
"ds_read_b64_tr_b16 v[206:207], v237 offset:38400\n"
"ds_read_b64_tr_b16 v[200:201], v237 offset:24576\n"
"v_mfma_f32_16x16x32_bf16 v[164:167], v[208:211], a[32:35], v[164:167]\n"
"ds_read_b64_tr_b16 v[202:203], v237 offset:26112\n"
"v_mfma_f32_16x16x32_bf16 v[164:167], v[212:215], a[36:39], v[164:167]\n"
"v_mfma_f32_16x16x32_bf16 v[172:175], v[212:215], a[44:47], v[172:175]\n"
"v_mfma_f32_16x16x32_bf16 v[172:175], v[208:211], a[40:43], v[172:175]\n"
"v_mfma_f32_16x16x32_bf16 v[180:183], v[208:211], a[48:51], v[180:183]\n"
"v_mfma_f32_16x16x32_bf16 v[180:183], v[212:215], a[52:55], v[180:183]\n"
"v_mfma_f32_16x16x32_bf16 v[188:191], v[212:215], a[60:63], v[188:191]\n"
"ds_read_b64_tr_b16 v[212:213], v238 offset:36864\n"
"v_mfma_f32_16x16x32_bf16 v[188:191], v[208:211], a[56:59], v[188:191]\n"
"s_waitcnt lgkmcnt(5)\n"
"ds_read_b64_tr_b16 v[214:215], v238 offset:38400\n"
"ds_read_b64_tr_b16 v[208:209], v238 offset:24576\n"
"v_mfma_f32_16x16x32_bf16 v[0:3], v[192:195], a[0:3], v[0:3]\n"
"ds_read_b64_tr_b16 v[210:211], v238 offset:26112\n"
"v_mfma_f32_16x16x32_bf16 v[0:3], v[196:199], a[4:7], v[0:3]\n"
"s_add_u32 m0, s73, 0x6000\n"
"v_mfma_f32_16x16x32_bf16 v[8:11], v[196:199], a[12:15], v[8:11]\n"
"buffer_load_dwordx4 v216, s[44:47], s64 offen offset:0 nt lds\n"
"v_mfma_f32_16x16x32_bf16 v[8:11], v[192:195], a[8:11], v[8:11]\n"
"v_mfma_f32_16x16x32_bf16 v[16:19], v[192:195], a[16:19], v[16:19]\n"
"s_mov_b32 s80, 0x9e3779b1\n"
"v_mul_lo_u32 v253, v245, s80\n"
"v_add_u32 v253, 0x7f4a7c15, v253\n"
"v_mfma_f32_16x16x32_bf16 v[16:19], v[196:199], a[20:23], v[16:19]\n"
"v_mfma_f32_16x16x32_bf16 v[24:27], v[196:199], a[28:31], v[24:27]\n"
"ds_read_b64_tr_b16 v[196:197], v239 offset:36864\n"
"v_mfma_f32_16x16x32_bf16 v[24:27], v[192:195], a[24:27], v[24:27]\n"
"s_waitcnt lgkmcnt(5)\n"
"ds_read_b64_tr_b16 v[198:199], v239 offset:38400\n"
"ds_read_b64_tr_b16 v[192:193], v239 offset:24576\n"
"v_mfma_f32_16x16x32_bf16 v[4:7], v[200:203], a[0:3], v[4:7]\n"
"ds_read_b64_tr_b16 v[194:195], v239 offset:26112\n"
"v_mfma_f32_16x16x32_bf16 v[4:7], v[204:207], a[4:7], v[4:7]\n"
"v_mfma_f32_16x16x32_bf16 v[12:15], v[204:207], a[12:15], v[12:15]\n"
"v_mfma_f32_16x16x32_bf16 v[12:15], v[200:203], a[8:11], v[12:15]\n"
"v_mfma_f32_16x16x32_bf16 v[20:23], v[200:203], a[16:19], v[20:23]\n"
"buffer_load_dwordx4 v217, s[44:47], s64 offen offset:1024 nt lds\n"
"v_mfma_f32_16x16x32_bf16 v[20:23], v[204:207], a[20:23], v[20:23]\n"
"v_mfma_f32_16x16x32_bf16 v[28:31], v[204:207], a[28:31], v[28:31]\n"
"ds_read_b64_tr_b16 v[204:205], v236 offset:36992\n"
"v_mfma_f32_16x16x32_bf16 v[28:31], v[200:203], a[24:27], v[28:31]\n"
"s_waitcnt lgkmcnt(5)\n"
"ds_read_b64_tr_b16 v[206:207], v236 offset:38528\n"
"ds_read_b64_tr_b16 v[200:201], v236 offset:24704\n"
"v_mfma_f32_16x16x32_bf16 v[32:35], v[208:211], a[0:3], v[32:35]\n"
"s_waitcnt vmcnt(14)\n"
"ds_read_b64_tr_b16 v[202:203], v236 offset:26240\n"
"v_mfma_f32_16x16x32_bf16 v[32:35], v[212:215], a[4:7], v[32:35]\n"
"s_mov_b64 s[40:41], s[16:17]\n"
"ds_read_b128 a[32:35], v240 offset:32768\n"
"v_mfma_f32_16x16x32_bf16 v[40:43], v[212:215], a[12:15], v[40:43]\n"
"s_mov_b32 s42, s27\n"
"v_mfma_f32_16x16x32_bf16 v[40:43], v[208:211], a[8:11], v[40:43]\n"
"v_mfma_f32_16x16x32_bf16 v[48:51], v[208:211], a[16:19], v[48:51]\n"
"s_mul_i32 s87, %[w], 0x1400\n"
"v_mfma_f32_16x16x32_bf16 v[48:51], v[212:215], a[20:23], v[48:51]\n"
"s_and_b32 s81, %[w], 1\n"
"ds_read_b128 a[36:39], v241 offset:32768\n"
"v_mfma_f32_16x16x32_bf16 v[56:59], v[212:215], a[28:31], v[56:59]\n"
"s_mul_i32 s81, s81, 0xa000\n"
"ds_read_b64_tr_b16 v[212:213], v237 offset:36992\n"
"v_mfma_f32_16x16x32_bf16 v[56:59], v[208:211], a[24:27], v[56:59]\n"
"s_waitcnt lgkmcnt(7)\n"
"ds_read_b64_tr_b16 v[214:215], v237 offset:38528\n"
"ds_read_b64_tr_b16 v[208:209], v237 offset:24704\n"
"v_mfma_f32_16x16x32_bf16 v[36:39], v[192:195], a[0:3], v[36:39]\n"
"s_lshr_b32 s62, %[w], 1\n"
"ds_read_b64_tr_b16 v[210:211], v237 offset:26240\n"
"v_mfma_f32_16x16x32_bf16 v[36:39], v[196:199], a[4:7], v[36:39]\n"
"s_mul_i32 s62, s62, 0xa0\n"
"ds_read_b128 a[40:43], v240 offset:34816\n"
"v_mfma_f32_16x16x32_bf16 v[44:47], v[196:199], a[12:15], v[44:47]\n"
"s_add_u32 s81, s81, s62\n"
"buffer_load_dwordx4 v218, s[44:47], s64 offen offset:2048 nt lds\n"
"v_mfma_f32_16x16x32_bf16 v[44:47], v[192:195], a[8:11], v[44:47]\n"
"v_lshl_add_u32 v244, v242, 4, s81\n"
"v_mfma_f32_16x16x32_bf16 v[52:55], v[192:195], a[16:19], v[52:55]\n"
"s_mov_b32 s62, 0x460\n"
"v_mad_u32_u24 v222, v248, s62, v244\n"
"v_mul_u32_u24 v248, 0x199a, v247\n"
"v_mfma_f32_16x16x32_bf16 v[52:55], v[196:199], a[20:23], v[52:55]\n"
"ds_read_b128 a[44:47], v241 offset:34816\n"
"v_lshrrev_b32 v248, 16, v248\n"
"v_mfma_f32_16x16x32_bf16 v[60:63], v[196:199], a[28:31], v[60:63]\n"
"ds_read_b64_tr_b16 v[196:197], v238 offset:36992\n"
"v_mad_u32_u24 v223, v248, s62, v244\n"
"v_mfma_f32_16x16x32_bf16 v[60:63], v[192:195], a[24:27], v[60:63]\n"
"s_waitcnt lgkmcnt(9)\n"
"ds_read_b64_tr_b16 v[198:199], v238 offset:38528\n"
"ds_read_b64_tr_b16 v[192:193], v238 offset:24704\n"
"v_mfma_f32_16x16x32_bf16 v[64:67], v[200:203], a[0:3], v[64:67]\n"
"ds_read_b64_tr_b16 v[194:195], v238 offset:26240\n"
"v_add_u32 v247, 0x80, v242\n"
"v_mfma_f32_16x16x32_bf16 v[64:67], v[204:207], a[4:7], v[64:67]\n"
"ds_read_b128 a[48:51], v240 offset:36864\n"
"v_mul_u32_u24 v248, 0x199a, v247\n"
"v_mfma_f32_16x16x32_bf16 v[72:75], v[204:207], a[12:15], v[72:75]\n"
"v_lshrrev_b32 v248, 16, v248\n"
"v_mad_u32_u24 v224, v248, s62, v244\n"
"v_mfma_f32_16x16x32_bf16 v[72:75], v[200:203], a[8:11], v[72:75]\n"
"v_add_u32 v247, 0xc0, v242\n"
"v_mul_u32_u24 v248, 0x199a, v247\n"
"v_mfma_f32_16x16x32_bf16 v[80:83], v[200:203], a[16:19], v[80:83]\n"
"buffer_load_dwordx4 v219, s[44:47], s64 offen offset:3072 nt lds\n"
"v_mfma_f32_16x16x32_bf16 v[80:83], v[204:207], a[20:23], v[80:83]\n"
"ds_read_b128 a[52:55], v241 offset:36864\n"
"v_lshrrev_b32 v248, 16, v248\n"
"v_mfma_f32_16x16x32_bf16 v[88:91], v[204:207], a[28:31], v[88:91]\n"
"ds_read_b64_tr_b16 v[204:205], v239 offset:36992\n"
"v_mad_u32_u24 v225, v248, s62, v244\n"
"v_mfma_f32_16x16x32_bf16 v[88:91], v[200:203], a[24:27], v[88:91]\n"
"s_waitcnt lgkmcnt(9)\n"
"ds_read_b64_tr_b16 v[206:207], v239 offset:38528\n"
"ds_read_b64_tr_b16 v[200:201], v239 offset:24704\n"
"v_mfma_f32_16x16x32_bf16 v[68:71], v[208:211], a[0:3], v[68:71]\n"
"ds_read_b64_tr_b16 v[202:203], v239 offset:26240\n"
"v_add_u32 v247, 0x100, v242\n"
"v_mfma_f32_16x16x32_bf16 v[68:71], v[212:215], a[4:7], v[68:71]\n"
"ds_read_b128 a[56:59], v240 offset:38912\n"
"v_mul_u32_u24 v248, 0x199a, v247\n"
"v_mfma_f32_16x16x32_bf16 v[76:79], v[212:215], a[12:15], v[76:79]\n"
"v_lshrrev_b32 v248, 16, v248\n"
"v_mad_u32_u24 v226, v248, s62, v244\n"
"v_mfma_f32_16x16x32_bf16 v[76:79], v[208:211], a[8:11], v[76:79]\n"
"v_add_u32 v226, 0x1000, v226\n"
"v_mfma_f32_16x16x32_bf16 v[84:87], v[208:211], a[16:19], v[84:87]\n"
"v_mfma_f32_16x16x32_bf16 v[84:87], v[212:215], a[20:23], v[84:87]\n"
"ds_read_b128 a[60:63], v241 offset:38912\n"
"v_mfma_f32_16x16x32_bf16 v[92:95], v[212:215], a[28:31], v[92:95]\n"
"ds_read_b64_tr_b16 v[212:213], v236 offset:37120\n"
"v_mfma_f32_16x16x32_bf16 v[92:95], v[208:211], a[24:27], v[92:95]\n"
"s_waitcnt lgkmcnt(9)\n"
"ds_read_b64_tr_b16 v[214:215], v236 offset:38656\n"
"ds_read_b64_tr_b16 v[208:209], v236 offset:24832\n"
"v_mfma_f32_16x16x32_bf16 v[96:99], v[192:195], a[0:3], v[96:99]\n"
"ds_read_b64_tr_b16 v[210:211], v236 offset:26368\n"
"v_mfma_f32_16x16x32_bf16 v[96:99], v[196:199], a[4:7], v[96:99]\n"
"s_add_u32 m0, s73, 0x7000\n"
"v_mfma_f32_16x16x32_bf16 v[104:107], v[196:199], a[12:15], v[104:107]\n"
"buffer_load_dwordx4 v220, s[44:47], s64 offen offset:0 nt lds\n"
"v_mfma_f32_16x16x32_bf16 v[104:107], v[192:195], a[8:11], v[104:107]\n"
"v_mfma_f32_16x16x32_bf16 v[112:115], v[192:195], a[16:19], v[112:115]\n"
"v_mfma_f32_16x16x32_bf16 v[112:115], v[196:199], a[20:23], v[112:115]\n"
"v_mfma_f32_16x16x32_bf16 v[120:123], v[196:199], a[28:31], v[120:123]\n"
"ds_read_b64_tr_b16 v[196:197], v237 offset:37120\n"
"v_mfma_f32_16x16x32_bf16 v[120:123], v[192:195], a[24:27], v[120:123]\n"
"s_waitcnt lgkmcnt(7)\n"
"ds_read_b64_tr_b16 v[198:199], v237 offset:38656\n"
"ds_read_b64_tr_b16 v[192:193], v237 offset:24832\n"
"v_mfma_f32_16x16x32_bf16 v[100:103], v[200:203], a[0:3], v[100:103]\n"
"ds_read_b64_tr_b16 v[194:195], v237 offset:26368\n"
"v_mfma_f32_16x16x32_bf16 v[100:103], v[204:207], a[4:7], v[100:103]\n"
"v_mfma_f32_16x16x32_bf16 v[108:111], v[204:207], a[12:15], v[108:111]\n"
"v_mfma_f32_16x16x32_bf16 v[108:111], v[200:203], a[8:11], v[108:111]\n"
"v_mfma_f32_16x16x32_bf16 v[116:119], v[200:203], a[16:19], v[116:119]\n"
"buffer_load_dwordx4 v221, s[44:47], s64 offen offset:1024 nt lds\n"
"v_mfma_f32_16x16x32_bf16 v[116:119], v[204:207], a[20:23], v[116:119]\n"
"v_mfma_f32_16x16x32_bf16 v[124:127], v[204:207], a[28:31], v[124:127]\n"
"ds_read_b64_tr_b16 v[204:205], v238 offset:37120\n"
"v_mfma_f32_16x16x32_bf16 v[124:127], v[200:203], a[24:27], v[124:127]\n"
"s_waitcnt lgkmcnt(5)\n"
"ds_read_b64_tr_b16 v[206:207], v238 offset:38656\n"
"ds_read_b64_tr_b16 v[200:201], v238 offset:24832\n"
"v_mfma_f32_16x16x32_bf16 v[128:131], v[208:211], a[0:3], v[128:131]\n"
"s_add_u32 s64, s64, 0x6000\n"
"ds_read_b64_tr_b16 v[202:203], v238 offset:26368\n"
"v_mfma_f32_16x16x32_bf16 v[128:131], v[212:215], a[4:7], v[128:131]\n"
"v_mfma_f32_16x16x32_bf16 v[136:139], v[212:215], a[12:15], v[136:139]\n"
"v_mfma_f32_16x16x32_bf16 v[136:139], v[208:211], a[8:11], v[136:139]\n"
"v_mfma_f32_16x16x32_bf16 v[144:147], v[208:211], a[16:19], v[144:147]\n"
"v_mfma_f32_16x16x32_bf16 v[144:147], v[212:215], a[20:23], v[144:147]\n"
"v_mfma_f32_16x16x32_bf16 v[152:155], v[212:215], a[28:31], v[152:155]\n"
"ds_read_b64_tr_b16 v[212:213], v239 offset:37120\n"
"v_mfma_f32_16x16x32_bf16 v[152:155], v[208:211], a[24:27], v[152:155]\n"
"s_waitcnt lgkmcnt(5)\n"
"ds_read_b64_tr_b16 v[214:215], v239 offset:38656\n"
"ds_read_b64_tr_b16 v[208:209], v239 offset:24832\n"
"v_mfma_f32_16x16x32_bf16 v[132:135], v[192:195], a[0:3], v[132:135]\n"
"ds_read_b64_tr_b16 v[210:211], v239 offset:26368\n"
"v_mfma_f32_16x16x32_bf16 v[132:135], v[196:199], a[4:7], v[132:135]\n"
"v_mfma_f32_16x16x32_bf16 v[140:143], v[196:199], a[12:15], v[140:143]\n"
"v_mfma_f32_16x16x32_bf16 v[140:143], v[192:195], a[8:11], v[140:143]\n"
"v_mfma_f32_16x16x32_bf16 v[148:151], v[192:195], a[16:19], v[148:151]\n"
"v_mfma_f32_16x16x32_bf16 v[148:151], v[196:199], a[20:23], v[148:151]\n"
"v_mfma_f32_16x16x32_bf16 v[156:159], v[196:199], a[28:31], v[156:159]\n"
"s_waitcnt vmcnt(12) lgkmcnt(0)\n"
"s_barrier\n"
"ds_read_b64_tr_b16 v[196:197], v236 offset:61440\n"
"v_mfma_f32_16x16x32_bf16 v[156:159], v[192:195], a[24:27], v[156:159]\n"
"ds_read_b64_tr_b16 v[198:199], v236 offset:62976\n"
"ds_read_b64_tr_b16 v[192:193], v236 offset:49152\n"
"v_mfma_f32_16x16x32_bf16 v[160:163], v[200:203], a[0:3], v[160:163]\n"
"ds_read_b64_tr_b16 v[194:195], v236 offset:50688\n"
"v_mfma_f32_16x16x32_bf16 v[160:163], v[204:207], a[4:7], v[160:163]\n"
"v_mfma_f32_16x16x32_bf16 v[168:171], v[204:207], a[12:15], v[168:171]\n"
"v_mfma_f32_16x16x32_bf16 v[168:171], v[200:203], a[8:11], v[168:171]\n"
"v_mfma_f32_16x16x32_bf16 v[176:179], v[200:203], a[16:19], v[176:179]\n"
"v_mfma_f32_16x16x32_bf16 v[176:179], v[204:207], a[20:23], v[176:179]\n"
"v_mfma_f32_16x16x32_bf16 v[184:187], v[204:207], a[28:31], v[184:187]\n"
"ds_read_b64_tr_b16 v[204:205], v237 offset:61440\n"
"v_mfma_f32_16x16x32_bf16 v[184:187], v[200:203], a[24:27], v[184:187]\n"
"ds_read_b64_tr_b16 v[206:207], v237 offset:62976\n"
"ds_read_b64_tr_b16 v[200:201], v237 offset:49152\n"
"v_mfma_f32_16x16x32_bf16 v[164:167], v[208:211], a[0:3], v[164:167]\n"
"ds_read_b64_tr_b16 v[202:203], v237 offset:50688\n"
"v_mfma_f32_16x16x32_bf16 v[164:167], v[212:215], a[4:7], v[164:167]\n"
"v_mfma_f32_16x16x32_bf16 v[172:175], v[212:215], a[12:15], v[172:175]\n"
"v_mfma_f32_16x16x32_bf16 v[172:175], v[208:211], a[8:11], v[172:175]\n"
"v_mfma_f32_16x16x32_bf16 v[180:183], v[208:211], a[16:19], v[180:183]\n"
"v_mfma_f32_16x16x32_bf16 v[180:183], v[212:215], a[20:23], v[180:183]\n"
"v_mfma_f32_16x16x32_bf16 v[188:191], v[212:215], a[28:31], v[188:191]\n"
"ds_read_b64_tr_b16 v[212:213], v238 offset:61440\n"
"v_mfma_f32_16x16x32_bf16 v[188:191], v[208:211], a[24:27], v[188:191]\n"
"s_waitcnt lgkmcnt(5)\n"
"ds_read_b64_tr_b16 v[214:215], v238 offset:62976\n"
"ds_read_b64_tr_b16 v[208:209], v238 offset:49152\n"
"v_mfma_f32_16x16x32_bf16 v[0:3], v[192:195], a[32:35], v[0:3]\n"
"ds_read_b64_tr_b16 v[210:211], v238 offset:50688\n"
"v_mov_b32 v242, 0\n"
"v_mfma_f32_16x16x32_bf16 v[0:3], v[196:199], a[36:39], v[0:3]\n"
"s_add_u32 m0, s73, 0xc000\n"
"v_mov_b32 v243, 0\n"
"v_mov_b32 v244, 0\n"
"v_mfma_f32_16x16x32_bf16 v[8:11], v[196:199], a[44:47], v[8:11]\n"
"buffer_load_dwordx4 v216, s[48:51], s65 offen offset:0 nt lds\n"
"v_mfma_f32_16x16x32_bf16 v[8:11], v[192:195], a[40:43], v[8:11]\n"
"v_mov_b32 v245, 0\n"
"v_mov_b32 v246, 0\n"
"v_mfma_f32_16x16x32_bf16 v[16:19], v[192:195], a[48:51], v[16:19]\n"
"v_mul_f32 v0, s72, v0\n"
"v_mul_f32 v1, s72, v1\n"
"v_mfma_f32_16x16x32_bf16 v[16:19], v[196:199], a[52:55], v[16:19]\n"
"v_mul_f32 v2, s72, v2\n"
"v_mul_f32 v3, s72, v3\n"
"v_mfma_f32_16x16x32_bf16 v[24:27], v[196:199], a[60:63], v[24:27]\n"
"ds_read_b64_tr_b16 v[196:197], v239 offset:61440\n"
"v_cvt_pk_bf16_f32 v0, v0, v1\n"
"v_mfma_f32_16x16x32_bf16 v[24:27], v[192:195], a[56:59], v[24:27]\n"
"s_waitcnt lgkmcnt(5)\n"
"ds_read_b64_tr_b16 v[198:199], v239 offset:62976\n"
"ds_read_b64_tr_b16 v[192:193], v239 offset:49152\n"
"v_mfma_f32_16x16x32_bf16 v[4:7], v[200:203], a[32:35], v[4:7]\n"
"ds_read_b64_tr_b16 v[194:195], v239 offset:50688\n"
"v_cvt_pk_bf16_f32 v1, v2, v3\n"
"v_mfma_f32_16x16x32_bf16 v[4:7], v[204:207], a[36:39], v[4:7]\n"
"v_mul_f32 v8, s72, v8\n"
"v_mul_f32 v9, s72, v9\n"
"v_mfma_f32_16x16x32_bf16 v[12:15], v[204:207], a[44:47], v[12:15]\n"
"v_mul_f32 v10, s72, v10\n"
"v_mul_f32 v11, s72, v11\n"
"v_mfma_f32_16x16x32_bf16 v[12:15], v[200:203], a[40:43], v[12:15]\n"
"v_cvt_pk_bf16_f32 v8, v8, v9\n"
"v_cvt_pk_bf16_f32 v9, v10, v11\n"
"v_mfma_f32_16x16x32_bf16 v[20:23], v[200:203], a[48:51], v[20:23]\n"
"buffer_load_dwordx4 v217, s[48:51], s65 offen offset:1024 nt lds\n"
"v_mfma_f32_16x16x32_bf16 v[20:23], v[204:207], a[52:55], v[20:23]\n"
"v_mul_f32 v4, s72, v4\n"
"v_mul_f32 v5, s72, v5\n"
"v_mfma_f32_16x16x32_bf16 v[28:31], v[204:207], a[60:63], v[28:31]\n"
"ds_read_b64_tr_b16 v[204:205], v236 offset:61568\n"
"v_mul_f32 v6, s72, v6\n"
"v_mfma_f32_16x16x32_bf16 v[28:31], v[200:203], a[56:59], v[28:31]\n"
"s_waitcnt lgkmcnt(5)\n"
"ds_read_b64_tr_b16 v[206:207], v236 offset:63104\n"
"ds_read_b64_tr_b16 v[200:201], v236 offset:49280\n"
"v_mfma_f32_16x16x32_bf16 v[32:35], v[208:211], a[32:35], v[32:35]\n"
"ds_read_b64_tr_b16 v[202:203], v236 offset:50816\n"
"v_mul_f32 v7, s72, v7\n"
"v_mfma_f32_16x16x32_bf16 v[32:35], v[212:215], a[36:39], v[32:35]\n"
"v_cvt_pk_bf16_f32 v2, v4, v5\n"
"v_cvt_pk_bf16_f32 v3, v6, v7\n"
"v_mfma_f32_16x16x32_bf16 v[40:43], v[212:215], a[44:47], v[40:43]\n"
"v_permlane16_swap_b32 v0, v2\n"
"v_permlane16_swap_b32 v1, v3\n"
"v_mfma_f32_16x16x32_bf16 v[40:43], v[208:211], a[40:43], v[40:43]\n"
"v_accvgpr_write_b32 a192, v0\n"
"v_accvgpr_write_b32 a193, v1\n"
"v_mfma_f32_16x16x32_bf16 v[48:51], v[208:211], a[48:51], v[48:51]\n"
"v_accvgpr_write_b32 a194, v2\n"
"v_accvgpr_write_b32 a195, v3\n"
"v_mfma_f32_16x16x32_bf16 v[48:51], v[212:215], a[52:55], v[48:51]\n"
"v_mul_f32 v32, s72, v32\n"
"v_mul_f32 v33, s72, v33\n"
"v_mfma_f32_16x16x32_bf16 v[56:59], v[212:215], a[60:63], v[56:59]\n"
"Ltp4%=:\n"
"s_cbranch_execz Ltp3%=\n"
"ds_read_b64_tr_b16 v[212:213], v237 offset:61568\n"
"v_mul_f32 v34, s72, v34\n"
"v_mfma_f32_16x16x32_bf16 v[56:59], v[208:211], a[56:59], v[56:59]\n"
"s_waitcnt lgkmcnt(5)\n"
"ds_read_b64_tr_b16 v[214:215], v237 offset:63104\n"
"ds_read_b64_tr_b16 v[208:209], v237 offset:49280\n"
"v_mfma_f32_16x16x32_bf16 v[36:39], v[192:195], a[32:35], v[36:39]\n"
"ds_read_b64_tr_b16 v[210:211], v237 offset:50816\n"
"v_mul_f32 v35, s72, v35\n"
"v_mfma_f32_16x16x32_bf16 v[36:39], v[196:199], a[36:39], v[36:39]\n"
"v_cvt_pk_bf16_f32 v32, v32, v33\n"
"v_cvt_pk_bf16_f32 v33, v34, v35\n"
"v_mfma_f32_16x16x32_bf16 v[44:47], v[196:199], a[44:47], v[44:47]\n"
"buffer_load_dwordx4 v218, s[48:51], s65 offen offset:2048 nt lds\n"
"v_mfma_f32_16x16x32_bf16 v[44:47], v[192:195], a[40:43], v[44:47]\n"
"v_mul_f32 v40, s72, v40\n"
"v_mul_f32 v41, s72, v41\n"
"v_mfma_f32_16x16x32_bf16 v[52:55], v[192:195], a[48:51], v[52:55]\n"
"v_mul_f32 v36, s72, v36\n"
"v_mul_f32 v37, s72, v37\n"
"v_mfma_f32_16x16x32_bf16 v[52:55], v[196:199], a[52:55], v[52:55]\n"
"v_mul_f32 v38, s72, v38\n"
"v_mul_f32 v39, s72, v39\n"
"v_mfma_f32_16x16x32_bf16 v[60:63], v[196:199], a[60:63], v[60:63]\n"
"ds_read_b64_tr_b16 v[196:197], v238 offset:61568\n"
"v_cvt_pk_bf16_f32 v34, v36, v37\n"
"v_mfma_f32_16x16x32_bf16 v[60:63], v[192:195], a[56:59], v[60:63]\n"
"s_waitcnt lgkmcnt(5)\n"
"ds_read_b64_tr_b16 v[198:199], v238 offset:63104\n"
"ds_read_b64_tr_b16 v[192:193], v238 offset:49280\n"
"v_mfma_f32_16x16x32_bf16 v[64:67], v[200:203], a[32:35], v[64:67]\n"
"ds_read_b64_tr_b16 v[194:195], v238 offset:50816\n"
"v_cvt_pk_bf16_f32 v35, v38, v39\n"
"v_mfma_f32_16x16x32_bf16 v[64:67], v[204:207], a[36:39], v[64:67]\n"
"v_permlane16_swap_b32 v32, v34\n"
"v_permlane16_swap_b32 v33, v35\n"
"v_mfma_f32_16x16x32_bf16 v[72:75], v[204:207], a[44:47], v[72:75]\n"
"v_accvgpr_write_b32 a208, v32\n"
"v_accvgpr_write_b32 a209, v33\n"
"v_mfma_f32_16x16x32_bf16 v[72:75], v[200:203], a[40:43], v[72:75]\n"
"v_accvgpr_write_b32 a210, v34\n"
"v_accvgpr_write_b32 a211, v35\n"
"v_mfma_f32_16x16x32_bf16 v[80:83], v[200:203], a[48:51], v[80:83]\n"
"buffer_load_dwordx4 v219, s[48:51], s65 offen offset:3072 nt lds\n"
"v_mfma_f32_16x16x32_bf16 v[80:83], v[204:207], a[52:55], v[80:83]\n"
"v_mul_f32 v42, s72, v42\n"
"v_mul_f32 v43, s72, v43\n"
"v_mfma_f32_16x16x32_bf16 v[88:91], v[204:207], a[60:63], v[88:91]\n"
"ds_read_b64_tr_b16 v[204:205], v239 offset:61568\n"
"v_mul_f32 v44, s72, v44\n"
"v_mfma_f32_16x16x32_bf16 v[88:91], v[200:203], a[56:59], v[88:91]\n"
"s_waitcnt lgkmcnt(5)\n"
"ds_read_b64_tr_b16 v[206:207], v239 offset:63104\n"
"ds_read_b64_tr_b16 v[200:201], v239 offset:49280\n"
"v_mfma_f32_16x16x32_bf16 v[68:71], v[208:211], a[32:35], v[68:71]\n"
"ds_read_b64_tr_b16 v[202:203], v239 offset:50816\n"
"v_mul_f32 v45, s72, v45\n"
"v_mfma_f32_16x16x32_bf16 v[68:71], v[212:215], a[36:39], v[68:71]\n"
"v_mul_f32 v46, s72, v46\n"
"v_mul_f32 v47, s72, v47\n"
"v_mfma_f32_16x16x32_bf16 v[76:79], v[212:215], a[44:47], v[76:79]\n"
"v_cvt_pk_bf16_f32 v40, v40, v41\n"
"v_cvt_pk_bf16_f32 v41, v42, v43\n"
"v_mfma_f32_16x16x32_bf16 v[76:79], v[208:211], a[40:43], v[76:79]\n"
"v_cvt_pk_bf16_f32 v42, v44, v45\n"
"v_cvt_pk_bf16_f32 v43, v46, v47\n"
"v_mfma_f32_16x16x32_bf16 v[84:87], v[208:211], a[48:51], v[84:87]\n"
"v_permlane16_swap_b32 v40, v42\n"
"v_permlane16_swap_b32 v41, v43\n"
"v_mfma_f32_16x16x32_bf16 v[84:87], v[212:215], a[52:55], v[84:87]\n"
"v_accvgpr_write_b32 a212, v40\n"
"v_accvgpr_write_b32 a213, v41\n"
"v_mfma_f32_16x16x32_bf16 v[92:95], v[212:215], a[60:63], v[92:95]\n"
"ds_read_b64_tr_b16 v[212:213], v236 offset:61696\n"
"v_accvgpr_write_b32 a214, v42\n"
"v_mfma_f32_16x16x32_bf16 v[92:95], v[208:211], a[56:59], v[92:95]\n"
"s_waitcnt lgkmcnt(5)\n"
"ds_read_b64_tr_b16 v[214:215], v236 offset:63232\n"
"ds_read_b64_tr_b16 v[208:209], v236 offset:49408\n"
"v_mfma_f32_16x16x32_bf16 v[96:99], v[192:195], a[32:35], v[96:99]\n"
"ds_read_b64_tr_b16 v[210:211], v236 offset:50944\n"
"v_accvgpr_write_b32 a215, v43\n"
"v_mfma_f32_16x16x32_bf16 v[96:99], v[196:199], a[36:39], v[96:99]\n"
"s_add_u32 m0, s73, 0xd000\n"
"v_mul_f32 v12, s72, v12\n"
"v_mul_f32 v13, s72, v13\n"
"v_mfma_f32_16x16x32_bf16 v[104:107], v[196:199], a[44:47], v[104:107]\n"
"buffer_load_dwordx4 v220, s[48:51], s65 offen offset:0 nt lds\n"
"v_mfma_f32_16x16x32_bf16 v[104:107], v[192:195], a[40:43], v[104:107]\n"
"v_mul_f32 v14, s72, v14\n"
"v_mul_f32 v15, s72, v15\n"
"v_mfma_f32_16x16x32_bf16 v[112:115], v[192:195], a[48:51], v[112:115]\n"
"v_cvt_pk_bf16_f32 v10, v12, v13\n"
"v_cvt_pk_bf16_f32 v11, v14, v15\n"
"v_mfma_f32_16x16x32_bf16 v[112:115], v[196:199], a[52:55], v[112:115]\n"
"v_permlane16_swap_b32 v8, v10\n"
"v_permlane16_swap_b32 v9, v11\n"
"v_mfma_f32_16x16x32_bf16 v[120:123], v[196:199], a[60:63], v[120:123]\n"
"ds_read_b64_tr_b16 v[196:197], v237 offset:61696\n"
"v_accvgpr_write_b32 a196, v8\n"
"v_mfma_f32_16x16x32_bf16 v[120:123], v[192:195], a[56:59], v[120:123]\n"
"s_waitcnt lgkmcnt(5)\n"
"ds_read_b64_tr_b16 v[198:199], v237 offset:63232\n"
"ds_read_b64_tr_b16 v[192:193], v237 offset:49408\n"
"v_mfma_f32_16x16x32_bf16 v[100:103], v[200:203], a[32:35], v[100:103]\n"
"ds_read_b64_tr_b16 v[194:195], v237 offset:50944\n"
"v_accvgpr_write_b32 a197, v9\n"
"v_mfma_f32_16x16x32_bf16 v[100:103], v[204:207], a[36:39], v[100:103]\n"
"v_accvgpr_write_b32 a198, v10\n"
"v_accvgpr_write_b32 a199, v11\n"
"v_mfma_f32_16x16x32_bf16 v[108:111], v[204:207], a[44:47], v[108:111]\n"
"v_mul_f32 v16, s72, v16\n"
"v_mul_f32 v17, s72, v17\n"
"v_mfma_f32_16x16x32_bf16 v[108:111], v[200:203], a[40:43], v[108:111]\n"
"v_mul_f32 v18, s72, v18\n"
"v_mul_f32 v19, s72, v19\n"
"v_mfma_f32_16x16x32_bf16 v[116:119], v[200:203], a[48:51], v[116:119]\n"
"buffer_load_dwordx4 v221, s[48:51], s65 offen offset:1024 nt lds\n"
"v_mfma_f32_16x16x32_bf16 v[116:119], v[204:207], a[52:55], v[116:119]\n"
"v_mul_f32 v20, s72, v20\n"
"v_mul_f32 v21, s72, v21\n"
"v_mfma_f32_16x16x32_bf16 v[124:127], v[204:207], a[60:63], v[124:127]\n"
"ds_read_b64_tr_b16 v[204:205], v238 offset:61696\n"
"v_mul_f32 v22, s72, v22\n"
"v_mfma_f32_16x16x32_bf16 v[124:127], v[200:203], a[56:59], v[124:127]\n"
"s_waitcnt lgkmcnt(5)\n"
"ds_read_b64_tr_b16 v[206:207], v238 offset:63232\n"
"ds_read_b64_tr_b16 v[200:201], v238 offset:49408\n"
"v_mfma_f32_16x16x32_bf16 v[128:131], v[208:211], a[32:35], v[128:131]\n"
"s_add_u32 s65, s65, 0x6000\n"
"ds_read_b64_tr_b16 v[202:203], v238 offset:50944\n"
"v_mul_f32 v23, s72, v23\n"
"v_mfma_f32_16x16x32_bf16 v[128:131], v[212:215], a[36:39], v[128:131]\n"
"s_mul_i32 s58, s23, 0x3c000\n"
"s_add_u32 m0, s87, 0x18000\n"
"v_cvt_pk_bf16_f32 v16, v16, v17\n"
"v_mfma_f32_16x16x32_bf16 v[136:139], v[212:215], a[44:47], v[136:139]\n"
"buffer_load_dwordx4 v222, s[40:43], s58 offen offset:0 lds\n"
"v_mfma_f32_16x16x32_bf16 v[136:139], v[208:211], a[40:43], v[136:139]\n"
"v_cvt_pk_bf16_f32 v17, v18, v19\n"
"v_cvt_pk_bf16_f32 v18, v20, v21\n"
"v_mfma_f32_16x16x32_bf16 v[144:147], v[208:211], a[48:51], v[144:147]\n"
"v_cvt_pk_bf16_f32 v19, v22, v23\n"
"v_permlane16_swap_b32 v16, v18\n"
"v_mfma_f32_16x16x32_bf16 v[144:147], v[212:215], a[52:55], v[144:147]\n"
"v_permlane16_swap_b32 v17, v19\n"
"v_accvgpr_write_b32 a200, v16\n"
"v_mfma_f32_16x16x32_bf16 v[152:155], v[212:215], a[60:63], v[152:155]\n"
"ds_read_b64_tr_b16 v[212:213], v239 offset:61696\n"
"v_accvgpr_write_b32 a201, v17\n"
"v_mfma_f32_16x16x32_bf16 v[152:155], v[208:211], a[56:59], v[152:155]\n"
"s_waitcnt lgkmcnt(5)\n"
"ds_read_b64_tr_b16 v[214:215], v239 offset:63232\n"
"ds_read_b64_tr_b16 v[208:209], v239 offset:49408\n"
"v_mfma_f32_16x16x32_bf16 v[132:135], v[192:195], a[32:35], v[132:135]\n"
"ds_read_b64_tr_b16 v[210:211], v239 offset:50944\n"
"v_accvgpr_write_b32 a202, v18\n"
"v_mfma_f32_16x16x32_bf16 v[132:135], v[196:199], a[36:39], v[132:135]\n"
"v_accvgpr_write_b32 a203, v19\n"
"v_mul_f32 v48, s72, v48\n"
"v_mfma_f32_16x16x32_bf16 v[140:143], v[196:199], a[44:47], v[140:143]\n"
"buffer_load_dwordx4 v223, s[40:43], s58 offen offset:1024 lds\n"
"v_mfma_f32_16x16x32_bf16 v[140:143], v[192:195], a[40:43], v[140:143]\n"
"v_mul_f32 v49, s72, v49\n"
"v_mul_f32 v50, s72, v50\n"
"v_mfma_f32_16x16x32_bf16 v[148:151], v[192:195], a[48:51], v[148:151]\n"
"v_mul_f32 v51, s72, v51\n"
"v_mul_f32 v52, s72, v52\n"
"v_mfma_f32_16x16x32_bf16 v[148:151], v[196:199], a[52:55], v[148:151]\n"
"v_mul_f32 v53, s72, v53\n"
"v_mul_f32 v54, s72, v54\n"
"v_mfma_f32_16x16x32_bf16 v[156:159], v[196:199], a[60:63], v[156:159]\n"
"s_waitcnt vmcnt(14) lgkmcnt(0)\n"
"s_barrier\n"
"ds_read_b128 v[196:199], v231 offset:0\n"
"v_mfma_f32_16x16x32_bf16 v[156:159], v[192:195], a[56:59], v[156:159]\n"
"ds_read_b128 v[192:195], v230 offset:0\n"
"v_mul_f32 v55, s72, v55\n"
"v_mfma_f32_16x16x32_bf16 v[160:163], v[200:203], a[32:35], v[160:163]\n"
"v_cvt_pk_bf16_f32 v48, v48, v49\n"
"v_cvt_pk_bf16_f32 v49, v50, v51\n"
"v_mfma_f32_16x16x32_bf16 v[160:163], v[204:207], a[36:39], v[160:163]\n"
"v_cvt_pk_bf16_f32 v50, v52, v53\n"
"v_cvt_pk_bf16_f32 v51, v54, v55\n"
"v_mfma_f32_16x16x32_bf16 v[168:171], v[204:207], a[44:47], v[168:171]\n"
"buffer_load_dwordx4 v224, s[40:43], s58 offen offset:2048 lds\n"
"v_mfma_f32_16x16x32_bf16 v[168:171], v[200:203], a[40:43], v[168:171]\n"
"v_permlane16_swap_b32 v48, v50\n"
"v_permlane16_swap_b32 v49, v51\n"
"v_mfma_f32_16x16x32_bf16 v[176:179], v[200:203], a[48:51], v[176:179]\n"
"buffer_load_dwordx4 v225, s[40:43], s58 offen offset:3072 lds\n"
"v_mfma_f32_16x16x32_bf16 v[176:179], v[204:207], a[52:55], v[176:179]\n"
"v_accvgpr_write_b32 a216, v48\n"
"v_accvgpr_write_b32 a217, v49\n"
"v_mfma_f32_16x16x32_bf16 v[184:187], v[204:207], a[60:63], v[184:187]\n"
"ds_read_b128 v[204:207], v231 offset:6144\n"
"v_accvgpr_write_b32 a218, v50\n"
"v_mfma_f32_16x16x32_bf16 v[184:187], v[200:203], a[56:59], v[184:187]\n"
"ds_read_b128 v[200:203], v230 offset:6144\n"
"v_accvgpr_write_b32 a219, v51\n"
"v_mfma_f32_16x16x32_bf16 v[164:167], v[208:211], a[32:35], v[164:167]\n"
"v_mul_f32 v56, s72, v56\n"
"v_mul_f32 v57, s72, v57\n"
"v_mfma_f32_16x16x32_bf16 v[164:167], v[212:215], a[36:39], v[164:167]\n"
"s_add_u32 m0, s87, 0x19000\n"
"v_mul_f32 v58, s72, v58\n"
"v_mul_f32 v59, s72, v59\n"
"v_mfma_f32_16x16x32_bf16 v[172:175], v[212:215], a[44:47], v[172:175]\n"
"buffer_load_dwordx4 v226, s[40:43], s58 offen offset:0 lds\n"
"v_mfma_f32_16x16x32_bf16 v[172:175], v[208:211], a[40:43], v[172:175]\n"
"v_mul_f32 v60, s72, v60\n"
"v_mul_f32 v61, s72, v61\n"
"v_mfma_f32_16x16x32_bf16 v[180:183], v[208:211], a[48:51], v[180:183]\n"
"v_mul_f32 v62, s72, v62\n"
"v_mul_f32 v63, s72, v63\n"
"v_mfma_f32_16x16x32_bf16 v[180:183], v[212:215], a[52:55], v[180:183]\n"
"v_cvt_pk_bf16_f32 v56, v56, v57\n"
"v_cvt_pk_bf16_f32 v57, v58, v59\n"
"v_mfma_f32_16x16x32_bf16 v[188:191], v[212:215], a[60:63], v[188:191]\n"
"ds_read_b128 v[212:215], v231 offset:12288\n"
"v_cvt_pk_bf16_f32 v58, v60, v61\n"
"v_mfma_f32_16x16x32_bf16 v[188:191], v[208:211], a[56:59], v[188:191]\n"
"s_waitcnt lgkmcnt(3)\n"
"ds_read_b128 v[208:211], v230 offset:12288\n"
"v_cvt_pk_bf16_f32 v59, v62, v63\n"
"v_mfma_f32_16x16x32_bf16 v[0:3], v[192:195], a[192:195], 0\n"
"v_permlane16_swap_b32 v56, v58\n"
"v_permlane16_swap_b32 v57, v59\n"
"v_mfma_f32_16x16x32_bf16 v[0:3], v[196:199], a[208:211], v[0:3]\n"
"s_add_u32 m0, s73, 0x12000\n"
"v_accvgpr_write_b32 a220, v56\n"
"v_accvgpr_write_b32 a221, v57\n"
"v_mfma_f32_16x16x32_bf16 v[8:11], v[196:199], a[212:215], 0\n"
"buffer_load_dwordx4 v216, s[48:51], s65 offen offset:0 nt lds\n"
"v_mfma_f32_16x16x32_bf16 v[8:11], v[192:195], a[196:199], v[8:11]\n"
"v_accvgpr_write_b32 a222, v58\n"
"v_accvgpr_write_b32 a223, v59\n"
"v_mfma_f32_16x16x32_bf16 v[16:19], v[192:195], a[200:203], 0\n"
"v_mul_f32 v24, s72, v24\n"
"v_mul_f32 v25, s72, v25\n"
"v_mfma_f32_16x16x32_bf16 v[16:19], v[196:199], a[216:219], v[16:19]\n"
"v_mul_f32 v26, s72, v26\n"
"v_mul_f32 v27, s72, v27\n"
"v_mul_f32 v28, s72, v28\n"
"v_mul_f32 v29, s72, v29\n"
"v_mul_f32 v30, s72, v30\n"
"v_mul_f32 v31, s72, v31\n"
"v_cvt_pk_bf16_f32 v24, v24, v25\n"
"v_cvt_pk_bf16_f32 v25, v26, v27\n"
"v_cvt_pk_bf16_f32 v26, v28, v29\n"
"v_cvt_pk_bf16_f32 v27, v30, v31\n"
"s_nop 0\n"
"v_permlane16_swap_b32 v24, v26\n"
"v_accvgpr_write_b32 a204, v24\n"
"v_accvgpr_write_b32 a206, v26\n"
"v_permlane16_swap_b32 v25, v27\n"
"v_accvgpr_write_b32 a205, v25\n"
"v_accvgpr_write_b32 a207, v27\n"
"v_mfma_f32_16x16x32_bf16 v[24:27], v[196:199], a[220:223], 0\n"
"ds_read_b128 v[196:199], v231 offset:18432\n"
"v_mul_f32 v64, s72, v64\n"
"v_mfma_f32_16x16x32_bf16 v[24:27], v[192:195], a[204:207], v[24:27]\n"
"s_waitcnt lgkmcnt(3)\n"
"ds_read_b128 v[192:195], v230 offset:18432\n"
"v_mul_f32 v65, s72, v65\n"
"v_mfma_f32_16x16x32_bf16 v[4:7], v[200:203], a[192:195], 0\n"
"v_mul_f32 v66, s72, v66\n"
"v_mul_f32 v67, s72, v67\n"
"v_mfma_f32_16x16x32_bf16 v[4:7], v[204:207], a[208:211], v[4:7]\n"
"v_mul_f32 v68, s72, v68\n"
"v_mul_f32 v69, s72, v69\n"
"v_mfma_f32_16x16x32_bf16 v[12:15], v[204:207], a[212:215], 0\n"
"v_mul_f32 v70, s72, v70\n"
"v_mul_f32 v71, s72, v71\n"
"v_mfma_f32_16x16x32_bf16 v[12:15], v[200:203], a[196:199], v[12:15]\n"
"v_cvt_pk_bf16_f32 v64, v64, v65\n"
"v_cvt_pk_bf16_f32 v65, v66, v67\n"
"v_mfma_f32_16x16x32_bf16 v[20:23], v[200:203], a[200:203], 0\n"
"buffer_load_dwordx4 v217, s[48:51], s65 offen offset:1024 nt lds\n"
"v_mfma_f32_16x16x32_bf16 v[20:23], v[204:207], a[216:219], v[20:23]\n"
"v_cvt_pk_bf16_f32 v66, v68, v69\n"
"v_cvt_pk_bf16_f32 v67, v70, v71\n"
"v_mfma_f32_16x16x32_bf16 v[28:31], v[204:207], a[220:223], 0\n"
"ds_read_b128 v[204:207], v231 offset:128\n"
"v_permlane16_swap_b32 v64, v66\n"
"v_mfma_f32_16x16x32_bf16 v[28:31], v[200:203], a[204:207], v[28:31]\n"
"s_waitcnt lgkmcnt(3)\n"
"ds_read_b128 v[200:203], v230 offset:128\n"
"v_permlane16_swap_b32 v65, v67\n"
"v_mfma_f32_16x16x32_bf16 v[32:35], v[208:211], a[192:195], 0\n"
"v_accvgpr_write_b32 a224, v64\n"
"v_accvgpr_write_b32 a225, v65\n"
"v_mfma_f32_16x16x32_bf16 v[32:35], v[212:215], a[208:211], v[32:35]\n"
"v_accvgpr_write_b32 a226, v66\n"
"v_accvgpr_write_b32 a227, v67\n"
"v_mfma_f32_16x16x32_bf16 v[40:43], v[212:215], a[212:215], 0\n"
"v_mul_f32 v96, s72, v96\n"
"v_mul_f32 v97, s72, v97\n"
"v_mfma_f32_16x16x32_bf16 v[40:43], v[208:211], a[196:199], v[40:43]\n"
"v_mul_f32 v98, s72, v98\n"
"v_mul_f32 v99, s72, v99\n"
"v_mfma_f32_16x16x32_bf16 v[48:51], v[208:211], a[200:203], 0\n"
"v_mul_f32 v100, s72, v100\n"
"v_mul_f32 v101, s72, v101\n"
"v_mfma_f32_16x16x32_bf16 v[48:51], v[212:215], a[216:219], v[48:51]\n"
"v_mul_f32 v102, s72, v102\n"
"v_mul_f32 v103, s72, v103\n"
"v_mfma_f32_16x16x32_bf16 v[56:59], v[212:215], a[220:223], 0\n"
"ds_read_b128 v[212:215], v231 offset:6272\n"
"v_cvt_pk_bf16_f32 v96, v96, v97\n"
"v_mfma_f32_16x16x32_bf16 v[56:59], v[208:211], a[204:207], v[56:59]\n"
"s_waitcnt lgkmcnt(3)\n"
"ds_read_b128 v[208:211], v230 offset:6272\n"
"v_cvt_pk_bf16_f32 v97, v98, v99\n"
"v_mfma_f32_16x16x32_bf16 v[36:39], v[192:195], a[192:195], 0\n"
"v_cvt_pk_bf16_f32 v98, v100, v101\n"
"v_cvt_pk_bf16_f32 v99, v102, v103\n"
"v_mfma_f32_16x16x32_bf16 v[36:39], v[196:199], a[208:211], v[36:39]\n"
"v_permlane16_swap_b32 v96, v98\n"
"v_permlane16_swap_b32 v97, v99\n"
"v_mfma_f32_16x16x32_bf16 v[44:47], v[196:199], a[212:215], 0\n"
"buffer_load_dwordx4 v218, s[48:51], s65 offen offset:2048 nt lds\n"
"v_mfma_f32_16x16x32_bf16 v[44:47], v[192:195], a[196:199], v[44:47]\n"
"v_accvgpr_write_b32 a240, v96\n"
"v_accvgpr_write_b32 a241, v97\n"
"v_mfma_f32_16x16x32_bf16 v[52:55], v[192:195], a[200:203], 0\n"
"v_accvgpr_write_b32 a242, v98\n"
"v_accvgpr_write_b32 a243, v99\n"
"v_mfma_f32_16x16x32_bf16 v[52:55], v[196:199], a[216:219], v[52:55]\n"
"v_mul_f32 v104, s72, v104\n"
"v_mul_f32 v105, s72, v105\n"
"v_mfma_f32_16x16x32_bf16 v[60:63], v[196:199], a[220:223], 0\n"
"ds_read_b128 v[196:199], v231 offset:12416\n"
"v_mul_f32 v106, s72, v106\n"
"v_mfma_f32_16x16x32_bf16 v[60:63], v[192:195], a[204:207], v[60:63]\n"
"s_waitcnt lgkmcnt(3)\n"
"ds_read_b128 v[192:195], v230 offset:12416\n"
"v_mul_f32 v107, s72, v107\n"
"v_mfma_f32_16x16x32_bf16 v[0:3], v[200:203], a[224:227], v[0:3]\n"
"v_mul_f32 v108, s72, v108\n"
"v_mul_f32 v109, s72, v109\n"
"v_mul_f32 v110, s72, v110\n"
"v_mul_f32 v111, s72, v111\n"
"v_cvt_pk_bf16_f32 v104, v104, v105\n"
"v_cvt_pk_bf16_f32 v105, v106, v107\n"
"v_cvt_pk_bf16_f32 v106, v108, v109\n"
"v_cvt_pk_bf16_f32 v107, v110, v111\n"
"s_nop 0\n"
"v_permlane16_swap_b32 v104, v106\n"
"v_accvgpr_write_b32 a244, v104\n"
"v_accvgpr_write_b32 a246, v106\n"
"v_permlane16_swap_b32 v105, v107\n"
"v_accvgpr_write_b32 a245, v105\n"
"v_accvgpr_write_b32 a247, v107\n"
"v_mfma_f32_16x16x32_bf16 v[0:3], v[204:207], a[240:243], v[0:3]\n"
"v_mul_f32 v72, s72, v72\n"
"v_mul_f32 v73, s72, v73\n"
"v_mul_f32 v74, s72, v74\n"
"v_mul_f32 v75, s72, v75\n"
"v_mul_f32 v76, s72, v76\n"
"v_mul_f32 v77, s72, v77\n"
"v_mul_f32 v78, s72, v78\n"
"v_mul_f32 v79, s72, v79\n"
"v_cvt_pk_bf16_f32 v72, v72, v73\n"
"v_cvt_pk_bf16_f32 v73, v74, v75\n"
"v_cvt_pk_bf16_f32 v74, v76, v77\n"
"v_cvt_pk_bf16_f32 v75, v78, v79\n"
"s_nop 0\n"
"v_permlane16_swap_b32 v72, v74\n"
"v_accvgpr_write_b32 a228, v72\n"
"v_accvgpr_write_b32 a230, v74\n"
"v_permlane16_swap_b32 v73, v75\n"
"v_accvgpr_write_b32 a229, v73\n"
"v_accvgpr_write_b32 a231, v75\n"
"v_mfma_f32_16x16x32_bf16 v[8:11], v[204:207], a[244:247], v[8:11]\n"
"v_mul_f32 v80, s72, v80\n"
"v_mul_f32 v81, s72, v81\n"
"v_mul_f32 v82, s72, v82\n"
"v_mul_f32 v83, s72, v83\n"
"v_mul_f32 v84, s72, v84\n"
"v_mul_f32 v85, s72, v85\n"
"v_mul_f32 v86, s72, v86\n"
"v_mul_f32 v87, s72, v87\n"
"v_cvt_pk_bf16_f32 v80, v80, v81\n"
"v_cvt_pk_bf16_f32 v81, v82, v83\n"
"v_cvt_pk_bf16_f32 v82, v84, v85\n"
"v_cvt_pk_bf16_f32 v83, v86, v87\n"
"s_nop 0\n"
"v_permlane16_swap_b32 v80, v82\n"
"v_accvgpr_write_b32 a232, v80\n"
"v_accvgpr_write_b32 a234, v82\n"
"v_permlane16_swap_b32 v81, v83\n"
"v_accvgpr_write_b32 a233, v81\n"
"v_accvgpr_write_b32 a235, v83\n"
"v_mfma_f32_16x16x32_bf16 v[8:11], v[200:203], a[228:231], v[8:11]\n"
"v_mul_f32 v112, s72, v112\n"
"v_mul_f32 v113, s72, v113\n"
"v_mul_f32 v114, s72, v114\n"
"v_mul_f32 v115, s72, v115\n"
"v_mul_f32 v116, s72, v116\n"
"v_mul_f32 v117, s72, v117\n"
"v_mul_f32 v118, s72, v118\n"
"v_mul_f32 v119, s72, v119\n"
"v_cvt_pk_bf16_f32 v112, v112, v113\n"
"v_cvt_pk_bf16_f32 v113, v114, v115\n"
"v_cvt_pk_bf16_f32 v114, v116, v117\n"
"v_cvt_pk_bf16_f32 v115, v118, v119\n"
"s_nop 0\n"
"v_permlane16_swap_b32 v112, v114\n"
"v_accvgpr_write_b32 a248, v112\n"
"v_accvgpr_write_b32 a250, v114\n"
"v_permlane16_swap_b32 v113, v115\n"
"v_accvgpr_write_b32 a249, v113\n"
"v_accvgpr_write_b32 a251, v115\n"
"v_mfma_f32_16x16x32_bf16 v[16:19], v[200:203], a[232:235], v[16:19]\n"
"buffer_load_dwordx4 v219, s[48:51], s65 offen offset:3072 nt lds\n"
"v_mul_f32 v120, s72, v120\n"
"v_mul_f32 v121, s72, v121\n"
"v_mul_f32 v122, s72, v122\n"
"v_mul_f32 v123, s72, v123\n"
"v_mul_f32 v124, s72, v124\n"
"v_mul_f32 v125, s72, v125\n"
"v_mul_f32 v126, s72, v126\n"
"v_mul_f32 v127, s72, v127\n"
"v_cvt_pk_bf16_f32 v120, v120, v121\n"
"v_cvt_pk_bf16_f32 v121, v122, v123\n"
"v_cvt_pk_bf16_f32 v122, v124, v125\n"
"v_cvt_pk_bf16_f32 v123, v126, v127\n"
"s_nop 0\n"
"v_permlane16_swap_b32 v120, v122\n"
"v_accvgpr_write_b32 a252, v120\n"
"v_accvgpr_write_b32 a254, v122\n"
"v_permlane16_swap_b32 v121, v123\n"
"v_accvgpr_write_b32 a253, v121\n"
"v_accvgpr_write_b32 a255, v123\n"
"v_mfma_f32_16x16x32_bf16 v[16:19], v[204:207], a[248:251], v[16:19]\n"
"v_mul_f32 v88, s72, v88\n"
"v_mul_f32 v89, s72, v89\n"
"v_mul_f32 v90, s72, v90\n"
"v_mul_f32 v91, s72, v91\n"
"v_mul_f32 v92, s72, v92\n"
"v_mul_f32 v93, s72, v93\n"
"v_mul_f32 v94, s72, v94\n"
"v_mul_f32 v95, s72, v95\n"
"v_cvt_pk_bf16_f32 v88, v88, v89\n"
"v_cvt_pk_bf16_f32 v89, v90, v91\n"
"v_cvt_pk_bf16_f32 v90, v92, v93\n"
"v_cvt_pk_bf16_f32 v91, v94, v95\n"
"s_nop 0\n"
"v_permlane16_swap_b32 v88, v90\n"
"v_accvgpr_write_b32 a236, v88\n"
"v_accvgpr_write_b32 a238, v90\n"
"v_permlane16_swap_b32 v89, v91\n"
"v_accvgpr_write_b32 a237, v89\n"
"v_accvgpr_write_b32 a239, v91\n"
"v_mfma_f32_16x16x32_bf16 v[24:27], v[204:207], a[252:255], v[24:27]\n"
"ds_read_b128 v[204:207], v231 offset:18560\n"
"v_mul_f32 v128, s72, v128\n"
"v_mfma_f32_16x16x32_bf16 v[24:27], v[200:203], a[236:239], v[24:27]\n"
"s_waitcnt lgkmcnt(3)\n"
"ds_read_b128 v[200:203], v230 offset:18560\n"
"v_mul_f32 v129, s72, v129\n"
"v_mfma_f32_16x16x32_bf16 v[4:7], v[208:211], a[224:227], v[4:7]\n"
"v_mul_f32 v130, s72, v130\n"
"v_mul_f32 v131, s72, v131\n"
"v_mfma_f32_16x16x32_bf16 v[4:7], v[212:215], a[240:243], v[4:7]\n"
"v_mul_f32 v132, s72, v132\n"
"v_mul_f32 v133, s72, v133\n"
"v_mfma_f32_16x16x32_bf16 v[12:15], v[212:215], a[244:247], v[12:15]\n"
"v_mul_f32 v134, s72, v134\n"
"v_mul_f32 v135, s72, v135\n"
"v_mfma_f32_16x16x32_bf16 v[12:15], v[208:211], a[228:231], v[12:15]\n"
"v_cvt_pk_bf16_f32 v128, v128, v129\n"
"v_cvt_pk_bf16_f32 v129, v130, v131\n"
"v_mfma_f32_16x16x32_bf16 v[20:23], v[208:211], a[232:235], v[20:23]\n"
"v_cvt_pk_bf16_f32 v130, v132, v133\n"
"v_cvt_pk_bf16_f32 v131, v134, v135\n"
"v_mfma_f32_16x16x32_bf16 v[20:23], v[212:215], a[248:251], v[20:23]\n"
"v_permlane16_swap_b32 v128, v130\n"
"v_permlane16_swap_b32 v129, v131\n"
"v_mfma_f32_16x16x32_bf16 v[28:31], v[212:215], a[252:255], v[28:31]\n"
"ds_read_b128 v[212:215], v231 offset:256\n"
"v_mul_f32 v160, s72, v160\n"
"v_mfma_f32_16x16x32_bf16 v[28:31], v[208:211], a[236:239], v[28:31]\n"
"s_waitcnt lgkmcnt(3)\n"
"ds_read_b128 v[208:211], v230 offset:256\n"
"v_mul_f32 v161, s72, v161\n"
"v_mfma_f32_16x16x32_bf16 v[32:35], v[192:195], a[224:227], v[32:35]\n"
"v_mul_f32 v162, s72, v162\n"
"v_mul_f32 v163, s72, v163\n"
"v_mfma_f32_16x16x32_bf16 v[32:35], v[196:199], a[240:243], v[32:35]\n"
"s_add_u32 m0, s73, 0x13000\n"
"v_mul_f32 v164, s72, v164\n"
"v_mul_f32 v165, s72, v165\n"
"v_mfma_f32_16x16x32_bf16 v[40:43], v[196:199], a[244:247], v[40:43]\n"
"buffer_load_dwordx4 v220, s[48:51], s65 offen offset:0 nt lds\n"
"v_mfma_f32_16x16x32_bf16 v[40:43], v[192:195], a[228:231], v[40:43]\n"
"v_mul_f32 v166, s72, v166\n"
"v_mul_f32 v167, s72, v167\n"
"v_mfma_f32_16x16x32_bf16 v[48:51], v[192:195], a[232:235], v[48:51]\n"
"v_cvt_pk_bf16_f32 v160, v160, v161\n"
"v_cvt_pk_bf16_f32 v161, v162, v163\n"
"v_mfma_f32_16x16x32_bf16 v[48:51], v[196:199], a[248:251], v[48:51]\n"
"v_cvt_pk_bf16_f32 v162, v164, v165\n"
"v_cvt_pk_bf16_f32 v163, v166, v167\n"
"v_mfma_f32_16x16x32_bf16 v[56:59], v[196:199], a[252:255], v[56:59]\n"
"ds_read_b128 v[196:199], v231 offset:6400\n"
"v_permlane16_swap_b32 v160, v162\n"
"v_mfma_f32_16x16x32_bf16 v[56:59], v[192:195], a[236:239], v[56:59]\n"
"s_waitcnt lgkmcnt(3)\n"
"ds_read_b128 v[192:195], v230 offset:6400\n"
"v_permlane16_swap_b32 v161, v163\n"
"v_mfma_f32_16x16x32_bf16 v[36:39], v[200:203], a[224:227], v[36:39]\n"
"v_mul_f32 v168, s72, v168\n"
"v_mul_f32 v169, s72, v169\n"
"v_mfma_f32_16x16x32_bf16 v[36:39], v[204:207], a[240:243], v[36:39]\n"
"v_mul_f32 v170, s72, v170\n"
"v_mul_f32 v171, s72, v171\n"
"v_mfma_f32_16x16x32_bf16 v[44:47], v[204:207], a[244:247], v[44:47]\n"
"v_mul_f32 v172, s72, v172\n"
"v_mul_f32 v173, s72, v173\n"
"v_mfma_f32_16x16x32_bf16 v[44:47], v[200:203], a[228:231], v[44:47]\n"
"v_mul_f32 v174, s72, v174\n"
"v_mul_f32 v175, s72, v175\n"
"v_mfma_f32_16x16x32_bf16 v[52:55], v[200:203], a[232:235], v[52:55]\n"
"buffer_load_dwordx4 v221, s[48:51], s65 offen offset:1024 nt lds\n"
"v_mfma_f32_16x16x32_bf16 v[52:55], v[204:207], a[248:251], v[52:55]\n"
"v_cvt_pk_bf16_f32 v168, v168, v169\n"
"v_cvt_pk_bf16_f32 v169, v170, v171\n"
"v_mfma_f32_16x16x32_bf16 v[60:63], v[204:207], a[252:255], v[60:63]\n"
"ds_read_b128 v[204:207], v231 offset:12544\n"
"v_cvt_pk_bf16_f32 v170, v172, v173\n"
"v_mfma_f32_16x16x32_bf16 v[60:63], v[200:203], a[236:239], v[60:63]\n"
"s_waitcnt lgkmcnt(3)\n"
"ds_read_b128 v[200:203], v230 offset:12544\n"
"v_cvt_pk_bf16_f32 v171, v174, v175\n"
"v_mfma_f32_16x16x32_bf16 v[0:3], v[208:211], v[128:131], v[0:3]\n"
"s_add_u32 s65, s65, 0x6000\n"
"v_permlane16_swap_b32 v168, v170\n"
"v_permlane16_swap_b32 v169, v171\n"
"v_mfma_f32_16x16x32_bf16 v[0:3], v[212:215], v[160:163], v[0:3]\n"
"s_mul_i32 s58, s23, 0x3c000\n"
"s_add_u32 m0, s87, 0x1d000\n"
"v_mul_f32 v136, s72, v136\n"
"v_mul_f32 v137, s72, v137\n"
"v_mul_f32 v138, s72, v138\n"
"v_mul_f32 v139, s72, v139\n"
"v_mul_f32 v140, s72, v140\n"
"v_mul_f32 v141, s72, v141\n"
"v_mul_f32 v142, s72, v142\n"
"v_mul_f32 v143, s72, v143\n"
"v_cvt_pk_bf16_f32 v136, v136, v137\n"
"v_cvt_pk_bf16_f32 v137, v138, v139\n"
"v_cvt_pk_bf16_f32 v138, v140, v141\n"
"v_cvt_pk_bf16_f32 v139, v142, v143\n"
"s_nop 0\n"
"v_permlane16_swap_b32 v136, v138\n"
"v_permlane16_swap_b32 v137, v139\n"
"v_mfma_f32_16x16x32_bf16 v[8:11], v[212:215], v[168:171], v[8:11]\n"
"s_add_u32 s58, s58, 0x14000\n"
"buffer_load_dwordx4 v222, s[40:43], s58 offen offset:0 lds\n"
"v_mul_f32 v144, s72, v144\n"
"v_mul_f32 v145, s72, v145\n"
"v_mul_f32 v146, s72, v146\n"
"v_mul_f32 v147, s72, v147\n"
"v_mul_f32 v148, s72, v148\n"
"v_mul_f32 v149, s72, v149\n"
"v_mul_f32 v150, s72, v150\n"
"v_mul_f32 v151, s72, v151\n"
"v_cvt_pk_bf16_f32 v144, v144, v145\n"
"v_cvt_pk_bf16_f32 v145, v146, v147\n"
"v_cvt_pk_bf16_f32 v146, v148, v149\n"
"v_cvt_pk_bf16_f32 v147, v150, v151\n"
"s_nop 0\n"
"v_permlane16_swap_b32 v144, v146\n"
"v_permlane16_swap_b32 v145, v147\n"
"v_mfma_f32_16x16x32_bf16 v[8:11], v[208:211], v[136:139], v[8:11]\n"
"v_mul_f32 v176, s72, v176\n"
"v_mul_f32 v177, s72, v177\n"
"v_mul_f32 v178, s72, v178\n"
"v_mul_f32 v179, s72, v179\n"
"v_mul_f32 v180, s72, v180\n"
"v_mul_f32 v181, s72, v181\n"
"v_mul_f32 v182, s72, v182\n"
"v_mul_f32 v183, s72, v183\n"
"v_cvt_pk_bf16_f32 v176, v176, v177\n"
"v_cvt_pk_bf16_f32 v177, v178, v179\n"
"v_cvt_pk_bf16_f32 v178, v180, v181\n"
"v_cvt_pk_bf16_f32 v179, v182, v183\n"
"s_nop 0\n"
"v_permlane16_swap_b32 v176, v178\n"
"v_permlane16_swap_b32 v177, v179\n"
"v_mfma_f32_16x16x32_bf16 v[16:19], v[208:211], v[144:147], v[16:19]\n"
"v_mul_f32 v184, s72, v184\n"
"v_mul_f32 v185, s72, v185\n"
"v_mul_f32 v186, s72, v186\n"
"v_mul_f32 v187, s72, v187\n"
"v_mul_f32 v188, s72, v188\n"
"v_mul_f32 v189, s72, v189\n"
"v_mul_f32 v190, s72, v190\n"
"v_mul_f32 v191, s72, v191\n"
"v_cvt_pk_bf16_f32 v184, v184, v185\n"
"v_cvt_pk_bf16_f32 v185, v186, v187\n"
"v_cvt_pk_bf16_f32 v186, v188, v189\n"
"v_cvt_pk_bf16_f32 v187, v190, v191\n"
"s_nop 0\n"
"v_permlane16_swap_b32 v184, v186\n"
"v_permlane16_swap_b32 v185, v187\n"
"v_mfma_f32_16x16x32_bf16 v[16:19], v[212:215], v[176:179], v[16:19]\n"
"v_mul_f32 v152, s72, v152\n"
"v_mul_f32 v153, s72, v153\n"
"v_mul_f32 v154, s72, v154\n"
"v_mul_f32 v155, s72, v155\n"
"v_mul_f32 v156, s72, v156\n"
"v_mul_f32 v157, s72, v157\n"
"v_mul_f32 v158, s72, v158\n"
"v_mul_f32 v159, s72, v159\n"
"v_cvt_pk_bf16_f32 v152, v152, v153\n"
"v_cvt_pk_bf16_f32 v153, v154, v155\n"
"v_cvt_pk_bf16_f32 v154, v156, v157\n"
"v_cvt_pk_bf16_f32 v155, v158, v159\n"
"s_nop 0\n"
"v_permlane16_swap_b32 v152, v154\n"
"v_permlane16_swap_b32 v153, v155\n"
"v_mfma_f32_16x16x32_bf16 v[24:27], v[212:215], v[184:187], v[24:27]\n"
"ds_read_b128 v[212:215], v231 offset:18688\n"
"v_max3_f32 v164, v0, v1, v2\n"
"v_mfma_f32_16x16x32_bf16 v[24:27], v[208:211], v[152:155], v[24:27]\n"
"s_waitcnt lgkmcnt(3)\n"
"ds_read_b128 v[208:211], v230 offset:18688\n"
"v_max_f32 v164, v164, v3\n"
"v_mfma_f32_16x16x32_bf16 v[4:7], v[192:195], v[128:131], v[4:7]\n"
"v_max3_f32 v165, v8, v9, v10\n"
"v_max_f32 v165, v165, v11\n"
"v_mfma_f32_16x16x32_bf16 v[4:7], v[196:199], v[160:163], v[4:7]\n"
"v_max3_f32 v166, v16, v17, v18\n"
"v_max_f32 v166, v166, v19\n"
"v_mfma_f32_16x16x32_bf16 v[12:15], v[196:199], v[168:171], v[12:15]\n"
"buffer_load_dwordx4 v223, s[40:43], s58 offen offset:1024 lds\n"
"v_mfma_f32_16x16x32_bf16 v[12:15], v[192:195], v[136:139], v[12:15]\n"
"Ltp5%=:\n"
"s_cbranch_execz Ltp7%=\n"
"v_max3_f32 v167, v24, v25, v26\n"
"v_max_f32 v167, v167, v27\n"
"v_mfma_f32_16x16x32_bf16 v[20:23], v[192:195], v[144:147], v[20:23]\n"
"v_max3_f32 v164, v164, v4, v5\n"
"v_max3_f32 v164, v164, v6, v7\n"
"v_mfma_f32_16x16x32_bf16 v[20:23], v[196:199], v[176:179], v[20:23]\n"
"v_mov_b32 v247, 0\n"
"v_mov_b32 v248, 0\n"
"v_mfma_f32_16x16x32_bf16 v[28:31], v[196:199], v[184:187], v[28:31]\n"
"s_waitcnt vmcnt(19) lgkmcnt(0)\n"
"s_barrier\n"
"ds_read_b128 v[196:199], v231 offset:24576\n"
"v_mfma_f32_16x16x32_bf16 v[28:31], v[192:195], v[152:155], v[28:31]\n"
"ds_read_b128 v[192:195], v230 offset:24576\n"
"v_max3_f32 v165, v165, v12, v13\n"
"v_mfma_f32_16x16x32_bf16 v[32:35], v[200:203], v[128:131], v[32:35]\n"
"v_max3_f32 v165, v165, v14, v15\n"
"v_max3_f32 v166, v166, v20, v21\n"
"v_mfma_f32_16x16x32_bf16 v[32:35], v[204:207], v[160:163], v[32:35]\n"
"v_max3_f32 v166, v166, v22, v23\n"
"v_mov_b32 v249, 0\n"
"v_mfma_f32_16x16x32_bf16 v[40:43], v[204:207], v[168:171], v[40:43]\n"
"buffer_load_dwordx4 v224, s[40:43], s58 offen offset:2048 lds\n"
"v_mfma_f32_16x16x32_bf16 v[40:43], v[200:203], v[136:139], v[40:43]\n"
"v_max3_f32 v167, v167, v28, v29\n"
"v_max3_f32 v167, v167, v30, v31\n"
"v_mfma_f32_16x16x32_bf16 v[48:51], v[200:203], v[144:147], v[48:51]\n"
"buffer_load_dwordx4 v225, s[40:43], s58 offen offset:3072 lds\n"
"v_mfma_f32_16x16x32_bf16 v[48:51], v[204:207], v[176:179], v[48:51]\n"
"v_max3_f32 v164, v164, v32, v33\n"
"v_max3_f32 v164, v164, v34, v35\n"
"v_mfma_f32_16x16x32_bf16 v[56:59], v[204:207], v[184:187], v[56:59]\n"
"ds_read_b128 v[204:207], v231 offset:24704\n"
"v_max3_f32 v165, v165, v40, v41\n"
"v_mfma_f32_16x16x32_bf16 v[56:59], v[200:203], v[152:155], v[56:59]\n"
"ds_read_b128 v[200:203], v230 offset:24704\n"
"v_max3_f32 v165, v165, v42, v43\n"
"v_mfma_f32_16x16x32_bf16 v[36:39], v[208:211], v[128:131], v[36:39]\n"
"v_max3_f32 v166, v166, v48, v49\n"
"v_max3_f32 v166, v166, v50, v51\n"
"v_mfma_f32_16x16x32_bf16 v[36:39], v[212:215], v[160:163], v[36:39]\n"
"s_add_u32 m0, s87, 0x1e000\n"
"v_mfma_f32_16x16x32_bf16 v[44:47], v[212:215], v[168:171], v[44:47]\n"
"buffer_load_dwordx4 v226, s[40:43], s58 offen offset:0 lds\n"
"v_mfma_f32_16x16x32_bf16 v[44:47], v[208:211], v[136:139], v[44:47]\n"
"v_max3_f32 v167, v167, v56, v57\n"
"v_max3_f32 v167, v167, v58, v59\n"
"v_mfma_f32_16x16x32_bf16 v[52:55], v[208:211], v[144:147], v[52:55]\n"
"v_max3_f32 v164, v164, v36, v37\n"
"v_max3_f32 v164, v164, v38, v39\n"
"v_mfma_f32_16x16x32_bf16 v[52:55], v[212:215], v[176:179], v[52:55]\n"
"s_cmp_eq_u32 s63, 1\n"
"v_mfma_f32_16x16x32_bf16 v[60:63], v[212:215], v[184:187], v[60:63]\n"
"s_cbranch_scc1 Lmax%=\n"
"ds_read_b128 v[212:215], v231 offset:24832\n"
"v_max3_f32 v165, v165, v44, v45\n"
"v_mfma_f32_16x16x32_bf16 v[60:63], v[208:211], v[152:155], v[60:63]\n"
"s_waitcnt lgkmcnt(3)\n"
"ds_read_b128 v[208:211], v230 offset:24832\n"
"v_max3_f32 v165, v165, v46, v47\n"
"v_mfma_f32_16x16x32_bf16 v[64:67], v[192:195], a[192:195], 0\n"
"v_max3_f32 v166, v166, v52, v53\n"
"v_max3_f32 v166, v166, v54, v55\n"
"v_mfma_f32_16x16x32_bf16 v[64:67], v[196:199], a[208:211], v[64:67]\n"
"s_add_u32 m0, s73, 0\n"
"v_permlane32_swap_b32 v164, v165\n"
"v_max_f32 v164, v164, v165\n"
"v_mfma_f32_16x16x32_bf16 v[72:75], v[196:199], a[212:215], 0\n"
"buffer_load_dwordx4 v216, s[44:47], s64 offen offset:0 nt lds\n"
"v_mfma_f32_16x16x32_bf16 v[72:75], v[192:195], a[196:199], v[72:75]\n"
"v_max3_f32 v167, v167, v60, v61\n"
"v_max3_f32 v167, v167, v62, v63\n"
"v_mfma_f32_16x16x32_bf16 v[80:83], v[192:195], a[200:203], 0\n"
"v_permlane32_swap_b32 v166, v167\n"
"v_max_f32 v166, v166, v167\n"
"v_mfma_f32_16x16x32_bf16 v[80:83], v[196:199], a[216:219], v[80:83]\n"
"v_permlane16_swap_b32 v164, v166\n"
"v_max3_f32 v164, v164, v166, v251\n"
"v_mfma_f32_16x16x32_bf16 v[88:91], v[196:199], a[220:223], 0\n"
"ds_read_b128 v[196:199], v231 offset:30720\n"
"v_sub_f32 v132, 0, v164\n"
"v_mfma_f32_16x16x32_bf16 v[88:91], v[192:195], a[204:207], v[88:91]\n"
"s_waitcnt lgkmcnt(3)\n"
"ds_read_b128 v[192:195], v230 offset:30720\n"
"v_mov_b32 v148, v132\n"
"v_mfma_f32_16x16x32_bf16 v[64:67], v[200:203], a[224:227], v[64:67]\n"
"s_nop 0\n"
"v_permlane16_swap_b32 v132, v148\n"
"v_mov_b32 v140, v132\n"
"v_mfma_f32_16x16x32_bf16 v[64:67], v[204:207], a[240:243], v[64:67]\n"
"s_nop 0\n"
"v_permlane32_swap_b32 v132, v140\n"
"v_mov_b32 v133, v132\n"
"v_mfma_f32_16x16x32_bf16 v[72:75], v[204:207], a[244:247], v[72:75]\n"
"v_mov_b32 v134, v132\n"
"v_mov_b32 v135, v132\n"
"v_mfma_f32_16x16x32_bf16 v[72:75], v[200:203], a[228:231], v[72:75]\n"
"v_mov_b32 v141, v140\n"
"v_mov_b32 v142, v140\n"
"v_mfma_f32_16x16x32_bf16 v[80:83], v[200:203], a[232:235], v[80:83]\n"
"buffer_load_dwordx4 v217, s[44:47], s64 offen offset:1024 nt lds\n"
"v_mfma_f32_16x16x32_bf16 v[80:83], v[204:207], a[248:251], v[80:83]\n"
"v_mov_b32 v143, v140\n"
"v_mov_b32 v156, v148\n"
"v_mfma_f32_16x16x32_bf16 v[88:91], v[204:207], a[252:255], v[88:91]\n"
"ds_read_b128 v[204:207], v231 offset:30848\n"
"v_permlane32_swap_b32 v148, v156\n"
"v_mfma_f32_16x16x32_bf16 v[88:91], v[200:203], a[236:239], v[88:91]\n"
"s_waitcnt lgkmcnt(3)\n"
"ds_read_b128 v[200:203], v230 offset:30848\n"
"v_mov_b32 v149, v148\n"
"v_mfma_f32_16x16x32_bf16 v[64:67], v[208:211], v[128:131], v[64:67]\n"
"v_mov_b32 v150, v148\n"
"v_mov_b32 v151, v148\n"
"v_mfma_f32_16x16x32_bf16 v[64:67], v[212:215], v[160:163], v[64:67]\n"
"v_mov_b32 v157, v156\n"
"v_mov_b32 v158, v156\n"
"v_mfma_f32_16x16x32_bf16 v[72:75], v[212:215], v[168:171], v[72:75]\n"
"v_mov_b32 v159, v156\n"
"v_add_f32 v0, v0, v132\n"
"v_mfma_f32_16x16x32_bf16 v[72:75], v[208:211], v[136:139], v[72:75]\n"
"v_exp_f32 v0, v0\n"
"v_mfma_f32_16x16x32_bf16 v[80:83], v[208:211], v[144:147], v[80:83]\n"
"v_add_f32 v1, v1, v132\n"
"v_add_f32 v2, v2, v132\n"
"v_mfma_f32_16x16x32_bf16 v[80:83], v[212:215], v[176:179], v[80:83]\n"
"v_exp_f32 v1, v1\n"
"v_mfma_f32_16x16x32_bf16 v[88:91], v[212:215], v[184:187], v[88:91]\n"
"ds_read_b128 v[212:215], v231 offset:30976\n"
"v_add_f32 v3, v3, v132\n"
"v_mfma_f32_16x16x32_bf16 v[88:91], v[208:211], v[152:155], v[88:91]\n"
"s_waitcnt lgkmcnt(3)\n"
"ds_read_b128 v[208:211], v230 offset:30976\n"
"v_add_f32 v4, v4, v132\n"
"v_mfma_f32_16x16x32_bf16 v[68:71], v[192:195], a[192:195], 0\n"
"v_exp_f32 v2, v2\n"
"v_mfma_f32_16x16x32_bf16 v[68:71], v[196:199], a[208:211], v[68:71]\n"
"v_exp_f32 v3, v3\n"
"v_mfma_f32_16x16x32_bf16 v[76:79], v[196:199], a[212:215], 0\n"
"buffer_load_dwordx4 v218, s[44:47], s64 offen offset:2048 nt lds\n"
"v_mfma_f32_16x16x32_bf16 v[76:79], v[192:195], a[196:199], v[76:79]\n"
"v_exp_f32 v4, v4\n"
"v_mfma_f32_16x16x32_bf16 v[84:87], v[192:195], a[200:203], 0\n"
"v_add_f32 v5, v5, v132\n"
"v_add_f32 v6, v6, v132\n"
"v_mfma_f32_16x16x32_bf16 v[84:87], v[196:199], a[216:219], v[84:87]\n"
"v_exp_f32 v5, v5\n"
"v_mfma_f32_16x16x32_bf16 v[92:95], v[196:199], a[220:223], 0\n"
"ds_read_b128 v[196:199], v231 offset:36864\n"
"v_add_f32 v7, v7, v132\n"
"v_mfma_f32_16x16x32_bf16 v[92:95], v[192:195], a[204:207], v[92:95]\n"
"s_waitcnt lgkmcnt(3)\n"
"ds_read_b128 v[192:195], v230 offset:36864\n"
"v_add_f32 v242, v242, v0\n"
"v_mfma_f32_16x16x32_bf16 v[68:71], v[200:203], a[224:227], v[68:71]\n"
"v_exp_f32 v6, v6\n"
"v_mfma_f32_16x16x32_bf16 v[68:71], v[204:207], a[240:243], v[68:71]\n"
"v_exp_f32 v7, v7\n"
"v_mfma_f32_16x16x32_bf16 v[76:79], v[204:207], a[244:247], v[76:79]\n"
"v_add_f32 v243, v243, v1\n"
"v_cvt_pk_bf16_f32 v0, v0, v1\n"
"v_mfma_f32_16x16x32_bf16 v[76:79], v[200:203], a[228:231], v[76:79]\n"
"v_add_f32 v242, v242, v2\n"
"v_add_f32 v243, v243, v3\n"
"v_mfma_f32_16x16x32_bf16 v[84:87], v[200:203], a[232:235], v[84:87]\n"
"buffer_load_dwordx4 v219, s[44:47], s64 offen offset:3072 nt lds\n"
"v_mfma_f32_16x16x32_bf16 v[84:87], v[204:207], a[248:251], v[84:87]\n"
"v_cvt_pk_bf16_f32 v1, v2, v3\n"
"v_cvt_pk_bf16_f32 v2, v4, v5\n"
"v_mfma_f32_16x16x32_bf16 v[92:95], v[204:207], a[252:255], v[92:95]\n"
"ds_read_b128 v[204:207], v231 offset:36992\n"
"v_cvt_pk_bf16_f32 v3, v6, v7\n"
"v_mfma_f32_16x16x32_bf16 v[92:95], v[200:203], a[236:239], v[92:95]\n"
"s_waitcnt lgkmcnt(3)\n"
"ds_read_b128 v[200:203], v230 offset:36992\n"
"v_add_f32 v242, v242, v4\n"
"v_mfma_f32_16x16x32_bf16 v[68:71], v[208:211], v[128:131], v[68:71]\n"
"v_add_f32 v243, v243, v5\n"
"v_add_f32 v242, v242, v6\n"
"v_mfma_f32_16x16x32_bf16 v[68:71], v[212:215], v[160:163], v[68:71]\n"
"v_add_f32 v243, v243, v7\n"
"v_add_f32 v32, v32, v132\n"
"v_mfma_f32_16x16x32_bf16 v[76:79], v[212:215], v[168:171], v[76:79]\n"
"v_exp_f32 v32, v32\n"
"v_mfma_f32_16x16x32_bf16 v[76:79], v[208:211], v[136:139], v[76:79]\n"
"v_add_f32 v33, v33, v132\n"
"v_add_f32 v34, v34, v132\n"
"v_mfma_f32_16x16x32_bf16 v[84:87], v[208:211], v[144:147], v[84:87]\n"
"v_exp_f32 v33, v33\n"
"v_mfma_f32_16x16x32_bf16 v[84:87], v[212:215], v[176:179], v[84:87]\n"
"v_exp_f32 v34, v34\n"
"v_mfma_f32_16x16x32_bf16 v[92:95], v[212:215], v[184:187], v[92:95]\n"
"ds_read_b128 v[212:215], v231 offset:37120\n"
"v_add_f32 v35, v35, v132\n"
"v_mfma_f32_16x16x32_bf16 v[92:95], v[208:211], v[152:155], v[92:95]\n"
"s_waitcnt lgkmcnt(3)\n"
"ds_read_b128 v[208:211], v230 offset:37120\n"
"v_add_f32 v36, v36, v132\n"
"v_mfma_f32_16x16x32_bf16 v[96:99], v[192:195], a[192:195], v[132:135]\n"
"v_exp_f32 v35, v35\n"
"v_mfma_f32_16x16x32_bf16 v[96:99], v[196:199], a[208:211], v[96:99]\n"
"s_add_u32 m0, s73, 0x1000\n"
"v_exp_f32 v36, v36\n"
"v_mfma_f32_16x16x32_bf16 v[104:107], v[196:199], a[212:215], v[140:143]\n"
"buffer_load_dwordx4 v220, s[44:47], s64 offen offset:0 nt lds\n"
"v_mfma_f32_16x16x32_bf16 v[104:107], v[192:195], a[196:199], v[104:107]\n"
"v_add_f32 v37, v37, v132\n"
"v_add_f32 v38, v38, v132\n"
"v_mfma_f32_16x16x32_bf16 v[112:115], v[192:195], a[200:203], v[148:151]\n"
"v_exp_f32 v37, v37\n"
"v_mfma_f32_16x16x32_bf16 v[112:115], v[196:199], a[216:219], v[112:115]\n"
"v_exp_f32 v38, v38\n"
"v_mfma_f32_16x16x32_bf16 v[120:123], v[196:199], a[220:223], v[156:159]\n"
"ds_read_b128 v[196:199], v231 offset:43008\n"
"v_add_f32 v39, v39, v132\n"
"v_mfma_f32_16x16x32_bf16 v[120:123], v[192:195], a[204:207], v[120:123]\n"
"s_waitcnt lgkmcnt(3)\n"
"ds_read_b128 v[192:195], v230 offset:43008\n"
"v_add_f32 v242, v242, v32\n"
"v_mfma_f32_16x16x32_bf16 v[96:99], v[200:203], a[224:227], v[96:99]\n"
"v_exp_f32 v39, v39\n"
"v_mfma_f32_16x16x32_bf16 v[96:99], v[204:207], a[240:243], v[96:99]\n"
"v_add_f32 v243, v243, v33\n"
"v_cvt_pk_bf16_f32 v32, v32, v33\n"
"v_mfma_f32_16x16x32_bf16 v[104:107], v[204:207], a[244:247], v[104:107]\n"
"v_add_f32 v242, v242, v34\n"
"v_add_f32 v243, v243, v35\n"
"v_mfma_f32_16x16x32_bf16 v[104:107], v[200:203], a[228:231], v[104:107]\n"
"v_cvt_pk_bf16_f32 v33, v34, v35\n"
"v_cvt_pk_bf16_f32 v34, v36, v37\n"
"v_mfma_f32_16x16x32_bf16 v[112:115], v[200:203], a[232:235], v[112:115]\n"
"buffer_load_dwordx4 v221, s[44:47], s64 offen offset:1024 nt lds\n"
"v_mfma_f32_16x16x32_bf16 v[112:115], v[204:207], a[248:251], v[112:115]\n"
"v_cvt_pk_bf16_f32 v35, v38, v39\n"
"v_add_f32 v8, v8, v140\n"
"v_mfma_f32_16x16x32_bf16 v[120:123], v[204:207], a[252:255], v[120:123]\n"
"ds_read_b128 v[204:207], v231 offset:43136\n"
"v_add_f32 v9, v9, v140\n"
"v_mfma_f32_16x16x32_bf16 v[120:123], v[200:203], a[236:239], v[120:123]\n"
"s_waitcnt lgkmcnt(3)\n"
"ds_read_b128 v[200:203], v230 offset:43136\n"
"v_add_f32 v10, v10, v140\n"
"v_mfma_f32_16x16x32_bf16 v[96:99], v[208:211], v[128:131], v[96:99]\n"
"s_add_u32 s64, s64, 0x6000\n"
"v_exp_f32 v8, v8\n"
"v_mfma_f32_16x16x32_bf16 v[96:99], v[212:215], v[160:163], v[96:99]\n"
"v_exp_f32 v9, v9\n"
"v_mfma_f32_16x16x32_bf16 v[104:107], v[212:215], v[168:171], v[104:107]\n"
"v_exp_f32 v10, v10\n"
"v_mfma_f32_16x16x32_bf16 v[104:107], v[208:211], v[136:139], v[104:107]\n"
"v_add_f32 v11, v11, v140\n"
"v_add_f32 v12, v12, v140\n"
"v_mfma_f32_16x16x32_bf16 v[112:115], v[208:211], v[144:147], v[112:115]\n"
"v_exp_f32 v11, v11\n"
"v_mfma_f32_16x16x32_bf16 v[112:115], v[212:215], v[176:179], v[112:115]\n"
"v_exp_f32 v12, v12\n"
"v_mfma_f32_16x16x32_bf16 v[120:123], v[212:215], v[184:187], v[120:123]\n"
"ds_read_b128 v[212:215], v231 offset:43264\n"
"v_add_f32 v13, v13, v140\n"
"v_mfma_f32_16x16x32_bf16 v[120:123], v[208:211], v[152:155], v[120:123]\n"
"s_waitcnt lgkmcnt(3)\n"
"ds_read_b128 v[208:211], v230 offset:43264\n"
"v_add_f32 v14, v14, v140\n"
"v_mfma_f32_16x16x32_bf16 v[100:103], v[192:195], a[192:195], v[132:135]\n"
"v_exp_f32 v13, v13\n"
"v_mfma_f32_16x16x32_bf16 v[100:103], v[196:199], a[208:211], v[100:103]\n"
"v_exp_f32 v14, v14\n"
"v_mfma_f32_16x16x32_bf16 v[108:111], v[196:199], a[212:215], v[140:143]\n"
"v_add_f32 v15, v15, v140\n"
"v_add_f32 v244, v244, v8\n"
"v_mfma_f32_16x16x32_bf16 v[108:111], v[192:195], a[196:199], v[108:111]\n"
"v_exp_f32 v15, v15\n"
"v_mfma_f32_16x16x32_bf16 v[116:119], v[192:195], a[200:203], v[148:151]\n"
"v_add_f32 v245, v245, v9\n"
"v_add_f32 v244, v244, v10\n"
"v_mfma_f32_16x16x32_bf16 v[116:119], v[196:199], a[216:219], v[116:119]\n"
"v_add_f32 v245, v245, v11\n"
"v_add_f32 v244, v244, v12\n"
"v_mfma_f32_16x16x32_bf16 v[124:127], v[196:199], a[220:223], v[156:159]\n"
"s_waitcnt vmcnt(22) lgkmcnt(0)\n"
"s_barrier\n"
"ds_read_b64_tr_b16 v[196:197], v232 offset:12288\n"
"v_mfma_f32_16x16x32_bf16 v[124:127], v[192:195], a[204:207], v[124:127]\n"
"ds_read_b64_tr_b16 v[198:199], v232 offset:18432\n"
"ds_read_b64_tr_b16 v[192:193], v232 offset:0\n"
"v_mfma_f32_16x16x32_bf16 v[100:103], v[200:203], a[224:227], v[100:103]\n"
"ds_read_b64_tr_b16 v[194:195], v232 offset:6144\n"
"v_add_f32 v245, v245, v13\n"
"v_mfma_f32_16x16x32_bf16 v[100:103], v[204:207], a[240:243], v[100:103]\n"
"v_add_f32 v244, v244, v14\n"
"v_add_f32 v245, v245, v15\n"
"v_mfma_f32_16x16x32_bf16 v[108:111], v[204:207], a[244:247], v[108:111]\n"
"v_add_f32 v40, v40, v140\n"
"v_add_f32 v41, v41, v140\n"
"v_mfma_f32_16x16x32_bf16 v[108:111], v[200:203], a[228:231], v[108:111]\n"
"v_exp_f32 v40, v40\n"
"v_mfma_f32_16x16x32_bf16 v[116:119], v[200:203], a[232:235], v[116:119]\n"
"v_exp_f32 v41, v41\n"
"v_mfma_f32_16x16x32_bf16 v[116:119], v[204:207], a[248:251], v[116:119]\n"
"v_add_f32 v42, v42, v140\n"
"v_add_f32 v43, v43, v140\n"
"v_mfma_f32_16x16x32_bf16 v[124:127], v[204:207], a[252:255], v[124:127]\n"
"ds_read_b64_tr_b16 v[204:205], v233 offset:12288\n"
"v_add_f32 v44, v44, v140\n"
"v_mfma_f32_16x16x32_bf16 v[124:127], v[200:203], a[236:239], v[124:127]\n"
"ds_read_b64_tr_b16 v[206:207], v233 offset:18432\n"
"ds_read_b64_tr_b16 v[200:201], v233 offset:0\n"
"v_mfma_f32_16x16x32_bf16 v[100:103], v[208:211], v[128:131], v[100:103]\n"
"ds_read_b64_tr_b16 v[202:203], v233 offset:6144\n"
"v_add_f32 v45, v45, v140\n"
"v_mfma_f32_16x16x32_bf16 v[100:103], v[212:215], v[160:163], v[100:103]\n"
"v_exp_f32 v42, v42\n"
"v_mfma_f32_16x16x32_bf16 v[108:111], v[212:215], v[168:171], v[108:111]\n"
"v_exp_f32 v43, v43\n"
"v_mfma_f32_16x16x32_bf16 v[108:111], v[208:211], v[136:139], v[108:111]\n"
"v_exp_f32 v44, v44\n"
"v_mfma_f32_16x16x32_bf16 v[116:119], v[208:211], v[144:147], v[116:119]\n"
"v_exp_f32 v45, v45\n"
"v_mfma_f32_16x16x32_bf16 v[116:119], v[212:215], v[176:179], v[116:119]\n"
"v_add_f32 v46, v46, v140\n"
"v_add_f32 v47, v47, v140\n"
"v_mfma_f32_16x16x32_bf16 v[124:127], v[212:215], v[184:187], v[124:127]\n"
"ds_read_b64_tr_b16 v[212:213], v234 offset:12288\n"
"v_add_f32 v244, v244, v40\n"
"v_mfma_f32_16x16x32_bf16 v[124:127], v[208:211], v[152:155], v[124:127]\n"
"s_waitcnt lgkmcnt(5)\n"
"ds_read_b64_tr_b16 v[214:215], v234 offset:18432\n"
"ds_read_b64_tr_b16 v[208:209], v234 offset:0\n"
"v_mfma_f32_16x16x32_bf16 a[0:3], v[192:195], v[0:3], 0\n"
"ds_read_b64_tr_b16 v[210:211], v234 offset:6144\n"
"v_exp_f32 v46, v46\n"
"v_exp_f32 v47, v47\n"
"v_add_f32 v245, v245, v41\n"
"v_cvt_pk_bf16_f32 v40, v40, v41\n"
"v_add_f32 v244, v244, v42\n"
"v_add_f32 v245, v245, v43\n"
"v_cvt_pk_bf16_f32 v41, v42, v43\n"
"v_cvt_pk_bf16_f32 v42, v44, v45\n"
"v_cvt_pk_bf16_f32 v43, v46, v47\n"
"v_mfma_f32_16x16x32_bf16 a[0:3], v[196:199], v[32:35], a[0:3]\n"
"s_add_u32 m0, s73, 0x6000\n"
"v_cvt_pk_bf16_f32 v8, v8, v9\n"
"v_cvt_pk_bf16_f32 v9, v10, v11\n"
"v_cvt_pk_bf16_f32 v10, v12, v13\n"
"v_cvt_pk_bf16_f32 v11, v14, v15\n"
"v_mfma_f32_16x16x32_bf16 a[4:7], v[196:199], v[40:43], 0\n"
"buffer_load_dwordx4 v216, s[44:47], s64 offen offset:0 nt lds\n"
"v_add_f32 v16, v16, v148\n"
"v_add_f32 v17, v17, v148\n"
"v_add_f32 v18, v18, v148\n"
"v_add_f32 v19, v19, v148\n"
"v_add_f32 v20, v20, v148\n"
"v_add_f32 v21, v21, v148\n"
"v_add_f32 v22, v22, v148\n"
"v_add_f32 v23, v23, v148\n"
"v_exp_f32 v16, v16\n"
"v_exp_f32 v17, v17\n"
"v_exp_f32 v18, v18\n"
"v_exp_f32 v19, v19\n"
"v_exp_f32 v20, v20\n"
"v_exp_f32 v21, v21\n"
"v_exp_f32 v22, v22\n"
"v_exp_f32 v23, v23\n"
"v_add_f32 v246, v246, v16\n"
"v_add_f32 v247, v247, v17\n"
"v_cvt_pk_bf16_f32 v16, v16, v17\n"
"v_add_f32 v246, v246, v18\n"
"v_add_f32 v247, v247, v19\n"
"v_cvt_pk_bf16_f32 v17, v18, v19\n"
"v_cvt_pk_bf16_f32 v18, v20, v21\n"
"v_cvt_pk_bf16_f32 v19, v22, v23\n"
"v_mfma_f32_16x16x32_bf16 a[4:7], v[192:195], v[8:11], a[4:7]\n"
"v_add_f32 v246, v246, v20\n"
"v_add_f32 v247, v247, v21\n"
"v_add_f32 v246, v246, v22\n"
"v_add_f32 v247, v247, v23\n"
"v_add_f32 v48, v48, v148\n"
"v_add_f32 v49, v49, v148\n"
"v_add_f32 v50, v50, v148\n"
"v_add_f32 v51, v51, v148\n"
"v_add_f32 v52, v52, v148\n"
"v_add_f32 v53, v53, v148\n"
"v_add_f32 v54, v54, v148\n"
"v_add_f32 v55, v55, v148\n"
"v_exp_f32 v48, v48\n"
"v_exp_f32 v49, v49\n"
"v_exp_f32 v50, v50\n"
"v_exp_f32 v51, v51\n"
"v_exp_f32 v52, v52\n"
"v_exp_f32 v53, v53\n"
"v_exp_f32 v54, v54\n"
"v_exp_f32 v55, v55\n"
"v_add_f32 v246, v246, v48\n"
"v_add_f32 v247, v247, v49\n"
"v_cvt_pk_bf16_f32 v48, v48, v49\n"
"v_add_f32 v246, v246, v50\n"
"v_add_f32 v247, v247, v51\n"
"v_cvt_pk_bf16_f32 v49, v50, v51\n"
"v_cvt_pk_bf16_f32 v50, v52, v53\n"
"v_cvt_pk_bf16_f32 v51, v54, v55\n"
"v_mfma_f32_16x16x32_bf16 a[8:11], v[192:195], v[16:19], 0\n"
"v_add_f32 v24, v24, v156\n"
"v_add_f32 v25, v25, v156\n"
"v_add_f32 v26, v26, v156\n"
"v_add_f32 v27, v27, v156\n"
"v_add_f32 v28, v28, v156\n"
"v_add_f32 v29, v29, v156\n"
"v_add_f32 v30, v30, v156\n"
"v_add_f32 v31, v31, v156\n"
"v_exp_f32 v24, v24\n"
"v_exp_f32 v25, v25\n"
"v_exp_f32 v26, v26\n"
"v_exp_f32 v27, v27\n"
"v_exp_f32 v28, v28\n"
"v_exp_f32 v29, v29\n"
"v_exp_f32 v30, v30\n"
"v_exp_f32 v31, v31\n"
"v_add_f32 v56, v56, v156\n"
"v_add_f32 v57, v57, v156\n"
"v_add_f32 v58, v58, v156\n"
"v_add_f32 v59, v59, v156\n"
"v_add_f32 v60, v60, v156\n"
"v_add_f32 v61, v61, v156\n"
"v_add_f32 v62, v62, v156\n"
"v_add_f32 v63, v63, v156\n"
"v_exp_f32 v56, v56\n"
"v_exp_f32 v57, v57\n"
"v_exp_f32 v58, v58\n"
"v_exp_f32 v59, v59\n"
"v_exp_f32 v60, v60\n"
"v_exp_f32 v61, v61\n"
"v_exp_f32 v62, v62\n"
"v_exp_f32 v63, v63\n"
"v_add_f32 v248, v248, v24\n"
"v_add_f32 v249, v249, v25\n"
"v_add_f32 v248, v248, v26\n"
"v_add_f32 v249, v249, v27\n"
"v_add_f32 v248, v248, v28\n"
"v_add_f32 v249, v249, v29\n"
"v_add_f32 v248, v248, v30\n"
"v_add_f32 v249, v249, v31\n"
"v_add_f32 v248, v248, v56\n"
"v_add_f32 v249, v249, v57\n"
"v_cvt_pk_bf16_f32 v56, v56, v57\n"
"v_add_f32 v248, v248, v58\n"
"v_add_f32 v249, v249, v59\n"
"v_cvt_pk_bf16_f32 v57, v58, v59\n"
"v_cvt_pk_bf16_f32 v58, v60, v61\n"
"v_cvt_pk_bf16_f32 v59, v62, v63\n"
"v_mfma_f32_16x16x32_bf16 a[8:11], v[196:199], v[48:51], a[8:11]\n"
"v_cvt_pk_bf16_f32 v24, v24, v25\n"
"v_cvt_pk_bf16_f32 v25, v26, v27\n"
"v_cvt_pk_bf16_f32 v26, v28, v29\n"
"v_cvt_pk_bf16_f32 v27, v30, v31\n"
"v_mfma_f32_16x16x32_bf16 a[12:15], v[196:199], v[56:59], 0\n"
"ds_read_b64_tr_b16 v[196:197], v235 offset:12288\n"
"v_add_f32 v242, v242, v36\n"
"v_mfma_f32_16x16x32_bf16 a[12:15], v[192:195], v[24:27], a[12:15]\n"
"s_waitcnt lgkmcnt(5)\n"
"ds_read_b64_tr_b16 v[198:199], v235 offset:18432\n"
"ds_read_b64_tr_b16 v[192:193], v235 offset:0\n"
"v_mfma_f32_16x16x32_bf16 a[16:19], v[200:203], v[0:3], 0\n"
"ds_read_b64_tr_b16 v[194:195], v235 offset:6144\n"
"v_add_f32 v243, v243, v37\n"
"v_mfma_f32_16x16x32_bf16 a[16:19], v[204:207], v[32:35], a[16:19]\n"
"v_add_f32 v242, v242, v38\n"
"v_add_f32 v243, v243, v39\n"
"v_mfma_f32_16x16x32_bf16 a[20:23], v[204:207], v[40:43], 0\n"
"v_add_f32 v244, v244, v44\n"
"v_add_f32 v245, v245, v45\n"
"v_mfma_f32_16x16x32_bf16 a[20:23], v[200:203], v[8:11], a[20:23]\n"
"v_add_f32 v244, v244, v46\n"
"v_add_f32 v245, v245, v47\n"
"v_mfma_f32_16x16x32_bf16 a[24:27], v[200:203], v[16:19], 0\n"
"buffer_load_dwordx4 v217, s[44:47], s64 offen offset:1024 nt lds\n"
"v_mfma_f32_16x16x32_bf16 a[24:27], v[204:207], v[48:51], a[24:27]\n"
"v_add_f32 v246, v246, v52\n"
"v_add_f32 v247, v247, v53\n"
"v_mfma_f32_16x16x32_bf16 a[28:31], v[204:207], v[56:59], 0\n"
"ds_read_b64_tr_b16 v[204:205], v232 offset:12416\n"
"v_add_f32 v246, v246, v54\n"
"v_mfma_f32_16x16x32_bf16 a[28:31], v[200:203], v[24:27], a[28:31]\n"
"s_waitcnt lgkmcnt(5)\n"
"ds_read_b64_tr_b16 v[206:207], v232 offset:18560\n"
"ds_read_b64_tr_b16 v[200:201], v232 offset:128\n"
"v_mfma_f32_16x16x32_bf16 a[32:35], v[208:211], v[0:3], 0\n"
"ds_read_b64_tr_b16 v[202:203], v232 offset:6272\n"
"v_add_f32 v247, v247, v55\n"
"v_mfma_f32_16x16x32_bf16 a[32:35], v[212:215], v[32:35], a[32:35]\n"
"v_add_f32 v248, v248, v60\n"
"v_add_f32 v249, v249, v61\n"
"v_mfma_f32_16x16x32_bf16 a[36:39], v[212:215], v[40:43], 0\n"
"v_add_f32 v248, v248, v62\n"
"v_add_f32 v249, v249, v63\n"
"v_mfma_f32_16x16x32_bf16 a[36:39], v[208:211], v[8:11], a[36:39]\n"
"v_exp_f32 v96, v96\n"
"v_mfma_f32_16x16x32_bf16 a[40:43], v[208:211], v[16:19], 0\n"
"v_exp_f32 v97, v97\n"
"v_mfma_f32_16x16x32_bf16 a[40:43], v[212:215], v[48:51], a[40:43]\n"
"v_exp_f32 v98, v98\n"
"v_mfma_f32_16x16x32_bf16 a[44:47], v[212:215], v[56:59], 0\n"
"ds_read_b64_tr_b16 v[212:213], v233 offset:12416\n"
"v_add_f32 v64, v64, v132\n"
"v_mfma_f32_16x16x32_bf16 a[44:47], v[208:211], v[24:27], a[44:47]\n"
"s_waitcnt lgkmcnt(5)\n"
"ds_read_b64_tr_b16 v[214:215], v233 offset:18560\n"
"ds_read_b64_tr_b16 v[208:209], v233 offset:128\n"
"v_mfma_f32_16x16x32_bf16 a[48:51], v[192:195], v[0:3], 0\n"
"ds_read_b64_tr_b16 v[210:211], v233 offset:6272\n"
"v_add_f32 v65, v65, v132\n"
"v_mfma_f32_16x16x32_bf16 a[48:51], v[196:199], v[32:35], a[48:51]\n"
"v_exp_f32 v64, v64\n"
"v_mfma_f32_16x16x32_bf16 a[52:55], v[196:199], v[40:43], 0\n"
"buffer_load_dwordx4 v218, s[44:47], s64 offen offset:2048 nt lds\n"
"v_mfma_f32_16x16x32_bf16 a[52:55], v[192:195], v[8:11], a[52:55]\n"
"v_exp_f32 v65, v65\n"
"v_mfma_f32_16x16x32_bf16 a[56:59], v[192:195], v[16:19], 0\n"
"v_exp_f32 v99, v99\n"
"v_mfma_f32_16x16x32_bf16 a[56:59], v[196:199], v[48:51], a[56:59]\n"
"v_exp_f32 v100, v100\n"
"v_mfma_f32_16x16x32_bf16 a[60:63], v[196:199], v[56:59], 0\n"
"ds_read_b64_tr_b16 v[196:197], v234 offset:12416\n"
"v_add_f32 v66, v66, v132\n"
"v_mfma_f32_16x16x32_bf16 a[60:63], v[192:195], v[24:27], a[60:63]\n"
"s_waitcnt lgkmcnt(5)\n"
"ds_read_b64_tr_b16 v[198:199], v234 offset:18560\n"
"ds_read_b64_tr_b16 v[192:193], v234 offset:128\n"
"v_mfma_f32_16x16x32_bf16 a[64:67], v[200:203], v[0:3], 0\n"
"ds_read_b64_tr_b16 v[194:195], v234 offset:6272\n"
"v_add_f32 v67, v67, v132\n"
"v_mfma_f32_16x16x32_bf16 a[64:67], v[204:207], v[32:35], a[64:67]\n"
"v_exp_f32 v66, v66\n"
"v_mfma_f32_16x16x32_bf16 a[68:71], v[204:207], v[40:43], 0\n"
"v_exp_f32 v67, v67\n"
"v_mfma_f32_16x16x32_bf16 a[68:71], v[200:203], v[8:11], a[68:71]\n"
"v_exp_f32 v101, v101\n"
"v_mfma_f32_16x16x32_bf16 a[72:75], v[200:203], v[16:19], 0\n"
"buffer_load_dwordx4 v219, s[44:47], s64 offen offset:3072 nt lds\n"
"v_mfma_f32_16x16x32_bf16 a[72:75], v[204:207], v[48:51], a[72:75]\n"
"v_exp_f32 v102, v102\n"
"v_mfma_f32_16x16x32_bf16 a[76:79], v[204:207], v[56:59], 0\n"
"ds_read_b64_tr_b16 v[204:205], v235 offset:12416\n"
"v_add_f32 v68, v68, v132\n"
"v_mfma_f32_16x16x32_bf16 a[76:79], v[200:203], v[24:27], a[76:79]\n"
"s_waitcnt lgkmcnt(5)\n"
"ds_read_b64_tr_b16 v[206:207], v235 offset:18560\n"
"ds_read_b64_tr_b16 v[200:201], v235 offset:128\n"
"v_mfma_f32_16x16x32_bf16 a[80:83], v[208:211], v[0:3], 0\n"
"ds_read_b64_tr_b16 v[202:203], v235 offset:6272\n"
"v_add_f32 v69, v69, v132\n"
"v_mfma_f32_16x16x32_bf16 a[80:83], v[212:215], v[32:35], a[80:83]\n"
"v_exp_f32 v68, v68\n"
"v_mfma_f32_16x16x32_bf16 a[84:87], v[212:215], v[40:43], 0\n"
"v_exp_f32 v69, v69\n"
"v_mfma_f32_16x16x32_bf16 a[84:87], v[208:211], v[8:11], a[84:87]\n"
"v_exp_f32 v103, v103\n"
"v_mfma_f32_16x16x32_bf16 a[88:91], v[208:211], v[16:19], 0\n"
"v_exp_f32 v104, v104\n"
"v_mfma_f32_16x16x32_bf16 a[88:91], v[212:215], v[48:51], a[88:91]\n"
"v_exp_f32 v105, v105\n"
"v_mfma_f32_16x16x32_bf16 a[92:95], v[212:215], v[56:59], 0\n"
"ds_read_b64_tr_b16 v[212:213], v232 offset:12544\n"
"v_add_f32 v70, v70, v132\n"
"v_mfma_f32_16x16x32_bf16 a[92:95], v[208:211], v[24:27], a[92:95]\n"
"s_waitcnt lgkmcnt(5)\n"
"ds_read_b64_tr_b16 v[214:215], v232 offset:18688\n"
"ds_read_b64_tr_b16 v[208:209], v232 offset:256\n"
"v_mfma_f32_16x16x32_bf16 a[96:99], v[192:195], v[0:3], 0\n"
"ds_read_b64_tr_b16 v[210:211], v232 offset:6400\n"
"v_add_f32 v71, v71, v132\n"
"v_mfma_f32_16x16x32_bf16 a[96:99], v[196:199], v[32:35], a[96:99]\n"
"s_add_u32 m0, s73, 0x7000\n"
"v_exp_f32 v70, v70\n"
"v_mfma_f32_16x16x32_bf16 a[100:103], v[196:199], v[40:43], 0\n"
"buffer_load_dwordx4 v220, s[44:47], s64 offen offset:0 nt lds\n"
"v_mfma_f32_16x16x32_bf16 a[100:103], v[192:195], v[8:11], a[100:103]\n"
"v_exp_f32 v71, v71\n"
"v_mfma_f32_16x16x32_bf16 a[104:107], v[192:195], v[16:19], 0\n"
"v_exp_f32 v106, v106\n"
"v_mfma_f32_16x16x32_bf16 a[104:107], v[196:199], v[48:51], a[104:107]\n"
"v_exp_f32 v107, v107\n"
"v_mfma_f32_16x16x32_bf16 a[108:111], v[196:199], v[56:59], 0\n"
"ds_read_b64_tr_b16 v[196:197], v233 offset:12544\n"
"v_add_f32 v242, v242, v64\n"
"v_mfma_f32_16x16x32_bf16 a[108:111], v[192:195], v[24:27], a[108:111]\n"
"s_waitcnt lgkmcnt(5)\n"
"ds_read_b64_tr_b16 v[198:199], v233 offset:18688\n"
"ds_read_b64_tr_b16 v[192:193], v233 offset:256\n"
"v_mfma_f32_16x16x32_bf16 a[112:115], v[200:203], v[0:3], 0\n"
"ds_read_b64_tr_b16 v[194:195], v233 offset:6400\n"
"v_add_f32 v243, v243, v65\n"
"v_mfma_f32_16x16x32_bf16 a[112:115], v[204:207], v[32:35], a[112:115]\n"
"v_exp_f32 v108, v108\n"
"v_mfma_f32_16x16x32_bf16 a[116:119], v[204:207], v[40:43], 0\n"
"v_exp_f32 v109, v109\n"
"v_mfma_f32_16x16x32_bf16 a[116:119], v[200:203], v[8:11], a[116:119]\n"
"v_exp_f32 v110, v110\n"
"v_mfma_f32_16x16x32_bf16 a[120:123], v[200:203], v[16:19], 0\n"
"buffer_load_dwordx4 v221, s[44:47], s64 offen offset:1024 nt lds\n"
"v_mfma_f32_16x16x32_bf16 a[120:123], v[204:207], v[48:51], a[120:123]\n"
"v_exp_f32 v111, v111\n"
"v_mfma_f32_16x16x32_bf16 a[124:127], v[204:207], v[56:59], 0\n"
"ds_read_b64_tr_b16 v[204:205], v234 offset:12544\n"
"v_cvt_pk_bf16_f32 v64, v64, v65\n"
"v_mfma_f32_16x16x32_bf16 a[124:127], v[200:203], v[24:27], a[124:127]\n"
"s_waitcnt lgkmcnt(5)\n"
"ds_read_b64_tr_b16 v[206:207], v234 offset:18688\n"
"ds_read_b64_tr_b16 v[200:201], v234 offset:256\n"
"v_mfma_f32_16x16x32_bf16 a[128:131], v[208:211], v[0:3], 0\n"
"s_add_u32 s64, s64, 0x6000\n"
"ds_read_b64_tr_b16 v[202:203], v234 offset:6400\n"
"v_add_f32 v242, v242, v66\n"
"v_mfma_f32_16x16x32_bf16 a[128:131], v[212:215], v[32:35], a[128:131]\n"
"s_mul_i32 s58, s23, 0x3c000\n"
"s_add_u32 m0, s87, 0x22000\n"
"v_add_f32 v243, v243, v67\n"
"v_mfma_f32_16x16x32_bf16 a[132:135], v[212:215], v[40:43], 0\n"
"s_add_u32 s58, s58, 0x28000\n"
"buffer_load_dwordx4 v222, s[40:43], s58 offen offset:0 lds\n"
"v_mfma_f32_16x16x32_bf16 a[132:135], v[208:211], v[8:11], a[132:135]\n"
"v_exp_f32 v112, v112\n"
"v_mfma_f32_16x16x32_bf16 a[136:139], v[208:211], v[16:19], 0\n"
"v_exp_f32 v113, v113\n"
"v_mfma_f32_16x16x32_bf16 a[136:139], v[212:215], v[48:51], a[136:139]\n"
"v_exp_f32 v114, v114\n"
"v_mfma_f32_16x16x32_bf16 a[140:143], v[212:215], v[56:59], 0\n"
"ds_read_b64_tr_b16 v[212:213], v235 offset:12544\n"
"v_cvt_pk_bf16_f32 v65, v66, v67\n"
"v_mfma_f32_16x16x32_bf16 a[140:143], v[208:211], v[24:27], a[140:143]\n"
"s_waitcnt lgkmcnt(5)\n"
"ds_read_b64_tr_b16 v[214:215], v235 offset:18688\n"
"ds_read_b64_tr_b16 v[208:209], v235 offset:256\n"
"v_mfma_f32_16x16x32_bf16 a[144:147], v[192:195], v[0:3], 0\n"
"ds_read_b64_tr_b16 v[210:211], v235 offset:6400\n"
"v_add_f32 v242, v242, v68\n"
"v_mfma_f32_16x16x32_bf16 a[144:147], v[196:199], v[32:35], a[144:147]\n"
"v_exp_f32 v115, v115\n"
"v_mfma_f32_16x16x32_bf16 a[148:151], v[196:199], v[40:43], 0\n"
"buffer_load_dwordx4 v223, s[40:43], s58 offen offset:1024 lds\n"
"v_mfma_f32_16x16x32_bf16 a[148:151], v[192:195], v[8:11], a[148:151]\n"
"v_exp_f32 v116, v116\n"
"v_mfma_f32_16x16x32_bf16 a[152:155], v[192:195], v[16:19], 0\n"
"v_exp_f32 v117, v117\n"
"v_mfma_f32_16x16x32_bf16 a[152:155], v[196:199], v[48:51], a[152:155]\n"
"v_exp_f32 v118, v118\n"
"v_mfma_f32_16x16x32_bf16 a[156:159], v[196:199], v[56:59], 0\n"
"s_waitcnt vmcnt(8) lgkmcnt(0)\n"
"s_barrier\n"
"ds_read_b128 v[196:199], v231 offset:0\n"
"v_mfma_f32_16x16x32_bf16 a[156:159], v[192:195], v[24:27], a[156:159]\n"
"ds_read_b128 v[192:195], v230 offset:0\n"
"v_add_f32 v243, v243, v69\n"
"v_mfma_f32_16x16x32_bf16 a[160:163], v[200:203], v[0:3], 0\n"
"v_exp_f32 v119, v119\n"
"v_mfma_f32_16x16x32_bf16 a[160:163], v[204:207], v[32:35], a[160:163]\n"
"v_exp_f32 v120, v120\n"
"v_mfma_f32_16x16x32_bf16 a[164:167], v[204:207], v[40:43], 0\n"
"buffer_load_dwordx4 v224, s[40:43], s58 offen offset:2048 lds\n"
"v_mfma_f32_16x16x32_bf16 a[164:167], v[200:203], v[8:11], a[164:167]\n"
"v_exp_f32 v121, v121\n"
"v_mfma_f32_16x16x32_bf16 a[168:171], v[200:203], v[16:19], 0\n"
"Ltp6%=:\n"
"s_cbranch_execz Ltp8%=\n"
"buffer_load_dwordx4 v225, s[40:43], s58 offen offset:3072 lds\n"
"v_mfma_f32_16x16x32_bf16 a[168:171], v[204:207], v[48:51], a[168:171]\n"
"v_exp_f32 v122, v122\n"
"v_mfma_f32_16x16x32_bf16 a[172:175], v[204:207], v[56:59], 0\n"
"ds_read_b128 v[204:207], v231 offset:128\n"
"v_cvt_pk_bf16_f32 v66, v68, v69\n"
"v_mfma_f32_16x16x32_bf16 a[172:175], v[200:203], v[24:27], a[172:175]\n"
"ds_read_b128 v[200:203], v230 offset:128\n"
"v_add_f32 v242, v242, v70\n"
"v_mfma_f32_16x16x32_bf16 a[176:179], v[208:211], v[0:3], 0\n"
"v_exp_f32 v123, v123\n"
"v_mfma_f32_16x16x32_bf16 a[176:179], v[212:215], v[32:35], a[176:179]\n"
"s_add_u32 m0, s87, 0x23000\n"
"v_exp_f32 v124, v124\n"
"v_mfma_f32_16x16x32_bf16 a[180:183], v[212:215], v[40:43], 0\n"
"buffer_load_dwordx4 v226, s[40:43], s58 offen offset:0 lds\n"
"v_mfma_f32_16x16x32_bf16 a[180:183], v[208:211], v[8:11], a[180:183]\n"
"v_exp_f32 v125, v125\n"
"v_mfma_f32_16x16x32_bf16 a[184:187], v[208:211], v[16:19], 0\n"
"v_exp_f32 v126, v126\n"
"v_mfma_f32_16x16x32_bf16 a[184:187], v[212:215], v[48:51], a[184:187]\n"
"v_exp_f32 v127, v127\n"
"v_mfma_f32_16x16x32_bf16 a[188:191], v[212:215], v[56:59], 0\n"
"ds_read_b128 v[212:215], v231 offset:256\n"
"v_add_f32 v243, v243, v71\n"
"v_mfma_f32_16x16x32_bf16 a[188:191], v[208:211], v[24:27], a[188:191]\n"
"s_waitcnt lgkmcnt(3)\n"
"ds_read_b128 v[208:211], v230 offset:256\n"
"v_cvt_pk_bf16_f32 v67, v70, v71\n"
"v_mfma_f32_16x16x32_bf16 v[0:3], v[192:195], a[192:195], v[132:135]\n"
"v_add_f32 v72, v72, v140\n"
"v_add_f32 v73, v73, v140\n"
"v_mfma_f32_16x16x32_bf16 v[0:3], v[196:199], a[208:211], v[0:3]\n"
"s_add_u32 m0, s73, 0xc000\n"
"v_exp_f32 v72, v72\n"
"v_mfma_f32_16x16x32_bf16 v[8:11], v[196:199], a[212:215], v[140:143]\n"
"buffer_load_dwordx4 v216, s[48:51], s65 offen offset:0 nt lds\n"
"v_mfma_f32_16x16x32_bf16 v[8:11], v[192:195], a[196:199], v[8:11]\n"
"v_exp_f32 v73, v73\n"
"v_mfma_f32_16x16x32_bf16 v[16:19], v[192:195], a[200:203], v[148:151]\n"
"s_mul_i32 s74, s23, 0x18000\n"
"v_add_f32 v74, v74, v140\n"
"v_add_f32 v75, v75, v140\n"
"v_mfma_f32_16x16x32_bf16 v[16:19], v[196:199], a[216:219], v[16:19]\n"
"s_add_u32 s75, s74, 0x4000\n"
"v_exp_f32 v74, v74\n"
"v_mfma_f32_16x16x32_bf16 v[24:27], v[196:199], a[220:223], v[156:159]\n"
"s_add_u32 s76, s74, 0x8000\n"
"ds_read_b128 v[196:199], v231 offset:6144\n"
"v_add_f32 v76, v76, v140\n"
"v_mfma_f32_16x16x32_bf16 v[24:27], v[192:195], a[204:207], v[24:27]\n"
"s_waitcnt lgkmcnt(3)\n"
"ds_read_b128 v[192:195], v230 offset:6144\n"
"v_add_f32 v77, v77, v140\n"
"v_mfma_f32_16x16x32_bf16 v[0:3], v[200:203], a[224:227], v[0:3]\n"
"s_add_u32 s77, s74, 0xc000\n"
"v_exp_f32 v75, v75\n"
"v_mfma_f32_16x16x32_bf16 v[0:3], v[204:207], a[240:243], v[0:3]\n"
"s_add_u32 s78, s74, 0x10000\n"
"v_exp_f32 v76, v76\n"
"v_mfma_f32_16x16x32_bf16 v[8:11], v[204:207], a[244:247], v[8:11]\n"
"s_add_u32 s79, s74, 0x14000\n"
"v_exp_f32 v77, v77\n"
"v_mfma_f32_16x16x32_bf16 v[8:11], v[200:203], a[228:231], v[8:11]\n"
"v_add_f32 v78, v78, v140\n"
"v_add_f32 v79, v79, v140\n"
"v_mfma_f32_16x16x32_bf16 v[16:19], v[200:203], a[232:235], v[16:19]\n"
"s_mov_b32 s68, 0x3c000\n"
"buffer_load_dwordx4 v217, s[48:51], s65 offen offset:1024 nt lds\n"
"v_mfma_f32_16x16x32_bf16 v[16:19], v[204:207], a[248:251], v[16:19]\n"
"v_exp_f32 v78, v78\n"
"v_mfma_f32_16x16x32_bf16 v[24:27], v[204:207], a[252:255], v[24:27]\n"
"ds_read_b128 v[204:207], v231 offset:6272\n"
"v_add_f32 v244, v244, v72\n"
"v_mfma_f32_16x16x32_bf16 v[24:27], v[200:203], a[236:239], v[24:27]\n"
"s_waitcnt lgkmcnt(3)\n"
"ds_read_b128 v[200:203], v230 offset:6272\n"
"v_add_f32 v245, v245, v73\n"
"v_mfma_f32_16x16x32_bf16 v[0:3], v[208:211], v[128:131], v[0:3]\n"
"v_exp_f32 v79, v79\n"
"v_mfma_f32_16x16x32_bf16 v[0:3], v[212:215], v[160:163], v[0:3]\n"
"v_cvt_pk_bf16_f32 v72, v72, v73\n"
"v_add_f32 v244, v244, v74\n"
"v_mfma_f32_16x16x32_bf16 v[8:11], v[212:215], v[168:171], v[8:11]\n"
"v_add_f32 v245, v245, v75\n"
"v_cvt_pk_bf16_f32 v73, v74, v75\n"
"v_mfma_f32_16x16x32_bf16 v[8:11], v[208:211], v[136:139], v[8:11]\n"
"v_add_f32 v244, v244, v76\n"
"v_add_f32 v245, v245, v77\n"
"v_mfma_f32_16x16x32_bf16 v[16:19], v[208:211], v[144:147], v[16:19]\n"
"v_cvt_pk_bf16_f32 v74, v76, v77\n"
"v_add_f32 v244, v244, v78\n"
"v_mfma_f32_16x16x32_bf16 v[16:19], v[212:215], v[176:179], v[16:19]\n"
"v_add_f32 v245, v245, v79\n"
"v_cvt_pk_bf16_f32 v75, v78, v79\n"
"v_mfma_f32_16x16x32_bf16 v[24:27], v[212:215], v[184:187], v[24:27]\n"
"ds_read_b128 v[212:215], v231 offset:6400\n"
"v_add_f32 v80, v80, v148\n"
"v_mfma_f32_16x16x32_bf16 v[24:27], v[208:211], v[152:155], v[24:27]\n"
"s_waitcnt lgkmcnt(3)\n"
"ds_read_b128 v[208:211], v230 offset:6400\n"
"v_add_f32 v81, v81, v148\n"
"v_mfma_f32_16x16x32_bf16 v[4:7], v[192:195], a[192:195], v[132:135]\n"
"v_exp_f32 v80, v80\n"
"v_mfma_f32_16x16x32_bf16 v[4:7], v[196:199], a[208:211], v[4:7]\n"
"v_exp_f32 v81, v81\n"
"v_mfma_f32_16x16x32_bf16 v[12:15], v[196:199], a[212:215], v[140:143]\n"
"buffer_load_dwordx4 v218, s[48:51], s65 offen offset:2048 nt lds\n"
"v_mfma_f32_16x16x32_bf16 v[12:15], v[192:195], a[196:199], v[12:15]\n"
"v_add_f32 v82, v82, v148\n"
"v_add_f32 v83, v83, v148\n"
"v_mfma_f32_16x16x32_bf16 v[20:23], v[192:195], a[200:203], v[148:151]\n"
"v_exp_f32 v82, v82\n"
"v_mfma_f32_16x16x32_bf16 v[20:23], v[196:199], a[216:219], v[20:23]\n"
"v_exp_f32 v83, v83\n"
"v_mfma_f32_16x16x32_bf16 v[28:31], v[196:199], a[220:223], v[156:159]\n"
"ds_read_b128 v[196:199], v231 offset:12288\n"
"v_add_f32 v84, v84, v148\n"
"v_mfma_f32_16x16x32_bf16 v[28:31], v[192:195], a[204:207], v[28:31]\n"
"s_waitcnt lgkmcnt(3)\n"
"ds_read_b128 v[192:195], v230 offset:12288\n"
"v_add_f32 v85, v85, v148\n"
"v_mfma_f32_16x16x32_bf16 v[4:7], v[200:203], a[224:227], v[4:7]\n"
"v_exp_f32 v84, v84\n"
"v_mfma_f32_16x16x32_bf16 v[4:7], v[204:207], a[240:243], v[4:7]\n"
"v_exp_f32 v85, v85\n"
"v_mfma_f32_16x16x32_bf16 v[12:15], v[204:207], a[244:247], v[12:15]\n"
"v_add_f32 v86, v86, v148\n"
"v_add_f32 v87, v87, v148\n"
"v_mfma_f32_16x16x32_bf16 v[12:15], v[200:203], a[228:231], v[12:15]\n"
"v_exp_f32 v86, v86\n"
"v_mfma_f32_16x16x32_bf16 v[20:23], v[200:203], a[232:235], v[20:23]\n"
"buffer_load_dwordx4 v219, s[48:51], s65 offen offset:3072 nt lds\n"
"v_mfma_f32_16x16x32_bf16 v[20:23], v[204:207], a[248:251], v[20:23]\n"
"v_exp_f32 v87, v87\n"
"v_mfma_f32_16x16x32_bf16 v[28:31], v[204:207], a[252:255], v[28:31]\n"
"ds_read_b128 v[204:207], v231 offset:12416\n"
"v_add_f32 v246, v246, v80\n"
"v_mfma_f32_16x16x32_bf16 v[28:31], v[200:203], a[236:239], v[28:31]\n"
"s_waitcnt lgkmcnt(3)\n"
"ds_read_b128 v[200:203], v230 offset:12416\n"
"v_add_f32 v247, v247, v81\n"
"v_mfma_f32_16x16x32_bf16 v[4:7], v[208:211], v[128:131], v[4:7]\n"
"s_lshr_b32 s70, s22, 3\n"
"v_cvt_pk_bf16_f32 v80, v80, v81\n"
"v_add_f32 v246, v246, v82\n"
"v_mfma_f32_16x16x32_bf16 v[4:7], v[212:215], v[160:163], v[4:7]\n"
"s_lshl_b32 s70, s70, 2\n"
"v_add_f32 v247, v247, v83\n"
"v_cvt_pk_bf16_f32 v81, v82, v83\n"
"v_mfma_f32_16x16x32_bf16 v[12:15], v[212:215], v[168:171], v[12:15]\n"
"s_add_u32 s70, s70, %[w]\n"
"v_add_f32 v246, v246, v84\n"
"v_add_f32 v247, v247, v85\n"
"v_mfma_f32_16x16x32_bf16 v[12:15], v[208:211], v[136:139], v[12:15]\n"
"v_cvt_pk_bf16_f32 v82, v84, v85\n"
"v_add_f32 v246, v246, v86\n"
"v_mfma_f32_16x16x32_bf16 v[20:23], v[208:211], v[144:147], v[20:23]\n"
"s_mul_i32 s70, s70, 0x1400\n"
"v_add_f32 v247, v247, v87\n"
"v_cvt_pk_bf16_f32 v83, v86, v87\n"
"v_mfma_f32_16x16x32_bf16 v[20:23], v[212:215], v[176:179], v[20:23]\n"
"v_add_f32 v88, v88, v156\n"
"v_add_f32 v89, v89, v156\n"
"v_mfma_f32_16x16x32_bf16 v[28:31], v[212:215], v[184:187], v[28:31]\n"
"ds_read_b128 v[212:215], v231 offset:12544\n"
"v_add_f32 v90, v90, v156\n"
"v_mfma_f32_16x16x32_bf16 v[28:31], v[208:211], v[152:155], v[28:31]\n"
"s_waitcnt lgkmcnt(3)\n"
"ds_read_b128 v[208:211], v230 offset:12544\n"
"v_add_f32 v91, v91, v156\n"
"v_mfma_f32_16x16x32_bf16 v[32:35], v[192:195], a[192:195], v[132:135]\n"
"v_exp_f32 v88, v88\n"
"v_mfma_f32_16x16x32_bf16 v[32:35], v[196:199], a[208:211], v[32:35]\n"
"s_add_u32 m0, s73, 0xd000\n"
"v_exp_f32 v89, v89\n"
"v_mfma_f32_16x16x32_bf16 v[40:43], v[196:199], a[212:215], v[140:143]\n"
"buffer_load_dwordx4 v220, s[48:51], s65 offen offset:0 nt lds\n"
"v_mfma_f32_16x16x32_bf16 v[40:43], v[192:195], a[196:199], v[40:43]\n"
"v_exp_f32 v90, v90\n"
"v_mfma_f32_16x16x32_bf16 v[48:51], v[192:195], a[200:203], v[148:151]\n"
"v_exp_f32 v91, v91\n"
"v_mfma_f32_16x16x32_bf16 v[48:51], v[196:199], a[216:219], v[48:51]\n"
"v_add_f32 v92, v92, v156\n"
"v_add_f32 v93, v93, v156\n"
"v_mfma_f32_16x16x32_bf16 v[56:59], v[196:199], a[220:223], v[156:159]\n"
"ds_read_b128 v[196:199], v231 offset:18432\n"
"v_add_f32 v94, v94, v156\n"
"v_mfma_f32_16x16x32_bf16 v[56:59], v[192:195], a[204:207], v[56:59]\n"
"s_waitcnt lgkmcnt(3)\n"
"ds_read_b128 v[192:195], v230 offset:18432\n"
"v_add_f32 v95, v95, v156\n"
"v_mfma_f32_16x16x32_bf16 v[32:35], v[200:203], a[224:227], v[32:35]\n"
"v_exp_f32 v92, v92\n"
"v_mfma_f32_16x16x32_bf16 v[32:35], v[204:207], a[240:243], v[32:35]\n"
"v_exp_f32 v93, v93\n"
"v_mfma_f32_16x16x32_bf16 v[40:43], v[204:207], a[244:247], v[40:43]\n"
"v_exp_f32 v94, v94\n"
"v_mfma_f32_16x16x32_bf16 v[40:43], v[200:203], a[228:231], v[40:43]\n"
"v_exp_f32 v95, v95\n"
"v_mfma_f32_16x16x32_bf16 v[48:51], v[200:203], a[232:235], v[48:51]\n"
"buffer_load_dwordx4 v221, s[48:51], s65 offen offset:1024 nt lds\n"
"v_mfma_f32_16x16x32_bf16 v[48:51], v[204:207], a[248:251], v[48:51]\n"
"v_add_f32 v248, v248, v88\n"
"v_add_f32 v249, v249, v89\n"
"v_mfma_f32_16x16x32_bf16 v[56:59], v[204:207], a[252:255], v[56:59]\n"
"ds_read_b128 v[204:207], v231 offset:18560\n"
"v_cvt_pk_bf16_f32 v88, v88, v89\n"
"v_mfma_f32_16x16x32_bf16 v[56:59], v[200:203], a[236:239], v[56:59]\n"
"s_waitcnt lgkmcnt(3)\n"
"ds_read_b128 v[200:203], v230 offset:18560\n"
"v_add_f32 v248, v248, v90\n"
"v_mfma_f32_16x16x32_bf16 v[32:35], v[208:211], v[128:131], v[32:35]\n"
"s_add_u32 s65, s65, 0x6000\n"
"v_add_f32 v249, v249, v91\n"
"v_cvt_pk_bf16_f32 v89, v90, v91\n"
"v_mfma_f32_16x16x32_bf16 v[32:35], v[212:215], v[160:163], v[32:35]\n"
"v_add_f32 v248, v248, v92\n"
"v_add_f32 v249, v249, v93\n"
"v_mfma_f32_16x16x32_bf16 v[40:43], v[212:215], v[168:171], v[40:43]\n"
"v_cvt_pk_bf16_f32 v90, v92, v93\n"
"v_add_f32 v248, v248, v94\n"
"v_mfma_f32_16x16x32_bf16 v[40:43], v[208:211], v[136:139], v[40:43]\n"
"v_add_f32 v249, v249, v95\n"
"v_cvt_pk_bf16_f32 v91, v94, v95\n"
"v_mfma_f32_16x16x32_bf16 v[48:51], v[208:211], v[144:147], v[48:51]\n"
"v_add_f32 v242, v242, v96\n"
"v_add_f32 v243, v243, v97\n"
"v_mfma_f32_16x16x32_bf16 v[48:51], v[212:215], v[176:179], v[48:51]\n"
"s_memrealtime s[82:83]\n"
"v_cvt_pk_bf16_f32 v96, v96, v97\n"
"v_add_f32 v242, v242, v98\n"
"v_mfma_f32_16x16x32_bf16 v[56:59], v[212:215], v[184:187], v[56:59]\n"
"ds_read_b128 v[212:215], v231 offset:18688\n"
"v_add_f32 v243, v243, v99\n"
"v_mfma_f32_16x16x32_bf16 v[56:59], v[208:211], v[152:155], v[56:59]\n"
"s_waitcnt lgkmcnt(3)\n"
"ds_read_b128 v[208:211], v230 offset:18688\n"
"v_cvt_pk_bf16_f32 v97, v98, v99\n"
"v_mfma_f32_16x16x32_bf16 v[36:39], v[192:195], a[192:195], v[132:135]\n"
"v_add_f32 v242, v242, v100\n"
"v_add_f32 v243, v243, v101\n"
"v_mfma_f32_16x16x32_bf16 v[36:39], v[196:199], a[208:211], v[36:39]\n"
"v_cvt_pk_bf16_f32 v98, v100, v101\n"
"v_add_f32 v242, v242, v102\n"
"v_mfma_f32_16x16x32_bf16 v[44:47], v[196:199], a[212:215], v[140:143]\n"
"v_add_f32 v243, v243, v103\n"
"v_cvt_pk_bf16_f32 v99, v102, v103\n"
"v_mfma_f32_16x16x32_bf16 v[44:47], v[192:195], a[196:199], v[44:47]\n"
"v_add_f32 v244, v244, v104\n"
"v_add_f32 v245, v245, v105\n"
"v_mfma_f32_16x16x32_bf16 v[52:55], v[192:195], a[200:203], v[148:151]\n"
"v_cvt_pk_bf16_f32 v104, v104, v105\n"
"v_add_f32 v244, v244, v106\n"
"v_mfma_f32_16x16x32_bf16 v[52:55], v[196:199], a[216:219], v[52:55]\n"
"v_add_f32 v245, v245, v107\n"
"v_cvt_pk_bf16_f32 v105, v106, v107\n"
"v_mfma_f32_16x16x32_bf16 v[60:63], v[196:199], a[220:223], v[156:159]\n"
"s_waitcnt vmcnt(28) lgkmcnt(0)\n"
"s_add_u32 s94, s82, s71\n"
"s_barrier\n"
"ds_read_b64_tr_b16 v[196:197], v232 offset:36864\n"
"v_mfma_f32_16x16x32_bf16 v[60:63], v[192:195], a[204:207], v[60:63]\n"
"ds_read_b64_tr_b16 v[198:199], v232 offset:43008\n"
"ds_read_b64_tr_b16 v[192:193], v232 offset:24576\n"
"v_mfma_f32_16x16x32_bf16 v[36:39], v[200:203], a[224:227], v[36:39]\n"
"ds_read_b64_tr_b16 v[194:195], v232 offset:30720\n"
"v_add_f32 v244, v244, v108\n"
"v_mfma_f32_16x16x32_bf16 v[36:39], v[204:207], a[240:243], v[36:39]\n"
"v_add_f32 v245, v245, v109\n"
"v_cvt_pk_bf16_f32 v106, v108, v109\n"
"v_mfma_f32_16x16x32_bf16 v[44:47], v[204:207], a[244:247], v[44:47]\n"
"v_add_f32 v244, v244, v110\n"
"v_add_f32 v245, v245, v111\n"
"v_mfma_f32_16x16x32_bf16 v[44:47], v[200:203], a[228:231], v[44:47]\n"
"v_cvt_pk_bf16_f32 v107, v110, v111\n"
"v_add_f32 v246, v246, v112\n"
"v_mfma_f32_16x16x32_bf16 v[52:55], v[200:203], a[232:235], v[52:55]\n"
"v_add_f32 v247, v247, v113\n"
"v_cvt_pk_bf16_f32 v112, v112, v113\n"
"v_mfma_f32_16x16x32_bf16 v[52:55], v[204:207], a[248:251], v[52:55]\n"
"v_add_f32 v246, v246, v114\n"
"v_add_f32 v247, v247, v115\n"
"v_mfma_f32_16x16x32_bf16 v[60:63], v[204:207], a[252:255], v[60:63]\n"
"ds_read_b64_tr_b16 v[204:205], v233 offset:36864\n"
"v_cvt_pk_bf16_f32 v113, v114, v115\n"
"v_mfma_f32_16x16x32_bf16 v[60:63], v[200:203], a[236:239], v[60:63]\n"
"ds_read_b64_tr_b16 v[206:207], v233 offset:43008\n"
"ds_read_b64_tr_b16 v[200:201], v233 offset:24576\n"
"v_mfma_f32_16x16x32_bf16 v[36:39], v[208:211], v[128:131], v[36:39]\n"
"ds_read_b64_tr_b16 v[202:203], v233 offset:30720\n"
"v_add_f32 v246, v246, v116\n"
"v_mfma_f32_16x16x32_bf16 v[36:39], v[212:215], v[160:163], v[36:39]\n"
"v_add_f32 v247, v247, v117\n"
"v_cvt_pk_bf16_f32 v114, v116, v117\n"
"v_mfma_f32_16x16x32_bf16 v[44:47], v[212:215], v[168:171], v[44:47]\n"
"v_add_f32 v246, v246, v118\n"
"v_add_f32 v247, v247, v119\n"
"v_mfma_f32_16x16x32_bf16 v[44:47], v[208:211], v[136:139], v[44:47]\n"
"v_cvt_pk_bf16_f32 v115, v118, v119\n"
"v_add_f32 v248, v248, v120\n"
"v_mfma_f32_16x16x32_bf16 v[52:55], v[208:211], v[144:147], v[52:55]\n"
"v_add_f32 v249, v249, v121\n"
"v_cvt_pk_bf16_f32 v120, v120, v121\n"
"v_mfma_f32_16x16x32_bf16 v[52:55], v[212:215], v[176:179], v[52:55]\n"
"v_add_f32 v248, v248, v122\n"
"v_add_f32 v249, v249, v123\n"
"v_mfma_f32_16x16x32_bf16 v[60:63], v[212:215], v[184:187], v[60:63]\n"
"ds_read_b64_tr_b16 v[212:213], v234 offset:36864\n"
"v_cvt_pk_bf16_f32 v121, v122, v123\n"
"v_add_f32 v248, v248, v124\n"
"v_add_f32 v249, v249, v125\n"
"v_cvt_pk_bf16_f32 v122, v124, v125\n"
"v_add_f32 v248, v248, v126\n"
"v_add_f32 v249, v249, v127\n"
"v_cvt_pk_bf16_f32 v123, v126, v127\n"
"v_mbcnt_lo_u32_b32 v189, -1, 0\n"
"v_mbcnt_hi_u32_b32 v189, -1, v189\n"
"v_mul_u32_u24 v190, 0x5556, v189\n"
"v_lshrrev_b32 v190, 16, v190\n"
"v_lshrrev_b32 v191, 2, v190\n"
"v_and_b32 v190, 3, v190\n"
"v_mul_u32_u24 v188, 0x5556, v189\n"
"v_lshrrev_b32 v188, 16, v188\n"
"v_mul_u32_u24 v188, 3, v188\n"
"v_sub_u32 v188, v189, v188\n"
"v_lshlrev_b32 v188, 7, v188\n"
"v_mul_u32_u24 v190, 0x500, v190\n"
"v_add_u32 v188, v188, v190\n"
"v_add3_u32 v191, v191, s23, 1\n"
"v_and_b32 v191, 3, v191\n"
"v_mad_u32_u24 v188, v191, s68, v188\n"
"v_mov_b32 v190, 0x7ff00000\n"
"v_cmp_gt_u32 vcc, 36, v189\n"
"s_nop 1\n"
"v_cndmask_b32 v188, v190, v188, vcc\n"
"v_mfma_f32_16x16x32_bf16 v[60:63], v[208:211], v[152:155], v[60:63]\n"
"s_waitcnt lgkmcnt(5)\n"
"ds_read_b64_tr_b16 v[214:215], v234 offset:43008\n"
"ds_read_b64_tr_b16 v[208:209], v234 offset:24576\n"
"s_mov_b32 s56, 5\n"
"s_cmp_lt_i32 s95, 0\n"
"s_cbranch_scc1 Lwa%=\n"
"La%=:\n"
"v_mfma_f32_16x16x32_bf16 a[0:3], v[192:195], v[64:67], a[0:3]\n"
"ds_read_b64_tr_b16 v[210:211], v234 offset:30720\n"
"v_mfma_f32_16x16x32_bf16 a[0:3], v[196:199], v[96:99], a[0:3]\n"
"s_add_u32 m0, s73, 0\n"
"v_exp_f32 v0, v0\n"
"v_mfma_f32_16x16x32_bf16 a[4:7], v[196:199], v[104:107], a[4:7]\n"
"buffer_load_dwordx4 v216, s[44:47], s64 offen offset:0 nt lds\n"
"v_mfma_f32_16x16x32_bf16 a[4:7], v[192:195], v[72:75], a[4:7]\n"
"v_exp_f32 v1, v1\n"
"v_mfma_f32_16x16x32_bf16 a[8:11], v[192:195], v[80:83], a[8:11]\n"
"v_exp_f32 v2, v2\n"
"v_mfma_f32_16x16x32_bf16 a[8:11], v[196:199], v[112:115], a[8:11]\n"
"v_exp_f32 v3, v3\n"
"v_mfma_f32_16x16x32_bf16 a[12:15], v[196:199], v[120:123], a[12:15]\n"
"ds_read_b64_tr_b16 v[196:197], v235 offset:36864\n"
"v_mfma_f32_16x16x32_bf16 a[12:15], v[192:195], v[88:91], a[12:15]\n"
"s_waitcnt lgkmcnt(5)\n"
"ds_read_b64_tr_b16 v[198:199], v235 offset:43008\n"
"ds_read_b64_tr_b16 v[192:193], v235 offset:24576\n"
"v_mfma_f32_16x16x32_bf16 a[16:19], v[200:203], v[64:67], a[16:19]\n"
"ds_read_b64_tr_b16 v[194:195], v235 offset:30720\n"
"v_mfma_f32_16x16x32_bf16 a[16:19], v[204:207], v[96:99], a[16:19]\n"
"v_exp_f32 v4, v4\n"
"v_mfma_f32_16x16x32_bf16 a[20:23], v[204:207], v[104:107], a[20:23]\n"
"v_exp_f32 v5, v5\n"
"v_mfma_f32_16x16x32_bf16 a[20:23], v[200:203], v[72:75], a[20:23]\n"
"v_exp_f32 v6, v6\n"
"v_mfma_f32_16x16x32_bf16 a[24:27], v[200:203], v[80:83], a[24:27]\n"
"buffer_load_dwordx4 v217, s[44:47], s64 offen offset:1024 nt lds\n"
"v_mfma_f32_16x16x32_bf16 a[24:27], v[204:207], v[112:115], a[24:27]\n"
"v_exp_f32 v7, v7\n"
"v_mfma_f32_16x16x32_bf16 a[28:31], v[204:207], v[120:123], a[28:31]\n"
"ds_read_b64_tr_b16 v[204:205], v232 offset:36992\n"
"v_add_f32 v242, v242, v0\n"
"v_mfma_f32_16x16x32_bf16 a[28:31], v[200:203], v[88:91], a[28:31]\n"
"s_waitcnt lgkmcnt(5)\n"
"ds_read_b64_tr_b16 v[206:207], v232 offset:43136\n"
"ds_read_b64_tr_b16 v[200:201], v232 offset:24704\n"
"v_mfma_f32_16x16x32_bf16 a[32:35], v[208:211], v[64:67], a[32:35]\n"
"ds_read_b64_tr_b16 v[202:203], v232 offset:30848\n"
"v_add_f32 v243, v243, v1\n"
"v_mfma_f32_16x16x32_bf16 a[32:35], v[212:215], v[96:99], a[32:35]\n"
"v_cvt_pk_bf16_f32 v0, v0, v1\n"
"v_add_f32 v242, v242, v2\n"
"v_mfma_f32_16x16x32_bf16 a[36:39], v[212:215], v[104:107], a[36:39]\n"
"v_add_f32 v243, v243, v3\n"
"v_cvt_pk_bf16_f32 v1, v2, v3\n"
"v_mfma_f32_16x16x32_bf16 a[36:39], v[208:211], v[72:75], a[36:39]\n"
"v_add_f32 v242, v242, v4\n"
"v_add_f32 v243, v243, v5\n"
"v_mfma_f32_16x16x32_bf16 a[40:43], v[208:211], v[80:83], a[40:43]\n"
"v_cvt_pk_bf16_f32 v2, v4, v5\n"
"v_add_f32 v242, v242, v6\n"
"v_mfma_f32_16x16x32_bf16 a[40:43], v[212:215], v[112:115], a[40:43]\n"
"v_add_f32 v243, v243, v7\n"
"v_mfma_f32_16x16x32_bf16 a[44:47], v[212:215], v[120:123], a[44:47]\n"
"ds_read_b64_tr_b16 v[212:213], v233 offset:36992\n"
"v_cvt_pk_bf16_f32 v3, v6, v7\n"
"v_mfma_f32_16x16x32_bf16 a[44:47], v[208:211], v[88:91], a[44:47]\n"
"s_waitcnt lgkmcnt(5)\n"
"ds_read_b64_tr_b16 v[214:215], v233 offset:43136\n"
"ds_read_b64_tr_b16 v[208:209], v233 offset:24704\n"
"v_mfma_f32_16x16x32_bf16 a[48:51], v[192:195], v[64:67], a[48:51]\n"
"ds_read_b64_tr_b16 v[210:211], v233 offset:30848\n"
"v_mfma_f32_16x16x32_bf16 a[48:51], v[196:199], v[96:99], a[48:51]\n"
"v_exp_f32 v8, v8\n"
"v_mfma_f32_16x16x32_bf16 a[52:55], v[196:199], v[104:107], a[52:55]\n"
"buffer_load_dwordx4 v218, s[44:47], s64 offen offset:2048 nt lds\n"
"v_mfma_f32_16x16x32_bf16 a[52:55], v[192:195], v[72:75], a[52:55]\n"
"v_exp_f32 v9, v9\n"
"v_mfma_f32_16x16x32_bf16 a[56:59], v[192:195], v[80:83], a[56:59]\n"
"v_exp_f32 v10, v10\n"
"v_mfma_f32_16x16x32_bf16 a[56:59], v[196:199], v[112:115], a[56:59]\n"
"v_exp_f32 v11, v11\n"
"v_mfma_f32_16x16x32_bf16 a[60:63], v[196:199], v[120:123], a[60:63]\n"
"ds_read_b64_tr_b16 v[196:197], v234 offset:36992\n"
"v_mfma_f32_16x16x32_bf16 a[60:63], v[192:195], v[88:91], a[60:63]\n"
"s_waitcnt lgkmcnt(5)\n"
"ds_read_b64_tr_b16 v[198:199], v234 offset:43136\n"
"ds_read_b64_tr_b16 v[192:193], v234 offset:24704\n"
"v_mfma_f32_16x16x32_bf16 a[64:67], v[200:203], v[64:67], a[64:67]\n"
"ds_read_b64_tr_b16 v[194:195], v234 offset:30848\n"
"v_mfma_f32_16x16x32_bf16 a[64:67], v[204:207], v[96:99], a[64:67]\n"
"v_exp_f32 v12, v12\n"
"v_mfma_f32_16x16x32_bf16 a[68:71], v[204:207], v[104:107], a[68:71]\n"
"v_exp_f32 v13, v13\n"
"v_mfma_f32_16x16x32_bf16 a[68:71], v[200:203], v[72:75], a[68:71]\n"
"v_exp_f32 v14, v14\n"
"v_mfma_f32_16x16x32_bf16 a[72:75], v[200:203], v[80:83], a[72:75]\n"
"buffer_load_dwordx4 v219, s[44:47], s64 offen offset:3072 nt lds\n"
"v_mfma_f32_16x16x32_bf16 a[72:75], v[204:207], v[112:115], a[72:75]\n"
"v_exp_f32 v15, v15\n"
"v_mfma_f32_16x16x32_bf16 a[76:79], v[204:207], v[120:123], a[76:79]\n"
"ds_read_b64_tr_b16 v[204:205], v235 offset:36992\n"
"v_add_f32 v244, v244, v8\n"
"v_mfma_f32_16x16x32_bf16 a[76:79], v[200:203], v[88:91], a[76:79]\n"
"s_waitcnt lgkmcnt(5)\n"
"ds_read_b64_tr_b16 v[206:207], v235 offset:43136\n"
"ds_read_b64_tr_b16 v[200:201], v235 offset:24704\n"
"v_mfma_f32_16x16x32_bf16 a[80:83], v[208:211], v[64:67], a[80:83]\n"
"ds_read_b64_tr_b16 v[202:203], v235 offset:30848\n"
"v_add_f32 v245, v245, v9\n"
"v_mfma_f32_16x16x32_bf16 a[80:83], v[212:215], v[96:99], a[80:83]\n"
"v_cvt_pk_bf16_f32 v8, v8, v9\n"
"v_add_f32 v244, v244, v10\n"
"v_mfma_f32_16x16x32_bf16 a[84:87], v[212:215], v[104:107], a[84:87]\n"
"v_add_f32 v245, v245, v11\n"
"v_cvt_pk_bf16_f32 v9, v10, v11\n"
"v_mfma_f32_16x16x32_bf16 a[84:87], v[208:211], v[72:75], a[84:87]\n"
"v_add_f32 v244, v244, v12\n"
"v_add_f32 v245, v245, v13\n"
"v_mfma_f32_16x16x32_bf16 a[88:91], v[208:211], v[80:83], a[88:91]\n"
"v_cvt_pk_bf16_f32 v10, v12, v13\n"
"v_add_f32 v244, v244, v14\n"
"v_mfma_f32_16x16x32_bf16 a[88:91], v[212:215], v[112:115], a[88:91]\n"
"v_add_f32 v245, v245, v15\n"
"v_cvt_pk_bf16_f32 v11, v14, v15\n"
"v_mfma_f32_16x16x32_bf16 a[92:95], v[212:215], v[120:123], a[92:95]\n"
"ds_read_b64_tr_b16 v[212:213], v232 offset:37120\n"
"v_mfma_f32_16x16x32_bf16 a[92:95], v[208:211], v[88:91], a[92:95]\n"
"s_waitcnt lgkmcnt(5)\n"
"ds_read_b64_tr_b16 v[214:215], v232 offset:43264\n"
"ds_read_b64_tr_b16 v[208:209], v232 offset:24832\n"
"v_mfma_f32_16x16x32_bf16 a[96:99], v[192:195], v[64:67], a[96:99]\n"
"ds_read_b64_tr_b16 v[210:211], v232 offset:30976\n"
"v_mfma_f32_16x16x32_bf16 a[96:99], v[196:199], v[96:99], a[96:99]\n"
"s_add_u32 m0, s73, 0x1000\n"
"v_exp_f32 v16, v16\n"
"v_mfma_f32_16x16x32_bf16 a[100:103], v[196:199], v[104:107], a[100:103]\n"
"buffer_load_dwordx4 v220, s[44:47], s64 offen offset:0 nt lds\n"
"v_mfma_f32_16x16x32_bf16 a[100:103], v[192:195], v[72:75], a[100:103]\n"
"v_exp_f32 v17, v17\n"
"v_mfma_f32_16x16x32_bf16 a[104:107], v[192:195], v[80:83], a[104:107]\n"
"v_exp_f32 v18, v18\n"
"v_mfma_f32_16x16x32_bf16 a[104:107], v[196:199], v[112:115], a[104:107]\n"
"v_exp_f32 v19, v19\n"
"v_mfma_f32_16x16x32_bf16 a[108:111], v[196:199], v[120:123], a[108:111]\n"
"ds_read_b64_tr_b16 v[196:197], v233 offset:37120\n"
"v_mfma_f32_16x16x32_bf16 a[108:111], v[192:195], v[88:91], a[108:111]\n"
"s_waitcnt lgkmcnt(5)\n"
"ds_read_b64_tr_b16 v[198:199], v233 offset:43264\n"
"ds_read_b64_tr_b16 v[192:193], v233 offset:24832\n"
"v_mfma_f32_16x16x32_bf16 a[112:115], v[200:203], v[64:67], a[112:115]\n"
"ds_read_b64_tr_b16 v[194:195], v233 offset:30976\n"
"v_mfma_f32_16x16x32_bf16 a[112:115], v[204:207], v[96:99], a[112:115]\n"
"v_exp_f32 v20, v20\n"
"v_mfma_f32_16x16x32_bf16 a[116:119], v[204:207], v[104:107], a[116:119]\n"
"v_exp_f32 v21, v21\n"
"v_mfma_f32_16x16x32_bf16 a[116:119], v[200:203], v[72:75], a[116:119]\n"
"v_exp_f32 v22, v22\n"
"v_mfma_f32_16x16x32_bf16 a[120:123], v[200:203], v[80:83], a[120:123]\n"
"buffer_load_dwordx4 v221, s[44:47], s64 offen offset:1024 nt lds\n"
"v_mfma_f32_16x16x32_bf16 a[120:123], v[204:207], v[112:115], a[120:123]\n"
"v_exp_f32 v23, v23\n"
"v_mfma_f32_16x16x32_bf16 a[124:127], v[204:207], v[120:123], a[124:127]\n"
"ds_read_b64_tr_b16 v[204:205], v234 offset:37120\n"
"v_add_f32 v246, v246, v16\n"
"v_mfma_f32_16x16x32_bf16 a[124:127], v[200:203], v[88:91], a[124:127]\n"
"s_waitcnt lgkmcnt(5)\n"
"ds_read_b64_tr_b16 v[206:207], v234 offset:43264\n"
"ds_read_b64_tr_b16 v[200:201], v234 offset:24832\n"
"v_mfma_f32_16x16x32_bf16 a[128:131], v[208:211], v[64:67], a[128:131]\n"
"s_add_u32 s64, s64, 0x6000\n"
"ds_read_b64_tr_b16 v[202:203], v234 offset:30976\n"
"v_add_f32 v247, v247, v17\n"
"v_mfma_f32_16x16x32_bf16 a[128:131], v[212:215], v[96:99], a[128:131]\n"
"v_cvt_pk_bf16_f32 v16, v16, v17\n"
"v_add_f32 v246, v246, v18\n"
"v_mfma_f32_16x16x32_bf16 a[132:135], v[212:215], v[104:107], a[132:135]\n"
"s_cmp_eq_u32 s56, 3\n"
"s_cbranch_scc0 Ltsl0x3%=\n"
"Ltil0x3%=:\n"
"s_mov_b64 exec, 0\n"
"s_cmp_eq_u32 %[w], 0\n"
"s_cbranch_scc1 Ltp9%=\n"
"s_cmp_eq_u32 %[w], 1\n"
"s_cbranch_scc1 Ltp10%=\n"
"s_cmp_eq_u32 %[w], 2\n"
"s_cbranch_scc1 Ltp13%=\n"
"s_cmp_eq_u32 %[w], 3\n"
"s_cbranch_scc1 Ltp14%=\n"
"Ltrl0x3%=:\n"
"s_mov_b64 exec, -1\n"
"s_cmp_lt_i32 s95, 0\n"
"s_cbranch_scc1 Lwtsl0x3%=\n"
"Ltsl0x3%=:\n"
"v_add_f32 v247, v247, v19\n"
"v_cvt_pk_bf16_f32 v17, v18, v19\n"
"v_mfma_f32_16x16x32_bf16 a[132:135], v[208:211], v[72:75], a[132:135]\n"
"v_add_f32 v246, v246, v20\n"
"v_add_f32 v247, v247, v21\n"
"v_mfma_f32_16x16x32_bf16 a[136:139], v[208:211], v[80:83], a[136:139]\n"
"v_cvt_pk_bf16_f32 v18, v20, v21\n"
"v_add_f32 v246, v246, v22\n"
"v_mfma_f32_16x16x32_bf16 a[136:139], v[212:215], v[112:115], a[136:139]\n"
"s_memrealtime s[82:83]\n"
"v_exp_f32 v24, v24\n"
"v_mfma_f32_16x16x32_bf16 a[140:143], v[212:215], v[120:123], a[140:143]\n"
"ds_read_b64_tr_b16 v[212:213], v235 offset:37120\n"
"v_add_f32 v247, v247, v23\n"
"v_mfma_f32_16x16x32_bf16 a[140:143], v[208:211], v[88:91], a[140:143]\n"
"s_waitcnt lgkmcnt(5)\n"
"ds_read_b64_tr_b16 v[214:215], v235 offset:43264\n"
"ds_read_b64_tr_b16 v[208:209], v235 offset:24832\n"
"v_mfma_f32_16x16x32_bf16 a[144:147], v[192:195], v[64:67], a[144:147]\n"
"ds_read_b64_tr_b16 v[210:211], v235 offset:30976\n"
"v_cvt_pk_bf16_f32 v19, v22, v23\n"
"v_mfma_f32_16x16x32_bf16 a[144:147], v[196:199], v[96:99], a[144:147]\n"
"v_exp_f32 v25, v25\n"
"v_mfma_f32_16x16x32_bf16 a[148:151], v[196:199], v[104:107], a[148:151]\n"
"v_exp_f32 v26, v26\n"
"v_mfma_f32_16x16x32_bf16 a[148:151], v[192:195], v[72:75], a[148:151]\n"
"v_exp_f32 v27, v27\n"
"v_mfma_f32_16x16x32_bf16 a[152:155], v[192:195], v[80:83], a[152:155]\n"
"v_exp_f32 v28, v28\n"
"v_mfma_f32_16x16x32_bf16 a[152:155], v[196:199], v[112:115], a[152:155]\n"
"v_mfma_f32_16x16x32_bf16 a[156:159], v[196:199], v[120:123], a[156:159]\n"
"s_waitcnt vmcnt(12) lgkmcnt(0)\n"
"s_sub_u32 s83, s94, s82\n"
"s_cmp_gt_i32 s83, 0\n"
"s_cbranch_scc0 Lrpc2%=\n"
"s_min_i32 s83, s83, 0xc8\n"
"Lqpc2%=:\n"
"s_sleep 1\n"
"s_sub_u32 s83, s83, 4\n"
"s_cmp_gt_i32 s83, 0\n"
"s_cbranch_scc1 Lqpc2%=\n"
"Lrpc2%=:\n"
"s_barrier\n"
"ds_read_b128 v[196:199], v231 offset:24576\n"
"v_mfma_f32_16x16x32_bf16 a[156:159], v[192:195], v[88:91], a[156:159]\n"
"s_sub_u32 s6, 120, s71\n"
"s_max_i32 s6, s6, 4\n"
"ds_read_b128 v[192:195], v230 offset:24576\n"
"v_mfma_f32_16x16x32_bf16 a[160:163], v[200:203], v[64:67], a[160:163]\n"
"s_sub_u32 s82, s82, 16\n"
"v_exp_f32 v29, v29\n"
"v_mfma_f32_16x16x32_bf16 a[160:163], v[204:207], v[96:99], a[160:163]\n"
"s_sub_u32 s7, s71, s29\n"
"s_add_u32 s7, s7, 10\n"
"v_exp_f32 v30, v30\n"
"v_mfma_f32_16x16x32_bf16 a[164:167], v[204:207], v[104:107], a[164:167]\n"
"s_sub_u32 s28, s82, s94\n"
"s_cmp_gt_i32 s28, s6\n"
"s_cselect_b32 s28, s7, -5\n"
"v_exp_f32 v31, v31\n"
"v_mfma_f32_16x16x32_bf16 a[164:167], v[200:203], v[72:75], a[164:167]\n"
"s_add_u32 s71, s71, s28\n"
"s_min_i32 s71, s71, 152\n"
"s_max_i32 s71, s71, s29\n"
"v_add_f32 v248, v248, v24\n"
"v_add_f32 v249, v249, v25\n"
"v_mfma_f32_16x16x32_bf16 a[168:171], v[200:203], v[80:83], a[168:171]\n"
"s_max_i32 s94, s94, s82\n"
"v_cvt_pk_bf16_f32 v24, v24, v25\n"
"v_mfma_f32_16x16x32_bf16 a[168:171], v[204:207], v[112:115], a[168:171]\n"
"s_add_u32 s94, s94, s71\n"
"v_add_f32 v248, v248, v26\n"
"v_mfma_f32_16x16x32_bf16 a[172:175], v[204:207], v[120:123], a[172:175]\n"
"ds_read_b128 v[204:207], v231 offset:24704\n"
"v_add_f32 v249, v249, v27\n"
"v_mfma_f32_16x16x32_bf16 a[172:175], v[200:203], v[88:91], a[172:175]\n"
"s_sub_u32 s28, s71, 36\n"
"s_max_i32 s29, s29, s28\n"
"ds_read_b128 v[200:203], v230 offset:24704\n"
"v_mfma_f32_16x16x32_bf16 a[176:179], v[208:211], v[64:67], a[176:179]\n"
"v_cvt_pk_bf16_f32 v25, v26, v27\n"
"v_add_f32 v248, v248, v28\n"
"v_mfma_f32_16x16x32_bf16 a[176:179], v[212:215], v[96:99], a[176:179]\n"
"v_add_f32 v249, v249, v29\n"
"v_cvt_pk_bf16_f32 v26, v28, v29\n"
"v_mfma_f32_16x16x32_bf16 a[180:183], v[212:215], v[104:107], a[180:183]\n"
"v_add_f32 v248, v248, v30\n"
"v_mfma_f32_16x16x32_bf16 a[180:183], v[208:211], v[72:75], a[180:183]\n"
"v_add_f32 v249, v249, v31\n"
"v_mfma_f32_16x16x32_bf16 a[184:187], v[208:211], v[80:83], a[184:187]\n"
"v_cvt_pk_bf16_f32 v27, v30, v31\n"
"v_mfma_f32_16x16x32_bf16 a[184:187], v[212:215], v[112:115], a[184:187]\n"
"v_exp_f32 v32, v32\n"
"v_mfma_f32_16x16x32_bf16 a[188:191], v[212:215], v[120:123], a[188:191]\n"
"ds_read_b128 v[212:215], v231 offset:24832\n"
"v_mfma_f32_16x16x32_bf16 a[188:191], v[208:211], v[88:91], a[188:191]\n"
"s_waitcnt lgkmcnt(3)\n"
"ds_read_b128 v[208:211], v230 offset:24832\n"
"v_mfma_f32_16x16x32_bf16 v[64:67], v[192:195], a[192:195], v[132:135]\n"
"v_exp_f32 v33, v33\n"
"v_mfma_f32_16x16x32_bf16 v[64:67], v[196:199], a[208:211], v[64:67]\n"
"s_add_u32 m0, s73, 0x12000\n"
"v_exp_f32 v34, v34\n"
"v_mfma_f32_16x16x32_bf16 v[72:75], v[196:199], a[212:215], v[140:143]\n"
"buffer_load_dwordx4 v216, s[48:51], s65 offen offset:0 nt lds\n"
"v_mfma_f32_16x16x32_bf16 v[72:75], v[192:195], a[196:199], v[72:75]\n"
"v_exp_f32 v35, v35\n"
"v_mfma_f32_16x16x32_bf16 v[80:83], v[192:195], a[200:203], v[148:151]\n"
"v_exp_f32 v36, v36\n"
"v_mfma_f32_16x16x32_bf16 v[80:83], v[196:199], a[216:219], v[80:83]\n"
"v_exp_f32 v37, v37\n"
"v_mfma_f32_16x16x32_bf16 v[88:91], v[196:199], a[220:223], v[156:159]\n"
"ds_read_b128 v[196:199], v231 offset:30720\n"
"v_mfma_f32_16x16x32_bf16 v[88:91], v[192:195], a[204:207], v[88:91]\n"
"s_waitcnt lgkmcnt(3)\n"
"ds_read_b128 v[192:195], v230 offset:30720\n"
"v_mfma_f32_16x16x32_bf16 v[64:67], v[200:203], a[224:227], v[64:67]\n"
"v_exp_f32 v38, v38\n"
"v_mfma_f32_16x16x32_bf16 v[64:67], v[204:207], a[240:243], v[64:67]\n"
"Ltp7%=:\n"
"s_cbranch_execz Ltrp%=\n"
"v_exp_f32 v39, v39\n"
"v_mfma_f32_16x16x32_bf16 v[72:75], v[204:207], a[244:247], v[72:75]\n"
"v_add_f32 v242, v242, v32\n"
"v_mfma_f32_16x16x32_bf16 v[72:75], v[200:203], a[228:231], v[72:75]\n"
"v_add_f32 v243, v243, v33\n"
"v_cvt_pk_bf16_f32 v32, v32, v33\n"
"v_mfma_f32_16x16x32_bf16 v[80:83], v[200:203], a[232:235], v[80:83]\n"
"buffer_load_dwordx4 v217, s[48:51], s65 offen offset:1024 nt lds\n"
"v_mfma_f32_16x16x32_bf16 v[80:83], v[204:207], a[248:251], v[80:83]\n"
"v_add_f32 v242, v242, v34\n"
"v_add_f32 v243, v243, v35\n"
"v_mfma_f32_16x16x32_bf16 v[88:91], v[204:207], a[252:255], v[88:91]\n"
"ds_read_b128 v[204:207], v231 offset:30848\n"
"v_cvt_pk_bf16_f32 v33, v34, v35\n"
"v_mfma_f32_16x16x32_bf16 v[88:91], v[200:203], a[236:239], v[88:91]\n"
"s_waitcnt lgkmcnt(3)\n"
"ds_read_b128 v[200:203], v230 offset:30848\n"
"v_add_f32 v242, v242, v36\n"
"v_mfma_f32_16x16x32_bf16 v[64:67], v[208:211], v[128:131], v[64:67]\n"
"v_add_f32 v243, v243, v37\n"
"v_cvt_pk_bf16_f32 v34, v36, v37\n"
"v_mfma_f32_16x16x32_bf16 v[64:67], v[212:215], v[160:163], v[64:67]\n"
"v_add_f32 v242, v242, v38\n"
"v_mfma_f32_16x16x32_bf16 v[72:75], v[212:215], v[168:171], v[72:75]\n"
"v_add_f32 v243, v243, v39\n"
"v_mfma_f32_16x16x32_bf16 v[72:75], v[208:211], v[136:139], v[72:75]\n"
"v_cvt_pk_bf16_f32 v35, v38, v39\n"
"v_mfma_f32_16x16x32_bf16 v[80:83], v[208:211], v[144:147], v[80:83]\n"
"v_exp_f32 v40, v40\n"
"v_mfma_f32_16x16x32_bf16 v[80:83], v[212:215], v[176:179], v[80:83]\n"
"v_exp_f32 v41, v41\n"
"v_mfma_f32_16x16x32_bf16 v[88:91], v[212:215], v[184:187], v[88:91]\n"
"ds_read_b128 v[212:215], v231 offset:30976\n"
"v_mfma_f32_16x16x32_bf16 v[88:91], v[208:211], v[152:155], v[88:91]\n"
"s_waitcnt lgkmcnt(3)\n"
"ds_read_b128 v[208:211], v230 offset:30976\n"
"v_mfma_f32_16x16x32_bf16 v[68:71], v[192:195], a[192:195], v[132:135]\n"
"v_exp_f32 v42, v42\n"
"v_mfma_f32_16x16x32_bf16 v[68:71], v[196:199], a[208:211], v[68:71]\n"
"v_exp_f32 v43, v43\n"
"v_mfma_f32_16x16x32_bf16 v[76:79], v[196:199], a[212:215], v[140:143]\n"
"buffer_load_dwordx4 v218, s[48:51], s65 offen offset:2048 nt lds\n"
"v_mfma_f32_16x16x32_bf16 v[76:79], v[192:195], a[196:199], v[76:79]\n"
"v_exp_f32 v44, v44\n"
"v_mfma_f32_16x16x32_bf16 v[84:87], v[192:195], a[200:203], v[148:151]\n"
"v_exp_f32 v45, v45\n"
"v_mfma_f32_16x16x32_bf16 v[84:87], v[196:199], a[216:219], v[84:87]\n"
"v_mfma_f32_16x16x32_bf16 v[92:95], v[196:199], a[220:223], v[156:159]\n"
"ds_read_b128 v[196:199], v231 offset:36864\n"
"v_mfma_f32_16x16x32_bf16 v[92:95], v[192:195], a[204:207], v[92:95]\n"
"s_waitcnt lgkmcnt(3)\n"
"ds_read_b128 v[192:195], v230 offset:36864\n"
"v_mfma_f32_16x16x32_bf16 v[68:71], v[200:203], a[224:227], v[68:71]\n"
"v_exp_f32 v46, v46\n"
"v_mfma_f32_16x16x32_bf16 v[68:71], v[204:207], a[240:243], v[68:71]\n"
"v_exp_f32 v47, v47\n"
"v_mfma_f32_16x16x32_bf16 v[76:79], v[204:207], a[244:247], v[76:79]\n"
"v_add_f32 v244, v244, v40\n"
"v_add_f32 v245, v245, v41\n"
"v_mfma_f32_16x16x32_bf16 v[76:79], v[200:203], a[228:231], v[76:79]\n"
"v_cvt_pk_bf16_f32 v40, v40, v41\n"
"v_mfma_f32_16x16x32_bf16 v[84:87], v[200:203], a[232:235], v[84:87]\n"
"buffer_load_dwordx4 v219, s[48:51], s65 offen offset:3072 nt lds\n"
"v_mfma_f32_16x16x32_bf16 v[84:87], v[204:207], a[248:251], v[84:87]\n"
"v_add_f32 v244, v244, v42\n"
"v_add_f32 v245, v245, v43\n"
"v_mfma_f32_16x16x32_bf16 v[92:95], v[204:207], a[252:255], v[92:95]\n"
"ds_read_b128 v[204:207], v231 offset:36992\n"
"v_cvt_pk_bf16_f32 v41, v42, v43\n"
"v_mfma_f32_16x16x32_bf16 v[92:95], v[200:203], a[236:239], v[92:95]\n"
"s_waitcnt lgkmcnt(3)\n"
"ds_read_b128 v[200:203], v230 offset:36992\n"
"v_add_f32 v244, v244, v44\n"
"v_mfma_f32_16x16x32_bf16 v[68:71], v[208:211], v[128:131], v[68:71]\n"
"v_add_f32 v245, v245, v45\n"
"v_cvt_pk_bf16_f32 v42, v44, v45\n"
"v_mfma_f32_16x16x32_bf16 v[68:71], v[212:215], v[160:163], v[68:71]\n"
"v_add_f32 v244, v244, v46\n"
"v_add_f32 v245, v245, v47\n"
"v_mfma_f32_16x16x32_bf16 v[76:79], v[212:215], v[168:171], v[76:79]\n"
"v_cvt_pk_bf16_f32 v43, v46, v47\n"
"v_mfma_f32_16x16x32_bf16 v[76:79], v[208:211], v[136:139], v[76:79]\n"
"v_exp_f32 v48, v48\n"
"v_mfma_f32_16x16x32_bf16 v[84:87], v[208:211], v[144:147], v[84:87]\n"
"v_mfma_f32_16x16x32_bf16 v[84:87], v[212:215], v[176:179], v[84:87]\n"
"v_exp_f32 v49, v49\n"
"v_mfma_f32_16x16x32_bf16 v[92:95], v[212:215], v[184:187], v[92:95]\n"
"ds_read_b128 v[212:215], v231 offset:37120\n"
"v_mfma_f32_16x16x32_bf16 v[92:95], v[208:211], v[152:155], v[92:95]\n"
"s_waitcnt lgkmcnt(3)\n"
"ds_read_b128 v[208:211], v230 offset:37120\n"
"v_mfma_f32_16x16x32_bf16 v[96:99], v[192:195], a[192:195], v[132:135]\n"
"v_exp_f32 v50, v50\n"
"v_mfma_f32_16x16x32_bf16 v[96:99], v[196:199], a[208:211], v[96:99]\n"
"s_add_u32 m0, s73, 0x13000\n"
"v_exp_f32 v51, v51\n"
"v_mfma_f32_16x16x32_bf16 v[104:107], v[196:199], a[212:215], v[140:143]\n"
"buffer_load_dwordx4 v220, s[48:51], s65 offen offset:0 nt lds\n"
"v_mfma_f32_16x16x32_bf16 v[104:107], v[192:195], a[196:199], v[104:107]\n"
"v_exp_f32 v52, v52\n"
"v_mfma_f32_16x16x32_bf16 v[112:115], v[192:195], a[200:203], v[148:151]\n"
"v_exp_f32 v53, v53\n"
"v_mfma_f32_16x16x32_bf16 v[112:115], v[196:199], a[216:219], v[112:115]\n"
"v_exp_f32 v54, v54\n"
"v_mfma_f32_16x16x32_bf16 v[120:123], v[196:199], a[220:223], v[156:159]\n"
"ds_read_b128 v[196:199], v231 offset:43008\n"
"v_mfma_f32_16x16x32_bf16 v[120:123], v[192:195], a[204:207], v[120:123]\n"
"s_waitcnt lgkmcnt(3)\n"
"ds_read_b128 v[192:195], v230 offset:43008\n"
"v_mfma_f32_16x16x32_bf16 v[96:99], v[200:203], a[224:227], v[96:99]\n"
"v_exp_f32 v55, v55\n"
"v_mfma_f32_16x16x32_bf16 v[96:99], v[204:207], a[240:243], v[96:99]\n"
"v_add_f32 v246, v246, v48\n"
"v_add_f32 v247, v247, v49\n"
"v_mfma_f32_16x16x32_bf16 v[104:107], v[204:207], a[244:247], v[104:107]\n"
"v_cvt_pk_bf16_f32 v48, v48, v49\n"
"v_mfma_f32_16x16x32_bf16 v[104:107], v[200:203], a[228:231], v[104:107]\n"
"v_add_f32 v246, v246, v50\n"
"v_mfma_f32_16x16x32_bf16 v[112:115], v[200:203], a[232:235], v[112:115]\n"
"buffer_load_dwordx4 v221, s[48:51], s65 offen offset:1024 nt lds\n"
"v_mfma_f32_16x16x32_bf16 v[112:115], v[204:207], a[248:251], v[112:115]\n"
"v_add_f32 v247, v247, v51\n"
"v_cvt_pk_bf16_f32 v49, v50, v51\n"
"v_mfma_f32_16x16x32_bf16 v[120:123], v[204:207], a[252:255], v[120:123]\n"
"ds_read_b128 v[204:207], v231 offset:43136\n"
"v_add_f32 v246, v246, v52\n"
"v_mfma_f32_16x16x32_bf16 v[120:123], v[200:203], a[236:239], v[120:123]\n"
"s_waitcnt lgkmcnt(3)\n"
"ds_read_b128 v[200:203], v230 offset:43136\n"
"v_add_f32 v247, v247, v53\n"
"v_mfma_f32_16x16x32_bf16 v[96:99], v[208:211], v[128:131], v[96:99]\n"
"s_add_u32 s65, s65, 0x6000\n"
"v_cvt_pk_bf16_f32 v50, v52, v53\n"
"v_add_f32 v246, v246, v54\n"
"v_mfma_f32_16x16x32_bf16 v[96:99], v[212:215], v[160:163], v[96:99]\n"
"v_add_f32 v247, v247, v55\n"
"v_cvt_pk_bf16_f32 v51, v54, v55\n"
"v_mfma_f32_16x16x32_bf16 v[104:107], v[212:215], v[168:171], v[104:107]\n"
"v_exp_f32 v56, v56\n"
"v_mfma_f32_16x16x32_bf16 v[104:107], v[208:211], v[136:139], v[104:107]\n"
"v_mfma_f32_16x16x32_bf16 v[112:115], v[208:211], v[144:147], v[112:115]\n"
"v_exp_f32 v57, v57\n"
"v_mfma_f32_16x16x32_bf16 v[112:115], v[212:215], v[176:179], v[112:115]\n"
"s_memrealtime s[82:83]\n"
"v_mfma_f32_16x16x32_bf16 v[120:123], v[212:215], v[184:187], v[120:123]\n"
"ds_read_b128 v[212:215], v231 offset:43264\n"
"v_mfma_f32_16x16x32_bf16 v[120:123], v[208:211], v[152:155], v[120:123]\n"
"s_waitcnt lgkmcnt(3)\n"
"ds_read_b128 v[208:211], v230 offset:43264\n"
"v_mfma_f32_16x16x32_bf16 v[100:103], v[192:195], a[192:195], v[132:135]\n"
"v_exp_f32 v58, v58\n"
"v_mfma_f32_16x16x32_bf16 v[100:103], v[196:199], a[208:211], v[100:103]\n"
"v_exp_f32 v59, v59\n"
"v_mfma_f32_16x16x32_bf16 v[108:111], v[196:199], a[212:215], v[140:143]\n"
"v_exp_f32 v60, v60\n"
"v_mfma_f32_16x16x32_bf16 v[108:111], v[192:195], a[196:199], v[108:111]\n"
"v_exp_f32 v61, v61\n"
"v_mfma_f32_16x16x32_bf16 v[116:119], v[192:195], a[200:203], v[148:151]\n"
"v_exp_f32 v62, v62\n"
"v_mfma_f32_16x16x32_bf16 v[116:119], v[196:199], a[216:219], v[116:119]\n"
"v_mfma_f32_16x16x32_bf16 v[124:127], v[196:199], a[220:223], v[156:159]\n"
"s_waitcnt vmcnt(12) lgkmcnt(0)\n"
"s_sub_u32 s83, s94, s82\n"
"s_cmp_gt_i32 s83, 0\n"
"s_cbranch_scc0 Lrpc3%=\n"
"s_min_i32 s83, s83, 0xc8\n"
"Lqpc3%=:\n"
"s_sleep 1\n"
"s_sub_u32 s83, s83, 4\n"
"s_cmp_gt_i32 s83, 0\n"
"s_cbranch_scc1 Lqpc3%=\n"
"Lrpc3%=:\n"
"s_barrier\n"
"ds_read_b64_tr_b16 v[196:197], v232 offset:12288\n"
"v_mfma_f32_16x16x32_bf16 v[124:127], v[192:195], a[204:207], v[124:127]\n"
"s_sub_u32 s6, 120, s71\n"
"s_max_i32 s6, s6, 4\n"
"ds_read_b64_tr_b16 v[198:199], v232 offset:18432\n"
"ds_read_b64_tr_b16 v[192:193], v232 offset:0\n"
"v_mfma_f32_16x16x32_bf16 v[100:103], v[200:203], a[224:227], v[100:103]\n"
"s_sub_u32 s82, s82, 16\n"
"ds_read_b64_tr_b16 v[194:195], v232 offset:6144\n"
"v_add_f32 v248, v248, v56\n"
"v_mfma_f32_16x16x32_bf16 v[100:103], v[204:207], a[240:243], v[100:103]\n"
"s_sub_u32 s7, s71, s29\n"
"s_add_u32 s7, s7, 10\n"
"v_exp_f32 v63, v63\n"
"v_mfma_f32_16x16x32_bf16 v[108:111], v[204:207], a[244:247], v[108:111]\n"
"s_sub_u32 s28, s82, s94\n"
"s_cmp_gt_i32 s28, s6\n"
"s_cselect_b32 s28, s7, -5\n"
"v_add_f32 v249, v249, v57\n"
"v_cvt_pk_bf16_f32 v56, v56, v57\n"
"v_mfma_f32_16x16x32_bf16 v[108:111], v[200:203], a[228:231], v[108:111]\n"
"s_add_u32 s71, s71, s28\n"
"s_min_i32 s71, s71, 152\n"
"s_max_i32 s71, s71, s29\n"
"v_add_f32 v248, v248, v58\n"
"v_add_f32 v249, v249, v59\n"
"v_mfma_f32_16x16x32_bf16 v[116:119], v[200:203], a[232:235], v[116:119]\n"
"s_max_i32 s94, s94, s82\n"
"v_cvt_pk_bf16_f32 v57, v58, v59\n"
"v_mfma_f32_16x16x32_bf16 v[116:119], v[204:207], a[248:251], v[116:119]\n"
"s_add_u32 s94, s94, s71\n"
"v_add_f32 v248, v248, v60\n"
"v_add_f32 v249, v249, v61\n"
"v_mfma_f32_16x16x32_bf16 v[124:127], v[204:207], a[252:255], v[124:127]\n"
"ds_read_b64_tr_b16 v[204:205], v233 offset:12288\n"
"v_cvt_pk_bf16_f32 v58, v60, v61\n"
"v_mfma_f32_16x16x32_bf16 v[124:127], v[200:203], a[236:239], v[124:127]\n"
"s_sub_u32 s28, s71, 36\n"
"s_max_i32 s29, s29, s28\n"
"ds_read_b64_tr_b16 v[206:207], v233 offset:18432\n"
"ds_read_b64_tr_b16 v[200:201], v233 offset:0\n"
"v_mfma_f32_16x16x32_bf16 v[100:103], v[208:211], v[128:131], v[100:103]\n"
"ds_read_b64_tr_b16 v[202:203], v233 offset:6144\n"
"v_add_f32 v248, v248, v62\n"
"v_mfma_f32_16x16x32_bf16 v[100:103], v[212:215], v[160:163], v[100:103]\n"
"v_add_f32 v249, v249, v63\n"
"v_cvt_pk_bf16_f32 v59, v62, v63\n"
"v_mfma_f32_16x16x32_bf16 v[108:111], v[212:215], v[168:171], v[108:111]\n"
"v_mfma_f32_16x16x32_bf16 v[108:111], v[208:211], v[136:139], v[108:111]\n"
"v_mfma_f32_16x16x32_bf16 v[116:119], v[208:211], v[144:147], v[116:119]\n"
"v_mfma_f32_16x16x32_bf16 v[116:119], v[212:215], v[176:179], v[116:119]\n"
"v_mfma_f32_16x16x32_bf16 v[124:127], v[212:215], v[184:187], v[124:127]\n"
"ds_read_b64_tr_b16 v[212:213], v234 offset:12288\n"
"v_mfma_f32_16x16x32_bf16 v[124:127], v[208:211], v[152:155], v[124:127]\n"
"s_waitcnt lgkmcnt(5)\n"
"ds_read_b64_tr_b16 v[214:215], v234 offset:18432\n"
"ds_read_b64_tr_b16 v[208:209], v234 offset:0\n"
"v_mfma_f32_16x16x32_bf16 a[0:3], v[192:195], v[0:3], a[0:3]\n"
"ds_read_b64_tr_b16 v[210:211], v234 offset:6144\n"
"v_mfma_f32_16x16x32_bf16 a[0:3], v[196:199], v[32:35], a[0:3]\n"
"s_add_u32 m0, s73, 0x6000\n"
"v_exp_f32 v64, v64\n"
"v_mfma_f32_16x16x32_bf16 a[4:7], v[196:199], v[40:43], a[4:7]\n"
"buffer_load_dwordx4 v216, s[44:47], s64 offen offset:0 nt lds\n"
"v_mfma_f32_16x16x32_bf16 a[4:7], v[192:195], v[8:11], a[4:7]\n"
"v_exp_f32 v65, v65\n"
"v_mfma_f32_16x16x32_bf16 a[8:11], v[192:195], v[16:19], a[8:11]\n"
"v_exp_f32 v66, v66\n"
"v_mfma_f32_16x16x32_bf16 a[8:11], v[196:199], v[48:51], a[8:11]\n"
"v_exp_f32 v67, v67\n"
"v_mfma_f32_16x16x32_bf16 a[12:15], v[196:199], v[56:59], a[12:15]\n"
"ds_read_b64_tr_b16 v[196:197], v235 offset:12288\n"
"v_mfma_f32_16x16x32_bf16 a[12:15], v[192:195], v[24:27], a[12:15]\n"
"s_waitcnt lgkmcnt(5)\n"
"ds_read_b64_tr_b16 v[198:199], v235 offset:18432\n"
"ds_read_b64_tr_b16 v[192:193], v235 offset:0\n"
"v_mfma_f32_16x16x32_bf16 a[16:19], v[200:203], v[0:3], a[16:19]\n"
"ds_read_b64_tr_b16 v[194:195], v235 offset:6144\n"
"v_mfma_f32_16x16x32_bf16 a[16:19], v[204:207], v[32:35], a[16:19]\n"
"v_exp_f32 v68, v68\n"
"v_mfma_f32_16x16x32_bf16 a[20:23], v[204:207], v[40:43], a[20:23]\n"
"v_exp_f32 v69, v69\n"
"v_mfma_f32_16x16x32_bf16 a[20:23], v[200:203], v[8:11], a[20:23]\n"
"v_exp_f32 v70, v70\n"
"v_mfma_f32_16x16x32_bf16 a[24:27], v[200:203], v[16:19], a[24:27]\n"
"buffer_load_dwordx4 v217, s[44:47], s64 offen offset:1024 nt lds\n"
"v_mfma_f32_16x16x32_bf16 a[24:27], v[204:207], v[48:51], a[24:27]\n"
"v_exp_f32 v71, v71\n"
"v_mfma_f32_16x16x32_bf16 a[28:31], v[204:207], v[56:59], a[28:31]\n"
"ds_read_b64_tr_b16 v[204:205], v232 offset:12416\n"
"v_add_f32 v242, v242, v64\n"
"v_mfma_f32_16x16x32_bf16 a[28:31], v[200:203], v[24:27], a[28:31]\n"
"s_waitcnt lgkmcnt(5)\n"
"ds_read_b64_tr_b16 v[206:207], v232 offset:18560\n"
"ds_read_b64_tr_b16 v[200:201], v232 offset:128\n"
"v_mfma_f32_16x16x32_bf16 a[32:35], v[208:211], v[0:3], a[32:35]\n"
"ds_read_b64_tr_b16 v[202:203], v232 offset:6272\n"
"v_add_f32 v243, v243, v65\n"
"v_mfma_f32_16x16x32_bf16 a[32:35], v[212:215], v[32:35], a[32:35]\n"
"v_cvt_pk_bf16_f32 v64, v64, v65\n"
"v_add_f32 v242, v242, v66\n"
"v_mfma_f32_16x16x32_bf16 a[36:39], v[212:215], v[40:43], a[36:39]\n"
"v_add_f32 v243, v243, v67\n"
"v_cvt_pk_bf16_f32 v65, v66, v67\n"
"v_mfma_f32_16x16x32_bf16 a[36:39], v[208:211], v[8:11], a[36:39]\n"
"v_add_f32 v242, v242, v68\n"
"v_add_f32 v243, v243, v69\n"
"v_mfma_f32_16x16x32_bf16 a[40:43], v[208:211], v[16:19], a[40:43]\n"
"v_cvt_pk_bf16_f32 v66, v68, v69\n"
"v_add_f32 v242, v242, v70\n"
"v_mfma_f32_16x16x32_bf16 a[40:43], v[212:215], v[48:51], a[40:43]\n"
"v_add_f32 v243, v243, v71\n"
"v_mfma_f32_16x16x32_bf16 a[44:47], v[212:215], v[56:59], a[44:47]\n"
"ds_read_b64_tr_b16 v[212:213], v233 offset:12416\n"
"v_cvt_pk_bf16_f32 v67, v70, v71\n"
"v_mfma_f32_16x16x32_bf16 a[44:47], v[208:211], v[24:27], a[44:47]\n"
"s_waitcnt lgkmcnt(5)\n"
"ds_read_b64_tr_b16 v[214:215], v233 offset:18560\n"
"ds_read_b64_tr_b16 v[208:209], v233 offset:128\n"
"v_mfma_f32_16x16x32_bf16 a[48:51], v[192:195], v[0:3], a[48:51]\n"
"ds_read_b64_tr_b16 v[210:211], v233 offset:6272\n"
"v_mfma_f32_16x16x32_bf16 a[48:51], v[196:199], v[32:35], a[48:51]\n"
"v_exp_f32 v72, v72\n"
"v_mfma_f32_16x16x32_bf16 a[52:55], v[196:199], v[40:43], a[52:55]\n"
"buffer_load_dwordx4 v218, s[44:47], s64 offen offset:2048 nt lds\n"
"v_mfma_f32_16x16x32_bf16 a[52:55], v[192:195], v[8:11], a[52:55]\n"
"v_exp_f32 v73, v73\n"
"v_mfma_f32_16x16x32_bf16 a[56:59], v[192:195], v[16:19], a[56:59]\n"
"v_exp_f32 v74, v74\n"
"v_mfma_f32_16x16x32_bf16 a[56:59], v[196:199], v[48:51], a[56:59]\n"
"v_exp_f32 v75, v75\n"
"v_mfma_f32_16x16x32_bf16 a[60:63], v[196:199], v[56:59], a[60:63]\n"
"ds_read_b64_tr_b16 v[196:197], v234 offset:12416\n"
"v_mfma_f32_16x16x32_bf16 a[60:63], v[192:195], v[24:27], a[60:63]\n"
"s_waitcnt lgkmcnt(5)\n"
"ds_read_b64_tr_b16 v[198:199], v234 offset:18560\n"
"ds_read_b64_tr_b16 v[192:193], v234 offset:128\n"
"v_mfma_f32_16x16x32_bf16 a[64:67], v[200:203], v[0:3], a[64:67]\n"
"ds_read_b64_tr_b16 v[194:195], v234 offset:6272\n"
"v_mfma_f32_16x16x32_bf16 a[64:67], v[204:207], v[32:35], a[64:67]\n"
"v_exp_f32 v76, v76\n"
"v_mfma_f32_16x16x32_bf16 a[68:71], v[204:207], v[40:43], a[68:71]\n"
"v_exp_f32 v77, v77\n"
"v_mfma_f32_16x16x32_bf16 a[68:71], v[200:203], v[8:11], a[68:71]\n"
"v_exp_f32 v78, v78\n"
"v_mfma_f32_16x16x32_bf16 a[72:75], v[200:203], v[16:19], a[72:75]\n"
"buffer_load_dwordx4 v219, s[44:47], s64 offen offset:3072 nt lds\n"
"v_mfma_f32_16x16x32_bf16 a[72:75], v[204:207], v[48:51], a[72:75]\n"
"v_exp_f32 v79, v79\n"
"v_mfma_f32_16x16x32_bf16 a[76:79], v[204:207], v[56:59], a[76:79]\n"
"ds_read_b64_tr_b16 v[204:205], v235 offset:12416\n"
"v_add_f32 v244, v244, v72\n"
"v_mfma_f32_16x16x32_bf16 a[76:79], v[200:203], v[24:27], a[76:79]\n"
"s_waitcnt lgkmcnt(5)\n"
"ds_read_b64_tr_b16 v[206:207], v235 offset:18560\n"
"ds_read_b64_tr_b16 v[200:201], v235 offset:128\n"
"v_mfma_f32_16x16x32_bf16 a[80:83], v[208:211], v[0:3], a[80:83]\n"
"ds_read_b64_tr_b16 v[202:203], v235 offset:6272\n"
"v_add_f32 v245, v245, v73\n"
"v_mfma_f32_16x16x32_bf16 a[80:83], v[212:215], v[32:35], a[80:83]\n"
"v_cvt_pk_bf16_f32 v72, v72, v73\n"
"v_add_f32 v244, v244, v74\n"
"v_mfma_f32_16x16x32_bf16 a[84:87], v[212:215], v[40:43], a[84:87]\n"
"v_add_f32 v245, v245, v75\n"
"v_cvt_pk_bf16_f32 v73, v74, v75\n"
"v_mfma_f32_16x16x32_bf16 a[84:87], v[208:211], v[8:11], a[84:87]\n"
"v_add_f32 v244, v244, v76\n"
"v_add_f32 v245, v245, v77\n"
"v_mfma_f32_16x16x32_bf16 a[88:91], v[208:211], v[16:19], a[88:91]\n"
"v_cvt_pk_bf16_f32 v74, v76, v77\n"
"v_add_f32 v244, v244, v78\n"
"v_mfma_f32_16x16x32_bf16 a[88:91], v[212:215], v[48:51], a[88:91]\n"
"v_add_f32 v245, v245, v79\n"
"v_cvt_pk_bf16_f32 v75, v78, v79\n"
"v_mfma_f32_16x16x32_bf16 a[92:95], v[212:215], v[56:59], a[92:95]\n"
"ds_read_b64_tr_b16 v[212:213], v232 offset:12544\n"
"v_mfma_f32_16x16x32_bf16 a[92:95], v[208:211], v[24:27], a[92:95]\n"
"s_waitcnt lgkmcnt(5)\n"
"ds_read_b64_tr_b16 v[214:215], v232 offset:18688\n"
"ds_read_b64_tr_b16 v[208:209], v232 offset:256\n"
"v_mfma_f32_16x16x32_bf16 a[96:99], v[192:195], v[0:3], a[96:99]\n"
"ds_read_b64_tr_b16 v[210:211], v232 offset:6400\n"
"v_mfma_f32_16x16x32_bf16 a[96:99], v[196:199], v[32:35], a[96:99]\n"
"s_add_u32 m0, s73, 0x7000\n"
"v_exp_f32 v80, v80\n"
"v_mfma_f32_16x16x32_bf16 a[100:103], v[196:199], v[40:43], a[100:103]\n"
"buffer_load_dwordx4 v220, s[44:47], s64 offen offset:0 nt lds\n"
"v_mfma_f32_16x16x32_bf16 a[100:103], v[192:195], v[8:11], a[100:103]\n"
"v_exp_f32 v81, v81\n"
"v_mfma_f32_16x16x32_bf16 a[104:107], v[192:195], v[16:19], a[104:107]\n"
"v_exp_f32 v82, v82\n"
"v_mfma_f32_16x16x32_bf16 a[104:107], v[196:199], v[48:51], a[104:107]\n"
"v_exp_f32 v83, v83\n"
"v_mfma_f32_16x16x32_bf16 a[108:111], v[196:199], v[56:59], a[108:111]\n"
"ds_read_b64_tr_b16 v[196:197], v233 offset:12544\n"
"v_mfma_f32_16x16x32_bf16 a[108:111], v[192:195], v[24:27], a[108:111]\n"
"s_waitcnt lgkmcnt(5)\n"
"ds_read_b64_tr_b16 v[198:199], v233 offset:18688\n"
"ds_read_b64_tr_b16 v[192:193], v233 offset:256\n"
"v_mfma_f32_16x16x32_bf16 a[112:115], v[200:203], v[0:3], a[112:115]\n"
"ds_read_b64_tr_b16 v[194:195], v233 offset:6400\n"
"v_mfma_f32_16x16x32_bf16 a[112:115], v[204:207], v[32:35], a[112:115]\n"
"v_exp_f32 v84, v84\n"
"v_mfma_f32_16x16x32_bf16 a[116:119], v[204:207], v[40:43], a[116:119]\n"
"v_exp_f32 v85, v85\n"
"v_mfma_f32_16x16x32_bf16 a[116:119], v[200:203], v[8:11], a[116:119]\n"
"v_exp_f32 v86, v86\n"
"v_mfma_f32_16x16x32_bf16 a[120:123], v[200:203], v[16:19], a[120:123]\n"
"buffer_load_dwordx4 v221, s[44:47], s64 offen offset:1024 nt lds\n"
"v_mfma_f32_16x16x32_bf16 a[120:123], v[204:207], v[48:51], a[120:123]\n"
"v_exp_f32 v87, v87\n"
"v_mfma_f32_16x16x32_bf16 a[124:127], v[204:207], v[56:59], a[124:127]\n"
"ds_read_b64_tr_b16 v[204:205], v234 offset:12544\n"
"v_add_f32 v246, v246, v80\n"
"v_mfma_f32_16x16x32_bf16 a[124:127], v[200:203], v[24:27], a[124:127]\n"
"s_waitcnt lgkmcnt(5)\n"
"ds_read_b64_tr_b16 v[206:207], v234 offset:18688\n"
"ds_read_b64_tr_b16 v[200:201], v234 offset:256\n"
"v_mfma_f32_16x16x32_bf16 a[128:131], v[208:211], v[0:3], a[128:131]\n"
"s_add_u32 s64, s64, 0x6000\n"
"ds_read_b64_tr_b16 v[202:203], v234 offset:6400\n"
"v_add_f32 v247, v247, v81\n"
"v_mfma_f32_16x16x32_bf16 a[128:131], v[212:215], v[32:35], a[128:131]\n"
"v_cvt_pk_bf16_f32 v80, v80, v81\n"
"v_add_f32 v246, v246, v82\n"
"v_mfma_f32_16x16x32_bf16 a[132:135], v[212:215], v[40:43], a[132:135]\n"
"v_add_f32 v247, v247, v83\n"
"v_cvt_pk_bf16_f32 v81, v82, v83\n"
"v_mfma_f32_16x16x32_bf16 a[132:135], v[208:211], v[8:11], a[132:135]\n"
"v_add_f32 v246, v246, v84\n"
"v_add_f32 v247, v247, v85\n"
"v_mfma_f32_16x16x32_bf16 a[136:139], v[208:211], v[16:19], a[136:139]\n"
"v_cvt_pk_bf16_f32 v82, v84, v85\n"
"v_add_f32 v246, v246, v86\n"
"v_mfma_f32_16x16x32_bf16 a[136:139], v[212:215], v[48:51], a[136:139]\n"
"s_memrealtime s[82:83]\n"
"v_exp_f32 v88, v88\n"
"v_mfma_f32_16x16x32_bf16 a[140:143], v[212:215], v[56:59], a[140:143]\n"
"ds_read_b64_tr_b16 v[212:213], v235 offset:12544\n"
"v_add_f32 v247, v247, v87\n"
"v_mfma_f32_16x16x32_bf16 a[140:143], v[208:211], v[24:27], a[140:143]\n"
"s_waitcnt lgkmcnt(5)\n"
"ds_read_b64_tr_b16 v[214:215], v235 offset:18688\n"
"ds_read_b64_tr_b16 v[208:209], v235 offset:256\n"
"v_mfma_f32_16x16x32_bf16 a[144:147], v[192:195], v[0:3], a[144:147]\n"
"ds_read_b64_tr_b16 v[210:211], v235 offset:6400\n"
"v_cvt_pk_bf16_f32 v83, v86, v87\n"
"v_mfma_f32_16x16x32_bf16 a[144:147], v[196:199], v[32:35], a[144:147]\n"
"v_exp_f32 v89, v89\n"
"v_mfma_f32_16x16x32_bf16 a[148:151], v[196:199], v[40:43], a[148:151]\n"
"v_exp_f32 v90, v90\n"
"v_mfma_f32_16x16x32_bf16 a[148:151], v[192:195], v[8:11], a[148:151]\n"
"v_exp_f32 v91, v91\n"
"v_mfma_f32_16x16x32_bf16 a[152:155], v[192:195], v[16:19], a[152:155]\n"
"v_exp_f32 v92, v92\n"
"v_mfma_f32_16x16x32_bf16 a[152:155], v[196:199], v[48:51], a[152:155]\n"
"v_mfma_f32_16x16x32_bf16 a[156:159], v[196:199], v[56:59], a[156:159]\n"
"s_waitcnt vmcnt(12) lgkmcnt(0)\n"
"s_sub_u32 s83, s94, s82\n"
"s_cmp_gt_i32 s83, 0\n"
"s_cbranch_scc0 Lrpc4%=\n"
"s_min_i32 s83, s83, 0xc8\n"
"Lqpc4%=:\n"
"s_sleep 1\n"
"s_sub_u32 s83, s83, 4\n"
"s_cmp_gt_i32 s83, 0\n"
"s_cbranch_scc1 Lqpc4%=\n"
"Lrpc4%=:\n"
"s_barrier\n"
"ds_read_b128 v[196:199], v231 offset:0\n"
"v_mfma_f32_16x16x32_bf16 a[156:159], v[192:195], v[24:27], a[156:159]\n"
"s_sub_u32 s6, 120, s71\n"
"s_max_i32 s6, s6, 4\n"
"ds_read_b128 v[192:195], v230 offset:0\n"
"v_mfma_f32_16x16x32_bf16 a[160:163], v[200:203], v[0:3], a[160:163]\n"
"s_sub_u32 s82, s82, 16\n"
"v_exp_f32 v93, v93\n"
"v_mfma_f32_16x16x32_bf16 a[160:163], v[204:207], v[32:35], a[160:163]\n"
"s_sub_u32 s7, s71, s29\n"
"s_add_u32 s7, s7, 10\n"
"v_exp_f32 v94, v94\n"
"v_mfma_f32_16x16x32_bf16 a[164:167], v[204:207], v[40:43], a[164:167]\n"
"s_sub_u32 s28, s82, s94\n"
"s_cmp_gt_i32 s28, s6\n"
"s_cselect_b32 s28, s7, -5\n"
"v_exp_f32 v95, v95\n"
"v_mfma_f32_16x16x32_bf16 a[164:167], v[200:203], v[8:11], a[164:167]\n"
"s_add_u32 s71, s71, s28\n"
"s_min_i32 s71, s71, 152\n"
"s_max_i32 s71, s71, s29\n"
"v_add_f32 v248, v248, v88\n"
"v_add_f32 v249, v249, v89\n"
"v_mfma_f32_16x16x32_bf16 a[168:171], v[200:203], v[16:19], a[168:171]\n"
"s_max_i32 s94, s94, s82\n"
"v_cvt_pk_bf16_f32 v88, v88, v89\n"
"v_mfma_f32_16x16x32_bf16 a[168:171], v[204:207], v[48:51], a[168:171]\n"
"s_add_u32 s94, s94, s71\n"
"v_add_f32 v248, v248, v90\n"
"v_mfma_f32_16x16x32_bf16 a[172:175], v[204:207], v[56:59], a[172:175]\n"
"ds_read_b128 v[204:207], v231 offset:128\n"
"v_add_f32 v249, v249, v91\n"
"v_mfma_f32_16x16x32_bf16 a[172:175], v[200:203], v[24:27], a[172:175]\n"
"s_sub_u32 s28, s71, 36\n"
"s_max_i32 s29, s29, s28\n"
"ds_read_b128 v[200:203], v230 offset:128\n"
"v_mfma_f32_16x16x32_bf16 a[176:179], v[208:211], v[0:3], a[176:179]\n"
"v_cvt_pk_bf16_f32 v89, v90, v91\n"
"v_add_f32 v248, v248, v92\n"
"v_mfma_f32_16x16x32_bf16 a[176:179], v[212:215], v[32:35], a[176:179]\n"
"v_add_f32 v249, v249, v93\n"
"v_cvt_pk_bf16_f32 v90, v92, v93\n"
"v_mfma_f32_16x16x32_bf16 a[180:183], v[212:215], v[40:43], a[180:183]\n"
"v_add_f32 v248, v248, v94\n"
"v_mfma_f32_16x16x32_bf16 a[180:183], v[208:211], v[8:11], a[180:183]\n"
"v_add_f32 v249, v249, v95\n"
"v_mfma_f32_16x16x32_bf16 a[184:187], v[208:211], v[16:19], a[184:187]\n"
"v_cvt_pk_bf16_f32 v91, v94, v95\n"
"v_mfma_f32_16x16x32_bf16 a[184:187], v[212:215], v[48:51], a[184:187]\n"
"v_exp_f32 v96, v96\n"
"v_mfma_f32_16x16x32_bf16 a[188:191], v[212:215], v[56:59], a[188:191]\n"
"ds_read_b128 v[212:215], v231 offset:256\n"
"v_mfma_f32_16x16x32_bf16 a[188:191], v[208:211], v[24:27], a[188:191]\n"
"s_waitcnt lgkmcnt(3)\n"
"ds_read_b128 v[208:211], v230 offset:256\n"
"v_mfma_f32_16x16x32_bf16 v[0:3], v[192:195], a[192:195], v[132:135]\n"
"v_exp_f32 v97, v97\n"
"v_mfma_f32_16x16x32_bf16 v[0:3], v[196:199], a[208:211], v[0:3]\n"
"s_add_u32 m0, s73, 0xc000\n"
"v_exp_f32 v98, v98\n"
"v_mfma_f32_16x16x32_bf16 v[8:11], v[196:199], a[212:215], v[140:143]\n"
"buffer_load_dwordx4 v216, s[48:51], s65 offen offset:0 nt lds\n"
"v_mfma_f32_16x16x32_bf16 v[8:11], v[192:195], a[196:199], v[8:11]\n"
"v_exp_f32 v99, v99\n"
"v_mfma_f32_16x16x32_bf16 v[16:19], v[192:195], a[200:203], v[148:151]\n"
"v_exp_f32 v100, v100\n"
"v_mfma_f32_16x16x32_bf16 v[16:19], v[196:199], a[216:219], v[16:19]\n"
"v_exp_f32 v101, v101\n"
"v_mfma_f32_16x16x32_bf16 v[24:27], v[196:199], a[220:223], v[156:159]\n"
"ds_read_b128 v[196:199], v231 offset:6144\n"
"v_mfma_f32_16x16x32_bf16 v[24:27], v[192:195], a[204:207], v[24:27]\n"
"s_waitcnt lgkmcnt(3)\n"
"ds_read_b128 v[192:195], v230 offset:6144\n"
"v_mfma_f32_16x16x32_bf16 v[0:3], v[200:203], a[224:227], v[0:3]\n"
"v_exp_f32 v102, v102\n"
"v_mfma_f32_16x16x32_bf16 v[0:3], v[204:207], a[240:243], v[0:3]\n"
"v_exp_f32 v103, v103\n"
"v_mfma_f32_16x16x32_bf16 v[8:11], v[204:207], a[244:247], v[8:11]\n"
"v_add_f32 v242, v242, v96\n"
"v_mfma_f32_16x16x32_bf16 v[8:11], v[200:203], a[228:231], v[8:11]\n"
"v_add_f32 v243, v243, v97\n"
"v_cvt_pk_bf16_f32 v96, v96, v97\n"
"v_mfma_f32_16x16x32_bf16 v[16:19], v[200:203], a[232:235], v[16:19]\n"
"buffer_load_dwordx4 v217, s[48:51], s65 offen offset:1024 nt lds\n"
"v_mfma_f32_16x16x32_bf16 v[16:19], v[204:207], a[248:251], v[16:19]\n"
"v_add_f32 v242, v242, v98\n"
"v_add_f32 v243, v243, v99\n"
"v_mfma_f32_16x16x32_bf16 v[24:27], v[204:207], a[252:255], v[24:27]\n"
"ds_read_b128 v[204:207], v231 offset:6272\n"
"v_cvt_pk_bf16_f32 v97, v98, v99\n"
"v_mfma_f32_16x16x32_bf16 v[24:27], v[200:203], a[236:239], v[24:27]\n"
"s_waitcnt lgkmcnt(3)\n"
"ds_read_b128 v[200:203], v230 offset:6272\n"
"v_add_f32 v242, v242, v100\n"
"v_mfma_f32_16x16x32_bf16 v[0:3], v[208:211], v[128:131], v[0:3]\n"
"v_add_f32 v243, v243, v101\n"
"v_cvt_pk_bf16_f32 v98, v100, v101\n"
"v_mfma_f32_16x16x32_bf16 v[0:3], v[212:215], v[160:163], v[0:3]\n"
"v_add_f32 v242, v242, v102\n"
"v_mfma_f32_16x16x32_bf16 v[8:11], v[212:215], v[168:171], v[8:11]\n"
"v_add_f32 v243, v243, v103\n"
"v_mfma_f32_16x16x32_bf16 v[8:11], v[208:211], v[136:139], v[8:11]\n"
"v_cvt_pk_bf16_f32 v99, v102, v103\n"
"v_mfma_f32_16x16x32_bf16 v[16:19], v[208:211], v[144:147], v[16:19]\n"
"v_exp_f32 v104, v104\n"
"v_mfma_f32_16x16x32_bf16 v[16:19], v[212:215], v[176:179], v[16:19]\n"
"v_exp_f32 v105, v105\n"
"v_mfma_f32_16x16x32_bf16 v[24:27], v[212:215], v[184:187], v[24:27]\n"
"ds_read_b128 v[212:215], v231 offset:6400\n"
"v_mfma_f32_16x16x32_bf16 v[24:27], v[208:211], v[152:155], v[24:27]\n"
"s_waitcnt lgkmcnt(3)\n"
"ds_read_b128 v[208:211], v230 offset:6400\n"
"v_mfma_f32_16x16x32_bf16 v[4:7], v[192:195], a[192:195], v[132:135]\n"
"v_exp_f32 v106, v106\n"
"v_mfma_f32_16x16x32_bf16 v[4:7], v[196:199], a[208:211], v[4:7]\n"
"v_exp_f32 v107, v107\n"
"v_mfma_f32_16x16x32_bf16 v[12:15], v[196:199], a[212:215], v[140:143]\n"
"buffer_load_dwordx4 v218, s[48:51], s65 offen offset:2048 nt lds\n"
"v_mfma_f32_16x16x32_bf16 v[12:15], v[192:195], a[196:199], v[12:15]\n"
"v_exp_f32 v108, v108\n"
"v_mfma_f32_16x16x32_bf16 v[20:23], v[192:195], a[200:203], v[148:151]\n"
"v_exp_f32 v109, v109\n"
"v_mfma_f32_16x16x32_bf16 v[20:23], v[196:199], a[216:219], v[20:23]\n"
"v_mfma_f32_16x16x32_bf16 v[28:31], v[196:199], a[220:223], v[156:159]\n"
"ds_read_b128 v[196:199], v231 offset:12288\n"
"v_mfma_f32_16x16x32_bf16 v[28:31], v[192:195], a[204:207], v[28:31]\n"
"s_waitcnt lgkmcnt(3)\n"
"ds_read_b128 v[192:195], v230 offset:12288\n"
"v_mfma_f32_16x16x32_bf16 v[4:7], v[200:203], a[224:227], v[4:7]\n"
"v_exp_f32 v110, v110\n"
"v_mfma_f32_16x16x32_bf16 v[4:7], v[204:207], a[240:243], v[4:7]\n"
"v_exp_f32 v111, v111\n"
"v_mfma_f32_16x16x32_bf16 v[12:15], v[204:207], a[244:247], v[12:15]\n"
"v_add_f32 v244, v244, v104\n"
"v_add_f32 v245, v245, v105\n"
"v_mfma_f32_16x16x32_bf16 v[12:15], v[200:203], a[228:231], v[12:15]\n"
"v_cvt_pk_bf16_f32 v104, v104, v105\n"
"v_mfma_f32_16x16x32_bf16 v[20:23], v[200:203], a[232:235], v[20:23]\n"
"buffer_load_dwordx4 v219, s[48:51], s65 offen offset:3072 nt lds\n"
"v_mfma_f32_16x16x32_bf16 v[20:23], v[204:207], a[248:251], v[20:23]\n"
"v_add_f32 v244, v244, v106\n"
"v_add_f32 v245, v245, v107\n"
"v_mfma_f32_16x16x32_bf16 v[28:31], v[204:207], a[252:255], v[28:31]\n"
"ds_read_b128 v[204:207], v231 offset:12416\n"
"v_cvt_pk_bf16_f32 v105, v106, v107\n"
"v_mfma_f32_16x16x32_bf16 v[28:31], v[200:203], a[236:239], v[28:31]\n"
"s_waitcnt lgkmcnt(3)\n"
"ds_read_b128 v[200:203], v230 offset:12416\n"
"v_add_f32 v244, v244, v108\n"
"v_mfma_f32_16x16x32_bf16 v[4:7], v[208:211], v[128:131], v[4:7]\n"
"v_add_f32 v245, v245, v109\n"
"v_cvt_pk_bf16_f32 v106, v108, v109\n"
"v_mfma_f32_16x16x32_bf16 v[4:7], v[212:215], v[160:163], v[4:7]\n"
"v_add_f32 v244, v244, v110\n"
"v_add_f32 v245, v245, v111\n"
"v_mfma_f32_16x16x32_bf16 v[12:15], v[212:215], v[168:171], v[12:15]\n"
"v_cvt_pk_bf16_f32 v107, v110, v111\n"
"v_mfma_f32_16x16x32_bf16 v[12:15], v[208:211], v[136:139], v[12:15]\n"
"v_exp_f32 v112, v112\n"
"v_mfma_f32_16x16x32_bf16 v[20:23], v[208:211], v[144:147], v[20:23]\n"
"v_mfma_f32_16x16x32_bf16 v[20:23], v[212:215], v[176:179], v[20:23]\n"
"v_exp_f32 v113, v113\n"
"v_mfma_f32_16x16x32_bf16 v[28:31], v[212:215], v[184:187], v[28:31]\n"
"ds_read_b128 v[212:215], v231 offset:12544\n"
"v_mfma_f32_16x16x32_bf16 v[28:31], v[208:211], v[152:155], v[28:31]\n"
"s_waitcnt lgkmcnt(3)\n"
"ds_read_b128 v[208:211], v230 offset:12544\n"
"v_mfma_f32_16x16x32_bf16 v[32:35], v[192:195], a[192:195], v[132:135]\n"
"v_exp_f32 v114, v114\n"
"v_mfma_f32_16x16x32_bf16 v[32:35], v[196:199], a[208:211], v[32:35]\n"
"s_add_u32 m0, s73, 0xd000\n"
"v_exp_f32 v115, v115\n"
"v_mfma_f32_16x16x32_bf16 v[40:43], v[196:199], a[212:215], v[140:143]\n"
"buffer_load_dwordx4 v220, s[48:51], s65 offen offset:0 nt lds\n"
"v_mfma_f32_16x16x32_bf16 v[40:43], v[192:195], a[196:199], v[40:43]\n"
"v_exp_f32 v116, v116\n"
"v_mfma_f32_16x16x32_bf16 v[48:51], v[192:195], a[200:203], v[148:151]\n"
"v_exp_f32 v117, v117\n"
"v_mfma_f32_16x16x32_bf16 v[48:51], v[196:199], a[216:219], v[48:51]\n"
"v_exp_f32 v118, v118\n"
"v_mfma_f32_16x16x32_bf16 v[56:59], v[196:199], a[220:223], v[156:159]\n"
"ds_read_b128 v[196:199], v231 offset:18432\n"
"v_mfma_f32_16x16x32_bf16 v[56:59], v[192:195], a[204:207], v[56:59]\n"
"s_waitcnt lgkmcnt(3)\n"
"ds_read_b128 v[192:195], v230 offset:18432\n"
"v_mfma_f32_16x16x32_bf16 v[32:35], v[200:203], a[224:227], v[32:35]\n"
"v_exp_f32 v119, v119\n"
"v_mfma_f32_16x16x32_bf16 v[32:35], v[204:207], a[240:243], v[32:35]\n"
"v_add_f32 v246, v246, v112\n"
"v_add_f32 v247, v247, v113\n"
"Ltp8%=:\n"
"s_cbranch_execz Ltrp%=\n"
"v_mfma_f32_16x16x32_bf16 v[40:43], v[204:207], a[244:247], v[40:43]\n"
"v_cvt_pk_bf16_f32 v112, v112, v113\n"
"v_mfma_f32_16x16x32_bf16 v[40:43], v[200:203], a[228:231], v[40:43]\n"
"v_add_f32 v246, v246, v114\n"
"v_mfma_f32_16x16x32_bf16 v[48:51], v[200:203], a[232:235], v[48:51]\n"
"buffer_load_dwordx4 v221, s[48:51], s65 offen offset:1024 nt lds\n"
"v_mfma_f32_16x16x32_bf16 v[48:51], v[204:207], a[248:251], v[48:51]\n"
"v_add_f32 v247, v247, v115\n"
"v_cvt_pk_bf16_f32 v113, v114, v115\n"
"v_mfma_f32_16x16x32_bf16 v[56:59], v[204:207], a[252:255], v[56:59]\n"
"ds_read_b128 v[204:207], v231 offset:18560\n"
"v_add_f32 v246, v246, v116\n"
"v_mfma_f32_16x16x32_bf16 v[56:59], v[200:203], a[236:239], v[56:59]\n"
"s_waitcnt lgkmcnt(3)\n"
"ds_read_b128 v[200:203], v230 offset:18560\n"
"v_add_f32 v247, v247, v117\n"
"v_mfma_f32_16x16x32_bf16 v[32:35], v[208:211], v[128:131], v[32:35]\n"
"s_add_u32 s65, s65, 0x6000\n"
"v_cvt_pk_bf16_f32 v114, v116, v117\n"
"v_add_f32 v246, v246, v118\n"
"v_mfma_f32_16x16x32_bf16 v[32:35], v[212:215], v[160:163], v[32:35]\n"
"v_add_f32 v247, v247, v119\n"
"v_cvt_pk_bf16_f32 v115, v118, v119\n"
"v_mfma_f32_16x16x32_bf16 v[40:43], v[212:215], v[168:171], v[40:43]\n"
"v_exp_f32 v120, v120\n"
"v_mfma_f32_16x16x32_bf16 v[40:43], v[208:211], v[136:139], v[40:43]\n"
"v_mfma_f32_16x16x32_bf16 v[48:51], v[208:211], v[144:147], v[48:51]\n"
"v_exp_f32 v121, v121\n"
"v_mfma_f32_16x16x32_bf16 v[48:51], v[212:215], v[176:179], v[48:51]\n"
"s_memrealtime s[82:83]\n"
"v_mfma_f32_16x16x32_bf16 v[56:59], v[212:215], v[184:187], v[56:59]\n"
"ds_read_b128 v[212:215], v231 offset:18688\n"
"v_mfma_f32_16x16x32_bf16 v[56:59], v[208:211], v[152:155], v[56:59]\n"
"s_waitcnt lgkmcnt(3)\n"
"ds_read_b128 v[208:211], v230 offset:18688\n"
"v_mfma_f32_16x16x32_bf16 v[36:39], v[192:195], a[192:195], v[132:135]\n"
"v_exp_f32 v122, v122\n"
"v_mfma_f32_16x16x32_bf16 v[36:39], v[196:199], a[208:211], v[36:39]\n"
"v_exp_f32 v123, v123\n"
"v_mfma_f32_16x16x32_bf16 v[44:47], v[196:199], a[212:215], v[140:143]\n"
"v_exp_f32 v124, v124\n"
"v_mfma_f32_16x16x32_bf16 v[44:47], v[192:195], a[196:199], v[44:47]\n"
"v_exp_f32 v125, v125\n"
"v_mfma_f32_16x16x32_bf16 v[52:55], v[192:195], a[200:203], v[148:151]\n"
"v_exp_f32 v126, v126\n"
"v_mfma_f32_16x16x32_bf16 v[52:55], v[196:199], a[216:219], v[52:55]\n"
"v_mfma_f32_16x16x32_bf16 v[60:63], v[196:199], a[220:223], v[156:159]\n"
"s_waitcnt vmcnt(12) lgkmcnt(0)\n"
"s_sub_u32 s83, s94, s82\n"
"s_cmp_gt_i32 s83, 0\n"
"s_cbranch_scc0 Lrpc5%=\n"
"s_min_i32 s83, s83, 0xc8\n"
"Lqpc5%=:\n"
"s_sleep 1\n"
"s_sub_u32 s83, s83, 4\n"
"s_cmp_gt_i32 s83, 0\n"
"s_cbranch_scc1 Lqpc5%=\n"
"Lrpc5%=:\n"
"s_barrier\n"
"ds_read_b64_tr_b16 v[196:197], v232 offset:36864\n"
"v_mfma_f32_16x16x32_bf16 v[60:63], v[192:195], a[204:207], v[60:63]\n"
"s_sub_u32 s6, 120, s71\n"
"s_max_i32 s6, s6, 4\n"
"ds_read_b64_tr_b16 v[198:199], v232 offset:43008\n"
"ds_read_b64_tr_b16 v[192:193], v232 offset:24576\n"
"v_mfma_f32_16x16x32_bf16 v[36:39], v[200:203], a[224:227], v[36:39]\n"
"s_sub_u32 s82, s82, 16\n"
"ds_read_b64_tr_b16 v[194:195], v232 offset:30720\n"
"v_add_f32 v248, v248, v120\n"
"v_mfma_f32_16x16x32_bf16 v[36:39], v[204:207], a[240:243], v[36:39]\n"
"s_sub_u32 s7, s71, s29\n"
"s_add_u32 s7, s7, 10\n"
"v_exp_f32 v127, v127\n"
"v_mfma_f32_16x16x32_bf16 v[44:47], v[204:207], a[244:247], v[44:47]\n"
"s_sub_u32 s28, s82, s94\n"
"s_cmp_gt_i32 s28, s6\n"
"s_cselect_b32 s28, s7, -5\n"
"v_add_f32 v249, v249, v121\n"
"v_cvt_pk_bf16_f32 v120, v120, v121\n"
"v_mfma_f32_16x16x32_bf16 v[44:47], v[200:203], a[228:231], v[44:47]\n"
"s_add_u32 s71, s71, s28\n"
"s_min_i32 s71, s71, 152\n"
"s_max_i32 s71, s71, s29\n"
"v_add_f32 v248, v248, v122\n"
"v_add_f32 v249, v249, v123\n"
"v_mfma_f32_16x16x32_bf16 v[52:55], v[200:203], a[232:235], v[52:55]\n"
"s_max_i32 s94, s94, s82\n"
"v_cvt_pk_bf16_f32 v121, v122, v123\n"
"v_mfma_f32_16x16x32_bf16 v[52:55], v[204:207], a[248:251], v[52:55]\n"
"s_add_u32 s94, s94, s71\n"
"v_add_f32 v248, v248, v124\n"
"v_add_f32 v249, v249, v125\n"
"v_mfma_f32_16x16x32_bf16 v[60:63], v[204:207], a[252:255], v[60:63]\n"
"ds_read_b64_tr_b16 v[204:205], v233 offset:36864\n"
"v_cvt_pk_bf16_f32 v122, v124, v125\n"
"v_mfma_f32_16x16x32_bf16 v[60:63], v[200:203], a[236:239], v[60:63]\n"
"s_sub_u32 s28, s71, 36\n"
"s_max_i32 s29, s29, s28\n"
"ds_read_b64_tr_b16 v[206:207], v233 offset:43008\n"
"ds_read_b64_tr_b16 v[200:201], v233 offset:24576\n"
"v_mfma_f32_16x16x32_bf16 v[36:39], v[208:211], v[128:131], v[36:39]\n"
"ds_read_b64_tr_b16 v[202:203], v233 offset:30720\n"
"v_add_f32 v248, v248, v126\n"
"v_mfma_f32_16x16x32_bf16 v[36:39], v[212:215], v[160:163], v[36:39]\n"
"v_add_f32 v249, v249, v127\n"
"v_cvt_pk_bf16_f32 v123, v126, v127\n"
"v_mfma_f32_16x16x32_bf16 v[44:47], v[212:215], v[168:171], v[44:47]\n"
"v_mfma_f32_16x16x32_bf16 v[44:47], v[208:211], v[136:139], v[44:47]\n"
"v_mfma_f32_16x16x32_bf16 v[52:55], v[208:211], v[144:147], v[52:55]\n"
"v_mfma_f32_16x16x32_bf16 v[52:55], v[212:215], v[176:179], v[52:55]\n"
"v_mfma_f32_16x16x32_bf16 v[60:63], v[212:215], v[184:187], v[60:63]\n"
"s_sub_u32 s56, s56, 1\n"
"ds_read_b64_tr_b16 v[212:213], v234 offset:36864\n"
"v_mfma_f32_16x16x32_bf16 v[60:63], v[208:211], v[152:155], v[60:63]\n"
"s_waitcnt lgkmcnt(5)\n"
"ds_read_b64_tr_b16 v[214:215], v234 offset:43008\n"
"ds_read_b64_tr_b16 v[208:209], v234 offset:24576\n"
"s_cbranch_scc0 La%=\n"
"Lwx%=:\n"
"v_mfma_f32_16x16x32_bf16 a[0:3], v[192:195], v[64:67], a[0:3]\n"
"s_mov_b64 s[36:37], s[34:35]\n"
"ds_read_b64_tr_b16 v[210:211], v234 offset:30720\n"
"v_mbcnt_lo_u32_b32 v172, -1, 0\n"
"v_mfma_f32_16x16x32_bf16 a[0:3], v[196:199], v[96:99], a[0:3]\n"
"s_mov_b32 s38, 0xa0000\n"
"v_mbcnt_hi_u32_b32 v172, -1, v172\n"
"v_lshrrev_b32 v173, 2, v172\n"
"v_mfma_f32_16x16x32_bf16 a[4:7], v[196:199], v[104:107], a[4:7]\n"
"s_mov_b64 s[44:45], s[20:21]\n"
"v_mul_u32_u24 v173, 0xa0, v173\n"
"v_and_b32 v174, 3, v172\n"
"v_mfma_f32_16x16x32_bf16 a[4:7], v[192:195], v[72:75], a[4:7]\n"
"buffer_load_dword v255, v188, s[40:43], s70 offen offset:0\n"
"v_mfma_f32_16x16x32_bf16 a[8:11], v[192:195], v[80:83], a[8:11]\n"
"s_mov_b32 s46, 0x9c40\n"
"v_lshl_add_u32 v227, v174, 3, v173\n"
"v_and_b32 v173, 15, v172\n"
"v_mfma_f32_16x16x32_bf16 a[8:11], v[196:199], v[112:115], a[8:11]\n"
"s_cmp_lt_u32 s23, 1\n"
"v_mul_u32_u24 v173, 0x500, v173\n"
"v_bfe_u32 v175, v172, 4, 1\n"
"v_mfma_f32_16x16x32_bf16 a[12:15], v[196:199], v[120:123], a[12:15]\n"
"s_cselect_b32 s95, 1, 0\n"
"ds_read_b64_tr_b16 v[196:197], v235 offset:36864\n"
"v_lshl_add_u32 v173, v175, 5, v173\n"
"v_mfma_f32_16x16x32_bf16 a[12:15], v[192:195], v[88:91], a[12:15]\n"
"s_waitcnt lgkmcnt(5)\n"
"ds_read_b64_tr_b16 v[198:199], v235 offset:43008\n"
"ds_read_b64_tr_b16 v[192:193], v235 offset:24576\n"
"v_mfma_f32_16x16x32_bf16 a[16:19], v[200:203], v[64:67], a[16:19]\n"
"s_cmp_lt_u32 s23, 2\n"
"ds_read_b64_tr_b16 v[194:195], v235 offset:30720\n"
"v_lshrrev_b32 v175, 5, v172\n"
"v_mfma_f32_16x16x32_bf16 a[16:19], v[204:207], v[96:99], a[16:19]\n"
"s_cselect_b32 s67, 2, 1\n"
"v_lshl_add_u32 v173, v175, 4, v173\n"
"v_mov_b32 v181, 0x7ff00000\n"
"v_mfma_f32_16x16x32_bf16 a[20:23], v[204:207], v[104:107], a[20:23]\n"
"s_cmp_lt_u32 s23, 3\n"
"v_exp_f32 v0, v0\n"
"v_mfma_f32_16x16x32_bf16 a[20:23], v[200:203], v[72:75], a[20:23]\n"
"v_exp_f32 v1, v1\n"
"v_mfma_f32_16x16x32_bf16 a[24:27], v[200:203], v[80:83], a[24:27]\n"
"s_cselect_b32 s69, 3, 2\n"
"v_exp_f32 v2, v2\n"
"v_mfma_f32_16x16x32_bf16 a[24:27], v[204:207], v[112:115], a[24:27]\n"
"s_lshl_b32 s60, s24, 7\n"
"v_exp_f32 v3, v3\n"
"v_mfma_f32_16x16x32_bf16 a[28:31], v[204:207], v[120:123], a[28:31]\n"
"s_add_u32 s60, s60, 0x100\n"
"ds_read_b64_tr_b16 v[204:205], v232 offset:36992\n"
"v_add_f32 v242, v242, v0\n"
"v_mfma_f32_16x16x32_bf16 a[28:31], v[200:203], v[88:91], a[28:31]\n"
"s_waitcnt lgkmcnt(5)\n"
"ds_read_b64_tr_b16 v[206:207], v232 offset:43136\n"
"ds_read_b64_tr_b16 v[200:201], v232 offset:24704\n"
"v_mfma_f32_16x16x32_bf16 a[32:35], v[208:211], v[64:67], a[32:35]\n"
"s_lshl_b32 s61, s23, 7\n"
"ds_read_b64_tr_b16 v[202:203], v232 offset:30848\n"
"v_add_f32 v243, v243, v1\n"
"v_mfma_f32_16x16x32_bf16 a[32:35], v[212:215], v[96:99], a[32:35]\n"
"s_add_u32 s61, s61, 0x500\n"
"v_exp_f32 v4, v4\n"
"v_mfma_f32_16x16x32_bf16 a[36:39], v[212:215], v[104:107], a[36:39]\n"
"s_mul_i32 s81, %[w], 0x28000\n"
"v_exp_f32 v5, v5\n"
"v_mfma_f32_16x16x32_bf16 a[36:39], v[208:211], v[72:75], a[36:39]\n"
"v_exp_f32 v6, v6\n"
"v_mfma_f32_16x16x32_bf16 a[40:43], v[208:211], v[80:83], a[40:43]\n"
"s_mul_i32 s82, s24, 0x2800\n"
"v_exp_f32 v7, v7\n"
"v_mfma_f32_16x16x32_bf16 a[40:43], v[212:215], v[112:115], a[40:43]\n"
"s_add_u32 s81, s81, s82\n"
"v_cvt_pk_bf16_f32 v0, v0, v1\n"
"v_add_f32 v242, v242, v2\n"
"v_mfma_f32_16x16x32_bf16 a[44:47], v[212:215], v[120:123], a[44:47]\n"
"s_lshr_b32 s83, %[w], 1\n"
"ds_read_b64_tr_b16 v[212:213], v233 offset:36992\n"
"v_add_f32 v243, v243, v3\n"
"v_mfma_f32_16x16x32_bf16 a[44:47], v[208:211], v[88:91], a[44:47]\n"
"s_waitcnt lgkmcnt(5)\n"
"ds_read_b64_tr_b16 v[214:215], v233 offset:43136\n"
"ds_read_b64_tr_b16 v[208:209], v233 offset:24704\n"
"v_mfma_f32_16x16x32_bf16 a[48:51], v[192:195], v[64:67], a[48:51]\n"
"s_and_b32 s62, %[w], 1\n"
"ds_read_b64_tr_b16 v[210:211], v233 offset:30848\n"
"v_cvt_pk_bf16_f32 v1, v2, v3\n"
"v_mfma_f32_16x16x32_bf16 a[48:51], v[196:199], v[96:99], a[48:51]\n"
"s_mul_i32 s82, s24, 0x14000\n"
"v_cvt_pk_bf16_f32 v2, v4, v5\n"
"v_cvt_pk_bf16_f32 v3, v6, v7\n"
"v_mfma_f32_16x16x32_bf16 a[52:55], v[196:199], v[104:107], a[52:55]\n"
"s_lshl_b32 s68, s62, 7\n"
"v_exp_f32 v32, v32\n"
"v_mfma_f32_16x16x32_bf16 a[52:55], v[192:195], v[72:75], a[52:55]\n"
"v_exp_f32 v33, v33\n"
"v_mfma_f32_16x16x32_bf16 a[56:59], v[192:195], v[80:83], a[56:59]\n"
"s_add_u32 s68, s68, s83\n"
"v_exp_f32 v34, v34\n"
"v_mfma_f32_16x16x32_bf16 a[56:59], v[196:199], v[112:115], a[56:59]\n"
"s_lshl_b32 s68, s68, 3\n"
"v_exp_f32 v35, v35\n"
"v_mfma_f32_16x16x32_bf16 a[60:63], v[196:199], v[120:123], a[60:63]\n"
"s_add_u32 s68, s68, s82\n"
"ds_read_b64_tr_b16 v[196:197], v234 offset:36992\n"
"v_add_f32 v242, v242, v4\n"
"v_mfma_f32_16x16x32_bf16 a[60:63], v[192:195], v[88:91], a[60:63]\n"
"s_waitcnt lgkmcnt(5)\n"
"ds_read_b64_tr_b16 v[198:199], v234 offset:43136\n"
"ds_read_b64_tr_b16 v[192:193], v234 offset:24704\n"
"v_mfma_f32_16x16x32_bf16 a[64:67], v[200:203], v[64:67], a[64:67]\n"
"s_add_u32 s68, s68, 0x2000\n"
"ds_read_b64_tr_b16 v[194:195], v234 offset:30848\n"
"v_add_f32 v243, v243, v5\n"
"v_mfma_f32_16x16x32_bf16 a[64:67], v[204:207], v[96:99], a[64:67]\n"
"s_lshl_b32 s58, s62, 1\n"
"v_exp_f32 v36, v36\n"
"v_mfma_f32_16x16x32_bf16 a[68:71], v[204:207], v[104:107], a[68:71]\n"
"s_add_u32 s58, s58, s83\n"
"v_exp_f32 v37, v37\n"
"v_mfma_f32_16x16x32_bf16 a[68:71], v[200:203], v[72:75], a[68:71]\n"
"v_exp_f32 v38, v38\n"
"v_mfma_f32_16x16x32_bf16 a[72:75], v[200:203], v[80:83], a[72:75]\n"
"s_lshl_b32 s58, s58, 10\n"
"v_exp_f32 v39, v39\n"
"v_mfma_f32_16x16x32_bf16 a[72:75], v[204:207], v[112:115], a[72:75]\n"
"s_add_u32 s82, s82, s58\n"
"v_lshl_add_u32 v237, v172, 4, s82\n"
"v_add_f32 v242, v242, v6\n"
"v_mfma_f32_16x16x32_bf16 a[76:79], v[204:207], v[120:123], a[76:79]\n"
"s_mul_i32 s83, s83, 0x5000\n"
"ds_read_b64_tr_b16 v[204:205], v235 offset:36992\n"
"v_add_f32 v243, v243, v7\n"
"v_mfma_f32_16x16x32_bf16 a[76:79], v[200:203], v[88:91], a[76:79]\n"
"s_waitcnt lgkmcnt(5)\n"
"ds_read_b64_tr_b16 v[206:207], v235 offset:43136\n"
"ds_read_b64_tr_b16 v[200:201], v235 offset:24704\n"
"v_mfma_f32_16x16x32_bf16 a[80:83], v[208:211], v[64:67], a[80:83]\n"
"s_mul_i32 s62, s62, 64\n"
"ds_read_b64_tr_b16 v[202:203], v235 offset:30848\n"
"v_add_f32 v242, v242, v32\n"
"v_mfma_f32_16x16x32_bf16 a[80:83], v[212:215], v[96:99], a[80:83]\n"
"s_add_u32 s83, s83, s62\n"
"v_add_u32 v238, s83, v173\n"
"v_and_b32 v173, 7, v172\n"
"v_mfma_f32_16x16x32_bf16 a[84:87], v[212:215], v[104:107], a[84:87]\n"
"s_cmp_eq_u32 %[w], 0\n"
"v_lshlrev_b32 v241, 2, v173\n"
"v_and_b32 v173, 15, v172\n"
"v_mfma_f32_16x16x32_bf16 a[84:87], v[208:211], v[72:75], a[84:87]\n"
"v_lshlrev_b32 v240, 2, v173\n"
"v_add_f32 v243, v243, v33\n"
"v_mfma_f32_16x16x32_bf16 a[88:91], v[208:211], v[80:83], a[88:91]\n"
"s_cselect_b32 s59, 1, 0\n"
"v_cvt_pk_bf16_f32 v32, v32, v33\n"
"v_add_f32 v242, v242, v34\n"
"v_mfma_f32_16x16x32_bf16 a[88:91], v[212:215], v[112:115], a[88:91]\n"
"v_add_f32 v243, v243, v35\n"
"v_cvt_pk_bf16_f32 v33, v34, v35\n"
"v_mfma_f32_16x16x32_bf16 a[92:95], v[212:215], v[120:123], a[92:95]\n"
"ds_read_b64_tr_b16 v[212:213], v232 offset:37120\n"
"v_cvt_pk_bf16_f32 v34, v36, v37\n"
"v_mfma_f32_16x16x32_bf16 a[92:95], v[208:211], v[88:91], a[92:95]\n"
"s_waitcnt lgkmcnt(5)\n"
"ds_read_b64_tr_b16 v[214:215], v232 offset:43264\n"
"ds_read_b64_tr_b16 v[208:209], v232 offset:24832\n"
"v_mfma_f32_16x16x32_bf16 a[96:99], v[192:195], v[64:67], a[96:99]\n"
"ds_read_b64_tr_b16 v[210:211], v232 offset:30976\n"
"v_cvt_pk_bf16_f32 v35, v38, v39\n"
"v_mfma_f32_16x16x32_bf16 a[96:99], v[196:199], v[96:99], a[96:99]\n"
"v_exp_f32 v8, v8\n"
"v_mfma_f32_16x16x32_bf16 a[100:103], v[196:199], v[104:107], a[100:103]\n"
"v_exp_f32 v9, v9\n"
"v_mfma_f32_16x16x32_bf16 a[100:103], v[192:195], v[72:75], a[100:103]\n"
"s_add_u32 s70, s70, 0x14000\n"
"buffer_load_dword v255, v188, s[40:43], s70 offen offset:0\n"
"v_mfma_f32_16x16x32_bf16 a[104:107], v[192:195], v[80:83], a[104:107]\n"
"v_exp_f32 v10, v10\n"
"v_mfma_f32_16x16x32_bf16 a[104:107], v[196:199], v[112:115], a[104:107]\n"
"v_exp_f32 v11, v11\n"
"v_mfma_f32_16x16x32_bf16 a[108:111], v[196:199], v[120:123], a[108:111]\n"
"ds_read_b64_tr_b16 v[196:197], v233 offset:37120\n"
"v_add_f32 v244, v244, v8\n"
"v_mfma_f32_16x16x32_bf16 a[108:111], v[192:195], v[88:91], a[108:111]\n"
"s_waitcnt lgkmcnt(5)\n"
"ds_read_b64_tr_b16 v[198:199], v233 offset:43264\n"
"ds_read_b64_tr_b16 v[192:193], v233 offset:24832\n"
"v_mfma_f32_16x16x32_bf16 a[112:115], v[200:203], v[64:67], a[112:115]\n"
"ds_read_b64_tr_b16 v[194:195], v233 offset:30976\n"
"v_add_f32 v245, v245, v9\n"
"v_mfma_f32_16x16x32_bf16 a[112:115], v[204:207], v[96:99], a[112:115]\n"
"v_exp_f32 v12, v12\n"
"v_mfma_f32_16x16x32_bf16 a[116:119], v[204:207], v[104:107], a[116:119]\n"
"v_exp_f32 v13, v13\n"
"v_mfma_f32_16x16x32_bf16 a[116:119], v[200:203], v[72:75], a[116:119]\n"
"v_exp_f32 v14, v14\n"
"v_mfma_f32_16x16x32_bf16 a[120:123], v[200:203], v[80:83], a[120:123]\n"
"v_exp_f32 v15, v15\n"
"v_mfma_f32_16x16x32_bf16 a[120:123], v[204:207], v[112:115], a[120:123]\n"
"v_exp_f32 v40, v40\n"
"v_mfma_f32_16x16x32_bf16 a[124:127], v[204:207], v[120:123], a[124:127]\n"
"ds_read_b64_tr_b16 v[204:205], v234 offset:37120\n"
"v_add_f32 v244, v244, v10\n"
"v_mfma_f32_16x16x32_bf16 a[124:127], v[200:203], v[88:91], a[124:127]\n"
"s_waitcnt lgkmcnt(5)\n"
"ds_read_b64_tr_b16 v[206:207], v234 offset:43264\n"
"ds_read_b64_tr_b16 v[200:201], v234 offset:24832\n"
"v_mfma_f32_16x16x32_bf16 a[128:131], v[208:211], v[64:67], a[128:131]\n"
"ds_read_b64_tr_b16 v[202:203], v234 offset:30976\n"
"v_add_f32 v245, v245, v11\n"
"v_mfma_f32_16x16x32_bf16 a[128:131], v[212:215], v[96:99], a[128:131]\n"
"v_exp_f32 v41, v41\n"
"v_mfma_f32_16x16x32_bf16 a[132:135], v[212:215], v[104:107], a[132:135]\n"
"v_exp_f32 v42, v42\n"
"v_mfma_f32_16x16x32_bf16 a[132:135], v[208:211], v[72:75], a[132:135]\n"
"v_exp_f32 v43, v43\n"
"v_mfma_f32_16x16x32_bf16 a[136:139], v[208:211], v[80:83], a[136:139]\n"
"v_exp_f32 v44, v44\n"
"v_mfma_f32_16x16x32_bf16 a[136:139], v[212:215], v[112:115], a[136:139]\n"
"v_exp_f32 v45, v45\n"
"v_mfma_f32_16x16x32_bf16 a[140:143], v[212:215], v[120:123], a[140:143]\n"
"ds_read_b64_tr_b16 v[212:213], v235 offset:37120\n"
"v_add_f32 v244, v244, v12\n"
"v_mfma_f32_16x16x32_bf16 a[140:143], v[208:211], v[88:91], a[140:143]\n"
"s_waitcnt lgkmcnt(5)\n"
"ds_read_b64_tr_b16 v[214:215], v235 offset:43264\n"
"ds_read_b64_tr_b16 v[208:209], v235 offset:24832\n"
"v_mfma_f32_16x16x32_bf16 a[144:147], v[192:195], v[64:67], a[144:147]\n"
"ds_read_b64_tr_b16 v[210:211], v235 offset:30976\n"
"v_add_f32 v245, v245, v13\n"
"v_mfma_f32_16x16x32_bf16 a[144:147], v[196:199], v[96:99], a[144:147]\n"
"v_exp_f32 v46, v46\n"
"v_mfma_f32_16x16x32_bf16 a[148:151], v[196:199], v[104:107], a[148:151]\n"
"v_exp_f32 v47, v47\n"
"v_mfma_f32_16x16x32_bf16 a[148:151], v[192:195], v[72:75], a[148:151]\n"
"v_add_f32 v244, v244, v14\n"
"v_add_f32 v245, v245, v15\n"
"v_mfma_f32_16x16x32_bf16 a[152:155], v[192:195], v[80:83], a[152:155]\n"
"v_add_f32 v244, v244, v40\n"
"v_add_f32 v245, v245, v41\n"
"v_mfma_f32_16x16x32_bf16 a[152:155], v[196:199], v[112:115], a[152:155]\n"
"v_cvt_pk_bf16_f32 v40, v40, v41\n"
"v_add_f32 v244, v244, v42\n"
"v_mfma_f32_16x16x32_bf16 a[156:159], v[196:199], v[120:123], a[156:159]\n"
"s_waitcnt vmcnt(8) lgkmcnt(0)\n"
"s_barrier\n"
"ds_read_b128 v[196:199], v231 offset:24576\n"
"v_mfma_f32_16x16x32_bf16 a[156:159], v[192:195], v[88:91], a[156:159]\n"
"ds_read_b128 v[192:195], v230 offset:24576\n"
"v_add_f32 v245, v245, v43\n"
"v_mfma_f32_16x16x32_bf16 a[160:163], v[200:203], v[64:67], a[160:163]\n"
"v_cvt_pk_bf16_f32 v41, v42, v43\n"
"v_cvt_pk_bf16_f32 v42, v44, v45\n"
"v_mfma_f32_16x16x32_bf16 a[160:163], v[204:207], v[96:99], a[160:163]\n"
"v_cvt_pk_bf16_f32 v43, v46, v47\n"
"v_cvt_pk_bf16_f32 v8, v8, v9\n"
"v_mfma_f32_16x16x32_bf16 a[164:167], v[204:207], v[104:107], a[164:167]\n"
"v_cvt_pk_bf16_f32 v9, v10, v11\n"
"v_cvt_pk_bf16_f32 v10, v12, v13\n"
"v_mfma_f32_16x16x32_bf16 a[164:167], v[200:203], v[72:75], a[164:167]\n"
"s_add_u32 s70, s70, 0x14000\n"
"buffer_load_dword v255, v188, s[40:43], s70 offen offset:0\n"
"v_mfma_f32_16x16x32_bf16 a[168:171], v[200:203], v[80:83], a[168:171]\n"
"v_cvt_pk_bf16_f32 v11, v14, v15\n"
"v_add_f32 v242, v242, v36\n"
"v_mfma_f32_16x16x32_bf16 a[168:171], v[204:207], v[112:115], a[168:171]\n"
"v_exp_f32 v16, v16\n"
"v_mfma_f32_16x16x32_bf16 a[172:175], v[204:207], v[120:123], a[172:175]\n"
"ds_read_b128 v[204:207], v231 offset:24704\n"
"v_add_f32 v246, v246, v16\n"
"v_mfma_f32_16x16x32_bf16 a[172:175], v[200:203], v[88:91], a[172:175]\n"
"ds_read_b128 v[200:203], v230 offset:24704\n"
"v_add_f32 v243, v243, v37\n"
"v_mfma_f32_16x16x32_bf16 a[176:179], v[208:211], v[64:67], a[176:179]\n"
"v_exp_f32 v17, v17\n"
"v_mfma_f32_16x16x32_bf16 a[176:179], v[212:215], v[96:99], a[176:179]\n"
"v_exp_f32 v18, v18\n"
"v_mfma_f32_16x16x32_bf16 a[180:183], v[212:215], v[104:107], a[180:183]\n"
"v_exp_f32 v19, v19\n"
"v_mfma_f32_16x16x32_bf16 a[180:183], v[208:211], v[72:75], a[180:183]\n"
"v_exp_f32 v20, v20\n"
"v_mfma_f32_16x16x32_bf16 a[184:187], v[208:211], v[80:83], a[184:187]\n"
"v_exp_f32 v21, v21\n"
"v_mfma_f32_16x16x32_bf16 a[184:187], v[212:215], v[112:115], a[184:187]\n"
"v_exp_f32 v22, v22\n"
"v_mfma_f32_16x16x32_bf16 a[188:191], v[212:215], v[120:123], a[188:191]\n"
"ds_read_b128 v[212:215], v231 offset:24832\n"
"v_add_f32 v247, v247, v17\n"
"v_mfma_f32_16x16x32_bf16 a[188:191], v[208:211], v[88:91], a[188:191]\n"
"s_waitcnt lgkmcnt(3)\n"
"ds_read_b128 v[208:211], v230 offset:24832\n"
"v_cvt_pk_bf16_f32 v16, v16, v17\n"
"v_mfma_f32_16x16x32_bf16 v[64:67], v[192:195], a[192:195], v[132:135]\n"
"v_exp_f32 v23, v23\n"
"v_mfma_f32_16x16x32_bf16 v[64:67], v[196:199], a[208:211], v[64:67]\n"
"s_add_u32 m0, s73, 0x12000\n"
"v_add_f32 v246, v246, v18\n"
"v_add_f32 v247, v247, v19\n"
"v_mfma_f32_16x16x32_bf16 v[72:75], v[196:199], a[212:215], v[140:143]\n"
"buffer_load_dwordx4 v216, s[48:51], s65 offen offset:0 nt lds\n"
"v_mfma_f32_16x16x32_bf16 v[72:75], v[192:195], a[196:199], v[72:75]\n"
"v_cvt_pk_bf16_f32 v17, v18, v19\n"
"v_cvt_pk_bf16_f32 v18, v20, v21\n"
"v_mfma_f32_16x16x32_bf16 v[80:83], v[192:195], a[200:203], v[148:151]\n"
"s_mov_b64 s[52:53], s[30:31]\n"
"v_cvt_pk_bf16_f32 v19, v22, v23\n"
"v_add_f32 v246, v246, v20\n"
"v_mfma_f32_16x16x32_bf16 v[80:83], v[196:199], a[216:219], v[80:83]\n"
"s_mov_b32 s54, 0x60000\n"
"v_exp_f32 v48, v48\n"
"v_mfma_f32_16x16x32_bf16 v[88:91], v[196:199], a[220:223], v[156:159]\n"
"ds_read_b128 v[196:199], v231 offset:30720\n"
"v_add_f32 v247, v247, v21\n"
"v_mfma_f32_16x16x32_bf16 v[88:91], v[192:195], a[204:207], v[88:91]\n"
"s_waitcnt lgkmcnt(3)\n"
"ds_read_b128 v[192:195], v230 offset:30720\n"
"v_add_f32 v246, v246, v22\n"
"v_mfma_f32_16x16x32_bf16 v[64:67], v[200:203], a[224:227], v[64:67]\n"
"v_exp_f32 v49, v49\n"
"v_mfma_f32_16x16x32_bf16 v[64:67], v[204:207], a[240:243], v[64:67]\n"
"v_exp_f32 v50, v50\n"
"v_mfma_f32_16x16x32_bf16 v[72:75], v[204:207], a[244:247], v[72:75]\n"
"v_exp_f32 v51, v51\n"
"v_mfma_f32_16x16x32_bf16 v[72:75], v[200:203], a[228:231], v[72:75]\n"
"v_exp_f32 v52, v52\n"
"v_mfma_f32_16x16x32_bf16 v[80:83], v[200:203], a[232:235], v[80:83]\n"
"buffer_load_dwordx4 v217, s[48:51], s65 offen offset:1024 nt lds\n"
"v_mfma_f32_16x16x32_bf16 v[80:83], v[204:207], a[248:251], v[80:83]\n"
"v_exp_f32 v53, v53\n"
"v_mfma_f32_16x16x32_bf16 v[88:91], v[204:207], a[252:255], v[88:91]\n"
"ds_read_b128 v[204:207], v231 offset:30848\n"
"v_add_f32 v247, v247, v23\n"
"v_mfma_f32_16x16x32_bf16 v[88:91], v[200:203], a[236:239], v[88:91]\n"
"s_waitcnt lgkmcnt(3)\n"
"ds_read_b128 v[200:203], v230 offset:30848\n"
"v_add_f32 v246, v246, v48\n"
"v_mfma_f32_16x16x32_bf16 v[64:67], v[208:211], v[128:131], v[64:67]\n"
"v_exp_f32 v54, v54\n"
"v_mfma_f32_16x16x32_bf16 v[64:67], v[212:215], v[160:163], v[64:67]\n"
"v_exp_f32 v55, v55\n"
"v_mfma_f32_16x16x32_bf16 v[72:75], v[212:215], v[168:171], v[72:75]\n"
"v_add_f32 v247, v247, v49\n"
"v_cvt_pk_bf16_f32 v48, v48, v49\n"
"v_mfma_f32_16x16x32_bf16 v[72:75], v[208:211], v[136:139], v[72:75]\n"
"v_add_f32 v246, v246, v50\n"
"v_add_f32 v247, v247, v51\n"
"v_mfma_f32_16x16x32_bf16 v[80:83], v[208:211], v[144:147], v[80:83]\n"
"v_cvt_pk_bf16_f32 v49, v50, v51\n"
"v_cvt_pk_bf16_f32 v50, v52, v53\n"
"v_mfma_f32_16x16x32_bf16 v[80:83], v[212:215], v[176:179], v[80:83]\n"
"v_cvt_pk_bf16_f32 v51, v54, v55\n"
"v_add_f32 v242, v242, v38\n"
"v_mfma_f32_16x16x32_bf16 v[88:91], v[212:215], v[184:187], v[88:91]\n"
"ds_read_b128 v[212:215], v231 offset:30976\n"
"v_add_f32 v243, v243, v39\n"
"v_mfma_f32_16x16x32_bf16 v[88:91], v[208:211], v[152:155], v[88:91]\n"
"s_waitcnt lgkmcnt(3)\n"
"ds_read_b128 v[208:211], v230 offset:30976\n"
"v_add_f32 v244, v244, v44\n"
"v_mfma_f32_16x16x32_bf16 v[68:71], v[192:195], a[192:195], v[132:135]\n"
"v_exp_f32 v24, v24\n"
"v_mfma_f32_16x16x32_bf16 v[68:71], v[196:199], a[208:211], v[68:71]\n"
"v_exp_f32 v25, v25\n"
"v_mfma_f32_16x16x32_bf16 v[76:79], v[196:199], a[212:215], v[140:143]\n"
"buffer_load_dwordx4 v218, s[48:51], s65 offen offset:2048 nt lds\n"
"v_mfma_f32_16x16x32_bf16 v[76:79], v[192:195], a[196:199], v[76:79]\n"
"v_exp_f32 v26, v26\n"
"v_mfma_f32_16x16x32_bf16 v[84:87], v[192:195], a[200:203], v[148:151]\n"
"v_exp_f32 v27, v27\n"
"v_mfma_f32_16x16x32_bf16 v[84:87], v[196:199], a[216:219], v[84:87]\n"
"v_exp_f32 v28, v28\n"
"v_mfma_f32_16x16x32_bf16 v[92:95], v[196:199], a[220:223], v[156:159]\n"
"ds_read_b128 v[196:199], v231 offset:36864\n"
"v_add_f32 v248, v248, v24\n"
"v_mfma_f32_16x16x32_bf16 v[92:95], v[192:195], a[204:207], v[92:95]\n"
"s_waitcnt lgkmcnt(3)\n"
"ds_read_b128 v[192:195], v230 offset:36864\n"
"v_add_f32 v249, v249, v25\n"
"v_mfma_f32_16x16x32_bf16 v[68:71], v[200:203], a[224:227], v[68:71]\n"
"v_exp_f32 v29, v29\n"
"v_mfma_f32_16x16x32_bf16 v[68:71], v[204:207], a[240:243], v[68:71]\n"
"v_exp_f32 v30, v30\n"
"v_mfma_f32_16x16x32_bf16 v[76:79], v[204:207], a[244:247], v[76:79]\n"
"v_exp_f32 v31, v31\n"
"v_mfma_f32_16x16x32_bf16 v[76:79], v[200:203], a[228:231], v[76:79]\n"
"v_exp_f32 v56, v56\n"
"v_mfma_f32_16x16x32_bf16 v[84:87], v[200:203], a[232:235], v[84:87]\n"
"buffer_load_dwordx4 v219, s[48:51], s65 offen offset:3072 nt lds\n"
"v_mfma_f32_16x16x32_bf16 v[84:87], v[204:207], a[248:251], v[84:87]\n"
"v_exp_f32 v57, v57\n"
"v_mfma_f32_16x16x32_bf16 v[92:95], v[204:207], a[252:255], v[92:95]\n"
"ds_read_b128 v[204:207], v231 offset:36992\n"
"v_add_f32 v248, v248, v26\n"
"v_mfma_f32_16x16x32_bf16 v[92:95], v[200:203], a[236:239], v[92:95]\n"
"s_waitcnt lgkmcnt(3)\n"
"ds_read_b128 v[200:203], v230 offset:36992\n"
"v_add_f32 v249, v249, v27\n"
"v_mfma_f32_16x16x32_bf16 v[68:71], v[208:211], v[128:131], v[68:71]\n"
"v_exp_f32 v58, v58\n"
"v_mfma_f32_16x16x32_bf16 v[68:71], v[212:215], v[160:163], v[68:71]\n"
"v_exp_f32 v59, v59\n"
"v_mfma_f32_16x16x32_bf16 v[76:79], v[212:215], v[168:171], v[76:79]\n"
"v_exp_f32 v60, v60\n"
"v_mfma_f32_16x16x32_bf16 v[76:79], v[208:211], v[136:139], v[76:79]\n"
"v_exp_f32 v61, v61\n"
"v_mfma_f32_16x16x32_bf16 v[84:87], v[208:211], v[144:147], v[84:87]\n"
"v_exp_f32 v62, v62\n"
"v_mfma_f32_16x16x32_bf16 v[84:87], v[212:215], v[176:179], v[84:87]\n"
"v_exp_f32 v63, v63\n"
"v_mfma_f32_16x16x32_bf16 v[92:95], v[212:215], v[184:187], v[92:95]\n"
"ds_read_b128 v[212:215], v231 offset:37120\n"
"v_add_f32 v248, v248, v28\n"
"v_mfma_f32_16x16x32_bf16 v[92:95], v[208:211], v[152:155], v[92:95]\n"
"s_waitcnt lgkmcnt(3)\n"
"ds_read_b128 v[208:211], v230 offset:37120\n"
"v_add_f32 v249, v249, v29\n"
"v_mfma_f32_16x16x32_bf16 v[96:99], v[192:195], a[192:195], v[132:135]\n"
"v_add_f32 v248, v248, v30\n"
"v_add_f32 v249, v249, v31\n"
"v_mfma_f32_16x16x32_bf16 v[96:99], v[196:199], a[208:211], v[96:99]\n"
"s_add_u32 m0, s73, 0x13000\n"
"v_add_f32 v248, v248, v56\n"
"v_add_f32 v249, v249, v57\n"
"v_mfma_f32_16x16x32_bf16 v[104:107], v[196:199], a[212:215], v[140:143]\n"
"buffer_load_dwordx4 v220, s[48:51], s65 offen offset:0 nt lds\n"
"v_mfma_f32_16x16x32_bf16 v[104:107], v[192:195], a[196:199], v[104:107]\n"
"v_cvt_pk_bf16_f32 v56, v56, v57\n"
"v_add_f32 v248, v248, v58\n"
"v_mfma_f32_16x16x32_bf16 v[112:115], v[192:195], a[200:203], v[148:151]\n"
"v_add_f32 v249, v249, v59\n"
"v_cvt_pk_bf16_f32 v57, v58, v59\n"
"v_mfma_f32_16x16x32_bf16 v[112:115], v[196:199], a[216:219], v[112:115]\n"
"v_cvt_pk_bf16_f32 v58, v60, v61\n"
"v_cvt_pk_bf16_f32 v59, v62, v63\n"
"v_mfma_f32_16x16x32_bf16 v[120:123], v[196:199], a[220:223], v[156:159]\n"
"ds_read_b128 v[196:199], v231 offset:43008\n"
"v_cvt_pk_bf16_f32 v24, v24, v25\n"
"v_mfma_f32_16x16x32_bf16 v[120:123], v[192:195], a[204:207], v[120:123]\n"
"s_waitcnt lgkmcnt(3)\n"
"ds_read_b128 v[192:195], v230 offset:43008\n"
"v_cvt_pk_bf16_f32 v25, v26, v27\n"
"v_mfma_f32_16x16x32_bf16 v[96:99], v[200:203], a[224:227], v[96:99]\n"
"v_cvt_pk_bf16_f32 v26, v28, v29\n"
"v_cvt_pk_bf16_f32 v27, v30, v31\n"
"v_mfma_f32_16x16x32_bf16 v[96:99], v[204:207], a[240:243], v[96:99]\n"
"v_exp_f32 v64, v64\n"
"v_mfma_f32_16x16x32_bf16 v[104:107], v[204:207], a[244:247], v[104:107]\n"
"v_exp_f32 v65, v65\n"
"v_mfma_f32_16x16x32_bf16 v[104:107], v[200:203], a[228:231], v[104:107]\n"
"v_exp_f32 v66, v66\n"
"v_mfma_f32_16x16x32_bf16 v[112:115], v[200:203], a[232:235], v[112:115]\n"
"buffer_load_dwordx4 v221, s[48:51], s65 offen offset:1024 nt lds\n"
"v_mfma_f32_16x16x32_bf16 v[112:115], v[204:207], a[248:251], v[112:115]\n"
"v_exp_f32 v67, v67\n"
"v_mfma_f32_16x16x32_bf16 v[120:123], v[204:207], a[252:255], v[120:123]\n"
"ds_read_b128 v[204:207], v231 offset:43136\n"
"v_add_f32 v242, v242, v64\n"
"v_mfma_f32_16x16x32_bf16 v[120:123], v[200:203], a[236:239], v[120:123]\n"
"s_waitcnt lgkmcnt(3)\n"
"ds_read_b128 v[200:203], v230 offset:43136\n"
"v_add_f32 v243, v243, v65\n"
"v_mfma_f32_16x16x32_bf16 v[96:99], v[208:211], v[128:131], v[96:99]\n"
"s_add_u32 s65, s65, 0x6000\n"
"v_exp_f32 v68, v68\n"
"v_mfma_f32_16x16x32_bf16 v[96:99], v[212:215], v[160:163], v[96:99]\n"
"v_exp_f32 v69, v69\n"
"v_mfma_f32_16x16x32_bf16 v[104:107], v[212:215], v[168:171], v[104:107]\n"
"v_exp_f32 v70, v70\n"
"v_mfma_f32_16x16x32_bf16 v[104:107], v[208:211], v[136:139], v[104:107]\n"
"v_exp_f32 v71, v71\n"
"v_mfma_f32_16x16x32_bf16 v[112:115], v[208:211], v[144:147], v[112:115]\n"
"buffer_load_dword v255, v240, s[96:99], s60 offen offset:0 sc1\n"
"v_mfma_f32_16x16x32_bf16 v[112:115], v[212:215], v[176:179], v[112:115]\n"
"s_memrealtime s[82:83]\n"
"v_cvt_pk_bf16_f32 v64, v64, v65\n"
"v_add_f32 v242, v242, v66\n"
"v_mfma_f32_16x16x32_bf16 v[120:123], v[212:215], v[184:187], v[120:123]\n"
"ds_read_b128 v[212:215], v231 offset:43264\n"
"v_add_f32 v243, v243, v67\n"
"v_mfma_f32_16x16x32_bf16 v[120:123], v[208:211], v[152:155], v[120:123]\n"
"s_waitcnt lgkmcnt(3)\n"
"ds_read_b128 v[208:211], v230 offset:43264\n"
"v_cvt_pk_bf16_f32 v65, v66, v67\n"
"v_mfma_f32_16x16x32_bf16 v[100:103], v[192:195], a[192:195], v[132:135]\n"
"v_cvt_pk_bf16_f32 v66, v68, v69\n"
"v_cvt_pk_bf16_f32 v67, v70, v71\n"
"v_mfma_f32_16x16x32_bf16 v[100:103], v[196:199], a[208:211], v[100:103]\n"
"v_exp_f32 v96, v96\n"
"v_mfma_f32_16x16x32_bf16 v[108:111], v[196:199], a[212:215], v[140:143]\n"
"buffer_load_dword v255, v241, s[96:99], s61 offen offset:0 sc1\n"
"v_mfma_f32_16x16x32_bf16 v[108:111], v[192:195], a[196:199], v[108:111]\n"
"v_exp_f32 v97, v97\n"
"v_mfma_f32_16x16x32_bf16 v[116:119], v[192:195], a[200:203], v[148:151]\n"
"buffer_load_dword v255, v241, s[96:99], s61 offen offset:32 sc1\n"
"v_mfma_f32_16x16x32_bf16 v[116:119], v[196:199], a[216:219], v[116:119]\n"
"v_exp_f32 v98, v98\n"
"v_mfma_f32_16x16x32_bf16 v[124:127], v[196:199], a[220:223], v[156:159]\n"
"s_waitcnt vmcnt(12) lgkmcnt(0)\n"
"s_sub_u32 s83, s94, s82\n"
"s_cmp_gt_i32 s83, 0\n"
"s_cbranch_scc0 Lrpc6%=\n"
"s_min_i32 s83, s83, 0xc8\n"
"Lqpc6%=:\n"
"s_sleep 1\n"
"s_sub_u32 s83, s83, 4\n"
"s_cmp_gt_i32 s83, 0\n"
"s_cbranch_scc1 Lqpc6%=\n"
"Lrpc6%=:\n"
"s_barrier\n"
"ds_read_b64_tr_b16 v[196:197], v232 offset:12288\n"
"v_mfma_f32_16x16x32_bf16 v[124:127], v[192:195], a[204:207], v[124:127]\n"
"s_sub_u32 s6, 120, s71\n"
"s_max_i32 s6, s6, 4\n"
"ds_read_b64_tr_b16 v[198:199], v232 offset:18432\n"
"ds_read_b64_tr_b16 v[192:193], v232 offset:0\n"
"v_mfma_f32_16x16x32_bf16 v[100:103], v[200:203], a[224:227], v[100:103]\n"
"s_sub_u32 s82, s82, 16\n"
"ds_read_b64_tr_b16 v[194:195], v232 offset:6144\n"
"v_add_f32 v242, v242, v68\n"
"v_mfma_f32_16x16x32_bf16 v[100:103], v[204:207], a[240:243], v[100:103]\n"
"s_sub_u32 s7, s71, s29\n"
"s_add_u32 s7, s7, 10\n"
"Ltp9%=:\n"
"s_cbranch_execz Ltrl0x3%=\n"
"v_exp_f32 v99, v99\n"
"v_mfma_f32_16x16x32_bf16 v[108:111], v[204:207], a[244:247], v[108:111]\n"
"s_sub_u32 s28, s82, s94\n"
"s_cmp_gt_i32 s28, s6\n"
"s_cselect_b32 s28, s7, -5\n"
"v_add_f32 v243, v243, v69\n"
"v_add_f32 v242, v242, v70\n"
"v_mfma_f32_16x16x32_bf16 v[108:111], v[200:203], a[228:231], v[108:111]\n"
"s_add_u32 s71, s71, s28\n"
"s_min_i32 s71, s71, 152\n"
"s_max_i32 s71, s71, s29\n"
"v_add_f32 v243, v243, v71\n"
"v_add_f32 v242, v242, v96\n"
"v_mfma_f32_16x16x32_bf16 v[116:119], v[200:203], a[232:235], v[116:119]\n"
"s_max_i32 s94, s94, s82\n"
"v_add_f32 v243, v243, v97\n"
"v_cvt_pk_bf16_f32 v96, v96, v97\n"
"v_mfma_f32_16x16x32_bf16 v[116:119], v[204:207], a[248:251], v[116:119]\n"
"s_add_u32 s94, s94, s71\n"
"v_add_f32 v242, v242, v98\n"
"v_add_f32 v243, v243, v99\n"
"v_mfma_f32_16x16x32_bf16 v[124:127], v[204:207], a[252:255], v[124:127]\n"
"ds_read_b64_tr_b16 v[204:205], v233 offset:12288\n"
"v_cvt_pk_bf16_f32 v97, v98, v99\n"
"v_mfma_f32_16x16x32_bf16 v[124:127], v[200:203], a[236:239], v[124:127]\n"
"s_sub_u32 s28, s71, 36\n"
"s_max_i32 s29, s29, s28\n"
"ds_read_b64_tr_b16 v[206:207], v233 offset:18432\n"
"ds_read_b64_tr_b16 v[200:201], v233 offset:0\n"
"v_mfma_f32_16x16x32_bf16 v[100:103], v[208:211], v[128:131], v[100:103]\n"
"ds_read_b64_tr_b16 v[202:203], v233 offset:6144\n"
"v_add_f32 v245, v245, v45\n"
"v_mfma_f32_16x16x32_bf16 v[100:103], v[212:215], v[160:163], v[100:103]\n"
"v_exp_f32 v72, v72\n"
"v_mfma_f32_16x16x32_bf16 v[108:111], v[212:215], v[168:171], v[108:111]\n"
"v_exp_f32 v73, v73\n"
"v_mfma_f32_16x16x32_bf16 v[108:111], v[208:211], v[136:139], v[108:111]\n"
"v_exp_f32 v74, v74\n"
"v_mfma_f32_16x16x32_bf16 v[116:119], v[208:211], v[144:147], v[116:119]\n"
"v_exp_f32 v100, v100\n"
"v_mfma_f32_16x16x32_bf16 v[116:119], v[212:215], v[176:179], v[116:119]\n"
"v_exp_f32 v101, v101\n"
"v_mfma_f32_16x16x32_bf16 v[124:127], v[212:215], v[184:187], v[124:127]\n"
"ds_read_b64_tr_b16 v[212:213], v234 offset:12288\n"
"v_cvt_pk_bf16_f32 v98, v100, v101\n"
"v_mfma_f32_16x16x32_bf16 v[124:127], v[208:211], v[152:155], v[124:127]\n"
"s_waitcnt lgkmcnt(5)\n"
"ds_read_b64_tr_b16 v[214:215], v234 offset:18432\n"
"ds_read_b64_tr_b16 v[208:209], v234 offset:0\n"
"v_mfma_f32_16x16x32_bf16 a[0:3], v[192:195], v[0:3], a[0:3]\n"
"ds_read_b64_tr_b16 v[210:211], v234 offset:6144\n"
"v_add_f32 v244, v244, v46\n"
"v_mfma_f32_16x16x32_bf16 a[0:3], v[196:199], v[32:35], a[0:3]\n"
"v_exp_f32 v102, v102\n"
"v_mfma_f32_16x16x32_bf16 a[4:7], v[196:199], v[40:43], a[4:7]\n"
"v_exp_f32 v103, v103\n"
"v_mfma_f32_16x16x32_bf16 a[4:7], v[192:195], v[8:11], a[4:7]\n"
"s_mul_i32 s58, s95, 0x3c000\n"
"s_add_u32 m0, s87, 0\n"
"v_cvt_pk_bf16_f32 v99, v102, v103\n"
"v_mfma_f32_16x16x32_bf16 a[8:11], v[192:195], v[16:19], a[8:11]\n"
"buffer_load_dwordx4 v222, s[40:43], s58 offen offset:0 lds\n"
"v_mfma_f32_16x16x32_bf16 a[8:11], v[196:199], v[48:51], a[8:11]\n"
"v_exp_f32 v75, v75\n"
"v_mfma_f32_16x16x32_bf16 a[12:15], v[196:199], v[56:59], a[12:15]\n"
"ds_read_b64_tr_b16 v[196:197], v235 offset:12288\n"
"v_add_f32 v245, v245, v47\n"
"v_mfma_f32_16x16x32_bf16 a[12:15], v[192:195], v[24:27], a[12:15]\n"
"s_waitcnt lgkmcnt(5)\n"
"ds_read_b64_tr_b16 v[198:199], v235 offset:18432\n"
"ds_read_b64_tr_b16 v[192:193], v235 offset:0\n"
"v_mfma_f32_16x16x32_bf16 a[16:19], v[200:203], v[0:3], a[16:19]\n"
"ds_read_b64_tr_b16 v[194:195], v235 offset:6144\n"
"v_add_f32 v244, v244, v72\n"
"v_mfma_f32_16x16x32_bf16 a[16:19], v[204:207], v[32:35], a[16:19]\n"
"v_exp_f32 v76, v76\n"
"v_mfma_f32_16x16x32_bf16 a[20:23], v[204:207], v[40:43], a[20:23]\n"
"v_exp_f32 v77, v77\n"
"v_mfma_f32_16x16x32_bf16 a[20:23], v[200:203], v[8:11], a[20:23]\n"
"v_exp_f32 v78, v78\n"
"v_mfma_f32_16x16x32_bf16 a[24:27], v[200:203], v[16:19], a[24:27]\n"
"buffer_load_dwordx4 v223, s[40:43], s58 offen offset:1024 lds\n"
"v_mfma_f32_16x16x32_bf16 a[24:27], v[204:207], v[48:51], a[24:27]\n"
"v_exp_f32 v79, v79\n"
"v_mfma_f32_16x16x32_bf16 a[28:31], v[204:207], v[56:59], a[28:31]\n"
"ds_read_b64_tr_b16 v[204:205], v232 offset:12416\n"
"v_add_f32 v245, v245, v73\n"
"v_mfma_f32_16x16x32_bf16 a[28:31], v[200:203], v[24:27], a[28:31]\n"
"s_waitcnt lgkmcnt(5)\n"
"ds_read_b64_tr_b16 v[206:207], v232 offset:18560\n"
"ds_read_b64_tr_b16 v[200:201], v232 offset:128\n"
"v_mfma_f32_16x16x32_bf16 a[32:35], v[208:211], v[0:3], a[32:35]\n"
"ds_read_b64_tr_b16 v[202:203], v232 offset:6272\n"
"v_add_f32 v244, v244, v74\n"
"v_mfma_f32_16x16x32_bf16 a[32:35], v[212:215], v[32:35], a[32:35]\n"
"v_exp_f32 v104, v104\n"
"v_mfma_f32_16x16x32_bf16 a[36:39], v[212:215], v[40:43], a[36:39]\n"
"v_exp_f32 v105, v105\n"
"v_mfma_f32_16x16x32_bf16 a[36:39], v[208:211], v[8:11], a[36:39]\n"
"v_exp_f32 v106, v106\n"
"v_mfma_f32_16x16x32_bf16 a[40:43], v[208:211], v[16:19], a[40:43]\n"
"buffer_load_dwordx4 v224, s[40:43], s58 offen offset:2048 lds\n"
"v_mfma_f32_16x16x32_bf16 a[40:43], v[212:215], v[48:51], a[40:43]\n"
"v_exp_f32 v107, v107\n"
"v_mfma_f32_16x16x32_bf16 a[44:47], v[212:215], v[56:59], a[44:47]\n"
"ds_read_b64_tr_b16 v[212:213], v233 offset:12416\n"
"v_add_f32 v245, v245, v75\n"
"v_mfma_f32_16x16x32_bf16 a[44:47], v[208:211], v[24:27], a[44:47]\n"
"s_waitcnt lgkmcnt(5)\n"
"ds_read_b64_tr_b16 v[214:215], v233 offset:18560\n"
"ds_read_b64_tr_b16 v[208:209], v233 offset:128\n"
"v_mfma_f32_16x16x32_bf16 a[48:51], v[192:195], v[0:3], a[48:51]\n"
"ds_read_b64_tr_b16 v[210:211], v233 offset:6272\n"
"v_add_f32 v244, v244, v76\n"
"v_mfma_f32_16x16x32_bf16 a[48:51], v[196:199], v[32:35], a[48:51]\n"
"v_exp_f32 v108, v108\n"
"v_mfma_f32_16x16x32_bf16 a[52:55], v[196:199], v[40:43], a[52:55]\n"
"v_exp_f32 v109, v109\n"
"v_mfma_f32_16x16x32_bf16 a[52:55], v[192:195], v[8:11], a[52:55]\n"
"v_exp_f32 v110, v110\n"
"v_mfma_f32_16x16x32_bf16 a[56:59], v[192:195], v[16:19], a[56:59]\n"
"buffer_load_dwordx4 v225, s[40:43], s58 offen offset:3072 lds\n"
"v_mfma_f32_16x16x32_bf16 a[56:59], v[196:199], v[48:51], a[56:59]\n"
"v_exp_f32 v111, v111\n"
"v_mfma_f32_16x16x32_bf16 a[60:63], v[196:199], v[56:59], a[60:63]\n"
"ds_read_b64_tr_b16 v[196:197], v234 offset:12416\n"
"v_add_f32 v245, v245, v77\n"
"v_mfma_f32_16x16x32_bf16 a[60:63], v[192:195], v[24:27], a[60:63]\n"
"s_waitcnt lgkmcnt(5)\n"
"ds_read_b64_tr_b16 v[198:199], v234 offset:18560\n"
"ds_read_b64_tr_b16 v[192:193], v234 offset:128\n"
"v_mfma_f32_16x16x32_bf16 a[64:67], v[200:203], v[0:3], a[64:67]\n"
"ds_read_b64_tr_b16 v[194:195], v234 offset:6272\n"
"v_add_f32 v244, v244, v78\n"
"v_mfma_f32_16x16x32_bf16 a[64:67], v[204:207], v[32:35], a[64:67]\n"
"v_add_f32 v245, v245, v79\n"
"v_add_f32 v244, v244, v104\n"
"v_mfma_f32_16x16x32_bf16 a[68:71], v[204:207], v[40:43], a[68:71]\n"
"v_add_f32 v245, v245, v105\n"
"v_cvt_pk_bf16_f32 v104, v104, v105\n"
"v_mfma_f32_16x16x32_bf16 a[68:71], v[200:203], v[8:11], a[68:71]\n"
"s_add_u32 m0, s87, 0x1000\n"
"v_add_f32 v244, v244, v106\n"
"v_add_f32 v245, v245, v107\n"
"v_mfma_f32_16x16x32_bf16 a[72:75], v[200:203], v[16:19], a[72:75]\n"
"buffer_load_dwordx4 v226, s[40:43], s58 offen offset:0 lds\n"
"v_mfma_f32_16x16x32_bf16 a[72:75], v[204:207], v[48:51], a[72:75]\n"
"v_cvt_pk_bf16_f32 v105, v106, v107\n"
"v_cvt_pk_bf16_f32 v106, v108, v109\n"
"v_mfma_f32_16x16x32_bf16 a[76:79], v[204:207], v[56:59], a[76:79]\n"
"ds_read_b64_tr_b16 v[204:205], v235 offset:12416\n"
"v_cvt_pk_bf16_f32 v107, v110, v111\n"
"v_mfma_f32_16x16x32_bf16 a[76:79], v[200:203], v[24:27], a[76:79]\n"
"s_waitcnt lgkmcnt(5)\n"
"ds_read_b64_tr_b16 v[206:207], v235 offset:18560\n"
"ds_read_b64_tr_b16 v[200:201], v235 offset:128\n"
"v_mfma_f32_16x16x32_bf16 a[80:83], v[208:211], v[0:3], a[80:83]\n"
"ds_read_b64_tr_b16 v[202:203], v235 offset:6272\n"
"v_cvt_pk_bf16_f32 v72, v72, v73\n"
"v_mfma_f32_16x16x32_bf16 a[80:83], v[212:215], v[32:35], a[80:83]\n"
"v_cvt_pk_bf16_f32 v73, v74, v75\n"
"v_cvt_pk_bf16_f32 v74, v76, v77\n"
"v_mfma_f32_16x16x32_bf16 a[84:87], v[212:215], v[40:43], a[84:87]\n"
"v_cvt_pk_bf16_f32 v75, v78, v79\n"
"v_add_f32 v246, v246, v52\n"
"v_mfma_f32_16x16x32_bf16 a[84:87], v[208:211], v[8:11], a[84:87]\n"
"s_mul_i32 s58, s67, 0x3c000\n"
"s_add_u32 m0, s87, 0x5000\n"
"v_add_f32 v247, v247, v53\n"
"v_mfma_f32_16x16x32_bf16 a[88:91], v[208:211], v[16:19], a[88:91]\n"
"buffer_load_dwordx4 v222, s[40:43], s58 offen offset:0 lds\n"
"v_mfma_f32_16x16x32_bf16 a[88:91], v[212:215], v[48:51], a[88:91]\n"
"v_exp_f32 v80, v80\n"
"v_mfma_f32_16x16x32_bf16 a[92:95], v[212:215], v[56:59], a[92:95]\n"
"ds_read_b64_tr_b16 v[212:213], v232 offset:12544\n"
"v_add_f32 v246, v246, v54\n"
"v_mfma_f32_16x16x32_bf16 a[92:95], v[208:211], v[24:27], a[92:95]\n"
"s_waitcnt lgkmcnt(5)\n"
"ds_read_b64_tr_b16 v[214:215], v232 offset:18688\n"
"ds_read_b64_tr_b16 v[208:209], v232 offset:256\n"
"v_mfma_f32_16x16x32_bf16 a[96:99], v[192:195], v[0:3], a[96:99]\n"
"ds_read_b64_tr_b16 v[210:211], v232 offset:6400\n"
"v_add_f32 v247, v247, v55\n"
"v_mfma_f32_16x16x32_bf16 a[96:99], v[196:199], v[32:35], a[96:99]\n"
"v_exp_f32 v81, v81\n"
"v_mfma_f32_16x16x32_bf16 a[100:103], v[196:199], v[40:43], a[100:103]\n"
"v_exp_f32 v82, v82\n"
"v_mfma_f32_16x16x32_bf16 a[100:103], v[192:195], v[8:11], a[100:103]\n"
"v_exp_f32 v83, v83\n"
"v_mfma_f32_16x16x32_bf16 a[104:107], v[192:195], v[16:19], a[104:107]\n"
"buffer_load_dwordx4 v223, s[40:43], s58 offen offset:1024 lds\n"
"v_mfma_f32_16x16x32_bf16 a[104:107], v[196:199], v[48:51], a[104:107]\n"
"v_exp_f32 v84, v84\n"
"v_mfma_f32_16x16x32_bf16 a[108:111], v[196:199], v[56:59], a[108:111]\n"
"ds_read_b64_tr_b16 v[196:197], v233 offset:12544\n"
"v_add_f32 v246, v246, v80\n"
"v_mfma_f32_16x16x32_bf16 a[108:111], v[192:195], v[24:27], a[108:111]\n"
"s_waitcnt lgkmcnt(5)\n"
"ds_read_b64_tr_b16 v[198:199], v233 offset:18688\n"
"ds_read_b64_tr_b16 v[192:193], v233 offset:256\n"
"v_mfma_f32_16x16x32_bf16 a[112:115], v[200:203], v[0:3], a[112:115]\n"
"ds_read_b64_tr_b16 v[194:195], v233 offset:6400\n"
"v_add_f32 v247, v247, v81\n"
"v_mfma_f32_16x16x32_bf16 a[112:115], v[204:207], v[32:35], a[112:115]\n"
"v_exp_f32 v85, v85\n"
"v_mfma_f32_16x16x32_bf16 a[116:119], v[204:207], v[40:43], a[116:119]\n"
"v_exp_f32 v86, v86\n"
"v_mfma_f32_16x16x32_bf16 a[116:119], v[200:203], v[8:11], a[116:119]\n"
"v_exp_f32 v87, v87\n"
"v_mfma_f32_16x16x32_bf16 a[120:123], v[200:203], v[16:19], a[120:123]\n"
"buffer_load_dwordx4 v224, s[40:43], s58 offen offset:2048 lds\n"
"v_mfma_f32_16x16x32_bf16 a[120:123], v[204:207], v[48:51], a[120:123]\n"
"v_cvt_pk_bf16_f32 v80, v80, v81\n"
"v_add_f32 v246, v246, v82\n"
"v_mfma_f32_16x16x32_bf16 a[124:127], v[204:207], v[56:59], a[124:127]\n"
"ds_read_b64_tr_b16 v[204:205], v234 offset:12544\n"
"v_add_f32 v247, v247, v83\n"
"v_mfma_f32_16x16x32_bf16 a[124:127], v[200:203], v[24:27], a[124:127]\n"
"s_waitcnt lgkmcnt(5)\n"
"ds_read_b64_tr_b16 v[206:207], v234 offset:18688\n"
"ds_read_b64_tr_b16 v[200:201], v234 offset:256\n"
"v_mfma_f32_16x16x32_bf16 a[128:131], v[208:211], v[0:3], a[128:131]\n"
"ds_read_b64_tr_b16 v[202:203], v234 offset:6400\n"
"v_cvt_pk_bf16_f32 v81, v82, v83\n"
"v_mfma_f32_16x16x32_bf16 a[128:131], v[212:215], v[32:35], a[128:131]\n"
"v_cvt_pk_bf16_f32 v82, v84, v85\n"
"v_cvt_pk_bf16_f32 v83, v86, v87\n"
"v_mfma_f32_16x16x32_bf16 a[132:135], v[212:215], v[40:43], a[132:135]\n"
"v_exp_f32 v112, v112\n"
"v_mfma_f32_16x16x32_bf16 a[132:135], v[208:211], v[8:11], a[132:135]\n"
"v_exp_f32 v113, v113\n"
"v_mfma_f32_16x16x32_bf16 a[136:139], v[208:211], v[16:19], a[136:139]\n"
"buffer_load_dwordx4 v225, s[40:43], s58 offen offset:3072 lds\n"
"v_mfma_f32_16x16x32_bf16 a[136:139], v[212:215], v[48:51], a[136:139]\n"
"v_exp_f32 v114, v114\n"
"v_mfma_f32_16x16x32_bf16 a[140:143], v[212:215], v[56:59], a[140:143]\n"
"ds_read_b64_tr_b16 v[212:213], v235 offset:12544\n"
"v_add_f32 v246, v246, v84\n"
"v_mfma_f32_16x16x32_bf16 a[140:143], v[208:211], v[24:27], a[140:143]\n"
"s_waitcnt lgkmcnt(5)\n"
"ds_read_b64_tr_b16 v[214:215], v235 offset:18688\n"
"ds_read_b64_tr_b16 v[208:209], v235 offset:256\n"
"v_mfma_f32_16x16x32_bf16 a[144:147], v[192:195], v[0:3], a[144:147]\n"
"ds_read_b64_tr_b16 v[210:211], v235 offset:6400\n"
"v_add_f32 v247, v247, v85\n"
"v_mfma_f32_16x16x32_bf16 a[144:147], v[196:199], v[32:35], a[144:147]\n"
"s_add_u32 m0, s87, 0x6000\n"
"v_exp_f32 v115, v115\n"
"v_mfma_f32_16x16x32_bf16 a[148:151], v[196:199], v[40:43], a[148:151]\n"
"buffer_load_dwordx4 v226, s[40:43], s58 offen offset:0 lds\n"
"v_mfma_f32_16x16x32_bf16 a[148:151], v[192:195], v[8:11], a[148:151]\n"
"v_exp_f32 v116, v116\n"
"v_mfma_f32_16x16x32_bf16 a[152:155], v[192:195], v[16:19], a[152:155]\n"
"v_exp_f32 v117, v117\n"
"v_mfma_f32_16x16x32_bf16 a[152:155], v[196:199], v[48:51], a[152:155]\n"
"v_exp_f32 v118, v118\n"
"v_mfma_f32_16x16x32_bf16 a[156:159], v[196:199], v[56:59], a[156:159]\n"
"s_waitcnt vmcnt(13) lgkmcnt(0)\n"
"s_barrier\n"
"ds_read_b64_tr_b16 v[196:197], v232 offset:36864\n"
"v_mfma_f32_16x16x32_bf16 a[156:159], v[192:195], v[24:27], a[156:159]\n"
"ds_read_b64_tr_b16 v[198:199], v232 offset:43008\n"
"ds_read_b64_tr_b16 v[192:193], v232 offset:24576\n"
"v_mfma_f32_16x16x32_bf16 a[160:163], v[200:203], v[0:3], a[160:163]\n"
"ds_read_b64_tr_b16 v[194:195], v232 offset:30720\n"
"v_add_f32 v246, v246, v86\n"
"v_mfma_f32_16x16x32_bf16 a[160:163], v[204:207], v[32:35], a[160:163]\n"
"s_mul_i32 s58, s69, 0x3c000\n"
"s_add_u32 m0, s87, 0xa000\n"
"v_add_f32 v247, v247, v87\n"
"v_mfma_f32_16x16x32_bf16 a[164:167], v[204:207], v[40:43], a[164:167]\n"
"buffer_load_dwordx4 v222, s[40:43], s58 offen offset:0 lds\n"
"v_mfma_f32_16x16x32_bf16 a[164:167], v[200:203], v[8:11], a[164:167]\n"
"v_exp_f32 v119, v119\n"
"v_mfma_f32_16x16x32_bf16 a[168:171], v[200:203], v[16:19], a[168:171]\n"
"v_add_f32 v246, v246, v112\n"
"v_add_f32 v247, v247, v113\n"
"v_mfma_f32_16x16x32_bf16 a[168:171], v[204:207], v[48:51], a[168:171]\n"
"v_cvt_pk_bf16_f32 v112, v112, v113\n"
"v_add_f32 v246, v246, v114\n"
"v_mfma_f32_16x16x32_bf16 a[172:175], v[204:207], v[56:59], a[172:175]\n"
"ds_read_b64_tr_b16 v[204:205], v233 offset:36864\n"
"v_add_f32 v247, v247, v115\n"
"v_mfma_f32_16x16x32_bf16 a[172:175], v[200:203], v[24:27], a[172:175]\n"
"ds_read_b64_tr_b16 v[206:207], v233 offset:43008\n"
"ds_read_b64_tr_b16 v[200:201], v233 offset:24576\n"
"v_mfma_f32_16x16x32_bf16 a[176:179], v[208:211], v[0:3], a[176:179]\n"
"ds_read_b64_tr_b16 v[202:203], v233 offset:30720\n"
"v_cvt_pk_bf16_f32 v113, v114, v115\n"
"v_mfma_f32_16x16x32_bf16 a[176:179], v[212:215], v[32:35], a[176:179]\n"
"v_cvt_pk_bf16_f32 v114, v116, v117\n"
"v_cvt_pk_bf16_f32 v115, v118, v119\n"
"v_mfma_f32_16x16x32_bf16 a[180:183], v[212:215], v[40:43], a[180:183]\n"
"buffer_load_dwordx4 v223, s[40:43], s58 offen offset:1024 lds\n"
"v_mfma_f32_16x16x32_bf16 a[180:183], v[208:211], v[8:11], a[180:183]\n"
"v_exp_f32 v88, v88\n"
"v_mfma_f32_16x16x32_bf16 a[184:187], v[208:211], v[16:19], a[184:187]\n"
"v_exp_f32 v89, v89\n"
"v_mfma_f32_16x16x32_bf16 a[184:187], v[212:215], v[48:51], a[184:187]\n"
"v_exp_f32 v90, v90\n"
"v_mfma_f32_16x16x32_bf16 a[188:191], v[212:215], v[56:59], a[188:191]\n"
"ds_read_b64_tr_b16 v[212:213], v234 offset:36864\n"
"v_add_f32 v248, v248, v60\n"
"v_mfma_f32_16x16x32_bf16 a[188:191], v[208:211], v[24:27], a[188:191]\n"
"s_waitcnt lgkmcnt(5)\n"
"ds_read_b64_tr_b16 v[214:215], v234 offset:43008\n"
"ds_read_b64_tr_b16 v[208:209], v234 offset:24576\n"
"v_mfma_f32_16x16x32_bf16 a[0:3], v[192:195], v[64:67], a[0:3]\n"
"ds_read_b64_tr_b16 v[210:211], v234 offset:30720\n"
"v_add_f32 v249, v249, v61\n"
"v_add_f32 v248, v248, v62\n"
"v_add_f32 v249, v249, v63\n"
"v_exp_f32 v91, v91\n"
"v_add_f32 v248, v248, v88\n"
"v_mfma_f32_16x16x32_bf16 a[0:3], v[196:199], v[96:99], a[0:3]\n"
"v_exp_f32 v92, v92\n"
"v_exp_f32 v93, v93\n"
"v_exp_f32 v94, v94\n"
"v_add_f32 v249, v249, v89\n"
"v_mfma_f32_16x16x32_bf16 a[4:7], v[196:199], v[104:107], a[4:7]\n"
"buffer_load_dwordx4 v224, s[40:43], s58 offen offset:2048 lds\n"
"v_exp_f32 v95, v95\n"
"v_add_f32 v248, v248, v90\n"
"v_exp_f32 v120, v120\n"
"v_mfma_f32_16x16x32_bf16 a[4:7], v[192:195], v[72:75], a[4:7]\n"
"v_add_f32 v249, v249, v91\n"
"v_add_f32 v248, v248, v92\n"
"v_add_f32 v249, v249, v93\n"
"v_add_f32 v248, v248, v94\n"
"v_add_f32 v249, v249, v95\n"
"v_exp_f32 v121, v121\n"
"v_mfma_f32_16x16x32_bf16 a[8:11], v[192:195], v[80:83], a[8:11]\n"
"buffer_load_dwordx4 v225, s[40:43], s58 offen offset:3072 lds\n"
"v_exp_f32 v122, v122\n"
"v_exp_f32 v123, v123\n"
"v_exp_f32 v124, v124\n"
"v_exp_f32 v125, v125\n"
"v_exp_f32 v126, v126\n"
"v_exp_f32 v127, v127\n"
"v_add_f32 v248, v248, v120\n"
"v_add_f32 v249, v249, v121\n"
"v_cvt_pk_bf16_f32 v120, v120, v121\n"
"v_add_f32 v248, v248, v122\n"
"v_add_f32 v249, v249, v123\n"
"v_cvt_pk_bf16_f32 v121, v122, v123\n"
"v_cvt_pk_bf16_f32 v122, v124, v125\n"
"v_cvt_pk_bf16_f32 v123, v126, v127\n"
"v_mfma_f32_16x16x32_bf16 a[8:11], v[196:199], v[112:115], a[8:11]\n"
"v_cvt_pk_bf16_f32 v88, v88, v89\n"
"v_cvt_pk_bf16_f32 v89, v90, v91\n"
"v_cvt_pk_bf16_f32 v90, v92, v93\n"
"v_cvt_pk_bf16_f32 v91, v94, v95\n"
"v_add_f32 v242, v242, v100\n"
"v_add_f32 v243, v243, v101\n"
"v_add_f32 v242, v242, v102\n"
"v_mfma_f32_16x16x32_bf16 a[12:15], v[196:199], v[120:123], a[12:15]\n"
"ds_read_b64_tr_b16 v[196:197], v235 offset:36864\n"
"v_add_f32 v243, v243, v103\n"
"v_add_f32 v244, v244, v108\n"
"v_add_f32 v245, v245, v109\n"
"v_add_f32 v244, v244, v110\n"
"v_add_f32 v245, v245, v111\n"
"v_add_f32 v246, v246, v116\n"
"v_mfma_f32_16x16x32_bf16 a[12:15], v[192:195], v[88:91], a[12:15]\n"
"s_waitcnt lgkmcnt(5)\n"
"ds_read_b64_tr_b16 v[198:199], v235 offset:43008\n"
"ds_read_b64_tr_b16 v[192:193], v235 offset:24576\n"
"v_add_f32 v247, v247, v117\n"
"v_add_f32 v246, v246, v118\n"
"v_add_f32 v247, v247, v119\n"
"v_add_f32 v248, v248, v124\n"
"v_add_f32 v249, v249, v125\n"
"v_mfma_f32_16x16x32_bf16 a[16:19], v[200:203], v[64:67], a[16:19]\n"
"ds_read_b64_tr_b16 v[194:195], v235 offset:30720\n"
"v_add_f32 v248, v248, v126\n"
"v_add_f32 v249, v249, v127\n"
"v_add_f32 v242, v242, v243\n"
"v_add_f32 v244, v244, v245\n"
"v_add_f32 v246, v246, v247\n"
"v_add_f32 v248, v248, v249\n"
"v_mfma_f32_16x16x32_bf16 a[16:19], v[204:207], v[96:99], a[16:19]\n"
"s_add_u32 m0, s87, 0xb000\n"
"v_permlane32_swap_b32 v242, v244\n"
"v_permlane32_swap_b32 v246, v248\n"
"v_add_f32 v242, v242, v244\n"
"v_add_f32 v246, v246, v248\n"
"v_accvgpr_read_b32 v32, a0\n"
"v_accvgpr_read_b32 v33, a1\n"
"v_accvgpr_read_b32 v34, a2\n"
"v_mfma_f32_16x16x32_bf16 a[20:23], v[204:207], v[104:107], a[20:23]\n"
"buffer_load_dwordx4 v226, s[40:43], s58 offen offset:0 lds\n"
"v_permlane16_swap_b32 v242, v246\n"
"v_add_f32 v242, v242, v246\n"
"v_rcp_f32 v243, v242\n"
"v_accvgpr_read_b32 v35, a3\n"
"v_mfma_f32_16x16x32_bf16 a[20:23], v[200:203], v[72:75], a[20:23]\n"
"v_mov_b32 v247, v243\n"
"v_accvgpr_read_b32 v40, a4\n"
"v_accvgpr_read_b32 v41, a5\n"
"v_accvgpr_read_b32 v42, a6\n"
"v_accvgpr_read_b32 v43, a7\n"
"v_accvgpr_read_b32 v48, a8\n"
"v_accvgpr_read_b32 v49, a9\n"
"v_mfma_f32_16x16x32_bf16 a[24:27], v[200:203], v[80:83], a[24:27]\n"
"v_permlane16_swap_b32 v243, v247\n"
"v_mov_b32 v245, v243\n"
"v_accvgpr_read_b32 v36, a16\n"
"v_accvgpr_read_b32 v37, a17\n"
"v_accvgpr_read_b32 v38, a18\n"
"v_accvgpr_read_b32 v39, a19\n"
"v_mov_b32 v249, v247\n"
"v_mfma_f32_16x16x32_bf16 a[24:27], v[204:207], v[112:115], a[24:27]\n"
"v_permlane32_swap_b32 v243, v245\n"
"v_mul_f32 v32, v32, v243\n"
"v_mul_f32 v33, v33, v243\n"
"v_mul_f32 v34, v34, v243\n"
"v_mul_f32 v35, v35, v243\n"
"v_mul_f32 v36, v36, v243\n"
"v_mul_f32 v37, v37, v243\n"
"v_mfma_f32_16x16x32_bf16 a[28:31], v[204:207], v[120:123], a[28:31]\n"
"ds_read_b64_tr_b16 v[204:205], v232 offset:36992\n"
"v_mul_f32 v38, v38, v243\n"
"v_mul_f32 v39, v39, v243\n"
"v_cvt_pk_bf16_f32 v128, v32, v33\n"
"v_cvt_pk_bf16_f32 v129, v34, v35\n"
"v_cvt_pk_bf16_f32 v130, v36, v37\n"
"v_cvt_pk_bf16_f32 v131, v38, v39\n"
"v_mfma_f32_16x16x32_bf16 a[28:31], v[200:203], v[88:91], a[28:31]\n"
"s_waitcnt lgkmcnt(5)\n"
"ds_read_b64_tr_b16 v[206:207], v232 offset:43136\n"
"ds_read_b64_tr_b16 v[200:201], v232 offset:24704\n"
"buffer_store_dwordx4 v[128:131], v250, s[52:55], s74 offen offset:0 sc1\n"
"v_accvgpr_read_b32 v44, a20\n"
"v_accvgpr_read_b32 v45, a21\n"
"v_accvgpr_read_b32 v46, a22\n"
"v_mfma_f32_16x16x32_bf16 a[32:35], v[208:211], v[64:67], a[32:35]\n"
"ds_read_b64_tr_b16 v[202:203], v232 offset:30848\n"
"v_accvgpr_read_b32 v47, a23\n"
"v_mul_f32 v40, v40, v245\n"
"v_mul_f32 v41, v41, v245\n"
"v_mul_f32 v42, v42, v245\n"
"v_mul_f32 v43, v43, v245\n"
"v_mul_f32 v44, v44, v245\n"
"v_mfma_f32_16x16x32_bf16 a[32:35], v[212:215], v[96:99], a[32:35]\n"
"v_mul_f32 v45, v45, v245\n"
"v_mul_f32 v46, v46, v245\n"
"v_mul_f32 v47, v47, v245\n"
"v_cvt_pk_bf16_f32 v132, v40, v41\n"
"v_cvt_pk_bf16_f32 v133, v42, v43\n"
"v_cvt_pk_bf16_f32 v134, v44, v45\n"
"v_cvt_pk_bf16_f32 v135, v46, v47\n"
"v_mfma_f32_16x16x32_bf16 a[36:39], v[212:215], v[104:107], a[36:39]\n"
"buffer_store_dwordx4 v[132:135], v250, s[52:55], s74 offen offset:1024 sc1\n"
"v_permlane32_swap_b32 v247, v249\n"
"v_accvgpr_read_b32 v50, a10\n"
"v_accvgpr_read_b32 v51, a11\n"
"v_accvgpr_read_b32 v52, a24\n"
"v_accvgpr_read_b32 v53, a25\n"
"v_mfma_f32_16x16x32_bf16 a[36:39], v[208:211], v[72:75], a[36:39]\n"
"v_accvgpr_read_b32 v54, a26\n"
"v_accvgpr_read_b32 v55, a27\n"
"v_mul_f32 v48, v48, v247\n"
"v_mul_f32 v49, v49, v247\n"
"v_mul_f32 v50, v50, v247\n"
"v_mul_f32 v51, v51, v247\n"
"v_mul_f32 v52, v52, v247\n"
"v_mfma_f32_16x16x32_bf16 a[40:43], v[208:211], v[80:83], a[40:43]\n"
"v_mul_f32 v53, v53, v247\n"
"v_mul_f32 v54, v54, v247\n"
"v_mul_f32 v55, v55, v247\n"
"v_cvt_pk_bf16_f32 v136, v48, v49\n"
"v_cvt_pk_bf16_f32 v137, v50, v51\n"
"v_cvt_pk_bf16_f32 v138, v52, v53\n"
"v_cvt_pk_bf16_f32 v139, v54, v55\n"
"v_mfma_f32_16x16x32_bf16 a[40:43], v[212:215], v[112:115], a[40:43]\n"
"buffer_store_dwordx4 v[136:139], v250, s[52:55], s74 offen offset:2048 sc1\n"
"v_accvgpr_read_b32 v56, a12\n"
"v_accvgpr_read_b32 v57, a13\n"
"v_accvgpr_read_b32 v58, a14\n"
"v_accvgpr_read_b32 v59, a15\n"
"v_accvgpr_read_b32 v60, a28\n"
"v_mfma_f32_16x16x32_bf16 a[44:47], v[212:215], v[120:123], a[44:47]\n"
"ds_read_b64_tr_b16 v[212:213], v233 offset:36992\n"
"v_accvgpr_read_b32 v61, a29\n"
"v_accvgpr_read_b32 v62, a30\n"
"v_accvgpr_read_b32 v63, a31\n"
"v_mul_f32 v56, v56, v249\n"
"v_mul_f32 v57, v57, v249\n"
"v_mul_f32 v58, v58, v249\n"
"v_mfma_f32_16x16x32_bf16 a[44:47], v[208:211], v[88:91], a[44:47]\n"
"s_waitcnt lgkmcnt(5)\n"
"ds_read_b64_tr_b16 v[214:215], v233 offset:43136\n"
"ds_read_b64_tr_b16 v[208:209], v233 offset:24704\n"
"v_mul_f32 v59, v59, v249\n"
"v_mul_f32 v60, v60, v249\n"
"v_mul_f32 v61, v61, v249\n"
"v_mul_f32 v62, v62, v249\n"
"v_mul_f32 v63, v63, v249\n"
"v_mfma_f32_16x16x32_bf16 a[48:51], v[192:195], v[64:67], a[48:51]\n"
"ds_read_b64_tr_b16 v[210:211], v233 offset:30848\n"
"v_cvt_pk_bf16_f32 v140, v56, v57\n"
"v_cvt_pk_bf16_f32 v141, v58, v59\n"
"v_cvt_pk_bf16_f32 v142, v60, v61\n"
"v_cvt_pk_bf16_f32 v143, v62, v63\n"
"v_accvgpr_read_b32 v32, a32\n"
"v_accvgpr_read_b32 v33, a33\n"
"v_mfma_f32_16x16x32_bf16 a[48:51], v[196:199], v[96:99], a[48:51]\n"
"buffer_store_dwordx4 v[140:143], v250, s[52:55], s74 offen offset:3072 sc1\n"
"v_accvgpr_read_b32 v34, a34\n"
"v_accvgpr_read_b32 v35, a35\n"
"v_mul_f32 v32, v32, v243\n"
"v_mul_f32 v33, v33, v243\n"
"v_mul_f32 v34, v34, v243\n"
"v_mfma_f32_16x16x32_bf16 a[52:55], v[196:199], v[104:107], a[52:55]\n"
"v_mul_f32 v35, v35, v243\n"
"v_cvt_pk_bf16_f32 v144, v32, v33\n"
"v_cvt_pk_bf16_f32 v145, v34, v35\n"
"v_accvgpr_read_b32 v40, a36\n"
"v_accvgpr_read_b32 v41, a37\n"
"v_accvgpr_read_b32 v42, a38\n"
"v_accvgpr_read_b32 v43, a39\n"
"v_mfma_f32_16x16x32_bf16 a[52:55], v[192:195], v[72:75], a[52:55]\n"
"v_mul_f32 v40, v40, v245\n"
"v_mul_f32 v41, v41, v245\n"
"v_mul_f32 v42, v42, v245\n"
"v_mul_f32 v43, v43, v245\n"
"v_cvt_pk_bf16_f32 v148, v40, v41\n"
"v_cvt_pk_bf16_f32 v149, v42, v43\n"
"v_accvgpr_read_b32 v48, a40\n"
"v_mfma_f32_16x16x32_bf16 a[56:59], v[192:195], v[80:83], a[56:59]\n"
"v_accvgpr_read_b32 v36, a48\n"
"v_accvgpr_read_b32 v37, a49\n"
"v_accvgpr_read_b32 v38, a50\n"
"v_accvgpr_read_b32 v39, a51\n"
"v_mul_f32 v36, v36, v243\n"
"v_mul_f32 v37, v37, v243\n"
"v_mul_f32 v38, v38, v243\n"
"v_mfma_f32_16x16x32_bf16 a[56:59], v[196:199], v[112:115], a[56:59]\n"
"v_mul_f32 v39, v39, v243\n"
"v_cvt_pk_bf16_f32 v146, v36, v37\n"
"v_cvt_pk_bf16_f32 v147, v38, v39\n"
"v_accvgpr_read_b32 v49, a41\n"
"v_accvgpr_read_b32 v50, a42\n"
"v_accvgpr_read_b32 v51, a43\n"
"v_mul_f32 v48, v48, v247\n"
"v_mfma_f32_16x16x32_bf16 a[60:63], v[196:199], v[120:123], a[60:63]\n"
"ds_read_b64_tr_b16 v[196:197], v234 offset:36992\n"
"buffer_store_dwordx4 v[144:147], v250, s[52:55], s75 offen offset:0 sc1\n"
"v_accvgpr_read_b32 v44, a52\n"
"v_accvgpr_read_b32 v45, a53\n"
"v_accvgpr_read_b32 v46, a54\n"
"v_accvgpr_read_b32 v47, a55\n"
"v_mfma_f32_16x16x32_bf16 a[60:63], v[192:195], v[88:91], a[60:63]\n"
"s_waitcnt lgkmcnt(5)\n"
"ds_read_b64_tr_b16 v[198:199], v234 offset:43136\n"
"ds_read_b64_tr_b16 v[192:193], v234 offset:24704\n"
"v_mul_f32 v44, v44, v245\n"
"v_mul_f32 v45, v45, v245\n"
"v_mul_f32 v46, v46, v245\n"
"v_mul_f32 v47, v47, v245\n"
"v_cvt_pk_bf16_f32 v150, v44, v45\n"
"v_mfma_f32_16x16x32_bf16 a[64:67], v[200:203], v[64:67], a[64:67]\n"
"ds_read_b64_tr_b16 v[194:195], v234 offset:30848\n"
"v_cvt_pk_bf16_f32 v151, v46, v47\n"
"v_accvgpr_read_b32 v52, a56\n"
"v_accvgpr_read_b32 v53, a57\n"
"v_accvgpr_read_b32 v54, a58\n"
"v_accvgpr_read_b32 v55, a59\n"
"v_mul_f32 v49, v49, v247\n"
"v_mfma_f32_16x16x32_bf16 a[64:67], v[204:207], v[96:99], a[64:67]\n"
"buffer_store_dwordx4 v[148:151], v250, s[52:55], s75 offen offset:1024 sc1\n"
"v_mul_f32 v50, v50, v247\n"
"v_mul_f32 v51, v51, v247\n"
"v_mul_f32 v52, v52, v247\n"
"v_mul_f32 v53, v53, v247\n"
"v_mul_f32 v54, v54, v247\n"
"v_mfma_f32_16x16x32_bf16 a[68:71], v[204:207], v[104:107], a[68:71]\n"
"v_mul_f32 v55, v55, v247\n"
"v_cvt_pk_bf16_f32 v152, v48, v49\n"
"v_cvt_pk_bf16_f32 v153, v50, v51\n"
"v_cvt_pk_bf16_f32 v154, v52, v53\n"
"v_cvt_pk_bf16_f32 v155, v54, v55\n"
"v_accvgpr_read_b32 v56, a44\n"
"v_accvgpr_read_b32 v57, a45\n"
"v_mfma_f32_16x16x32_bf16 a[68:71], v[200:203], v[72:75], a[68:71]\n"
"buffer_store_dwordx4 v[152:155], v250, s[52:55], s75 offen offset:2048 sc1\n"
"v_accvgpr_read_b32 v58, a46\n"
"v_accvgpr_read_b32 v59, a47\n"
"v_accvgpr_read_b32 v60, a60\n"
"v_accvgpr_read_b32 v61, a61\n"
"v_accvgpr_read_b32 v62, a62\n"
"v_mfma_f32_16x16x32_bf16 a[72:75], v[200:203], v[80:83], a[72:75]\n"
"v_accvgpr_read_b32 v63, a63\n"
"v_mul_f32 v56, v56, v249\n"
"v_mul_f32 v57, v57, v249\n"
"v_mul_f32 v58, v58, v249\n"
"v_mul_f32 v59, v59, v249\n"
"v_mul_f32 v60, v60, v249\n"
"v_mul_f32 v61, v61, v249\n"
"v_mfma_f32_16x16x32_bf16 a[72:75], v[204:207], v[112:115], a[72:75]\n"
"v_mul_f32 v62, v62, v249\n"
"v_mul_f32 v63, v63, v249\n"
"v_cvt_pk_bf16_f32 v156, v56, v57\n"
"v_cvt_pk_bf16_f32 v157, v58, v59\n"
"v_cvt_pk_bf16_f32 v158, v60, v61\n"
"v_cvt_pk_bf16_f32 v159, v62, v63\n"
"v_accvgpr_read_b32 v32, a64\n"
"v_mfma_f32_16x16x32_bf16 a[76:79], v[204:207], v[120:123], a[76:79]\n"
"ds_read_b64_tr_b16 v[204:205], v235 offset:36992\n"
"buffer_store_dwordx4 v[156:159], v250, s[52:55], s75 offen offset:3072 sc1\n"
"v_accvgpr_read_b32 v33, a65\n"
"v_accvgpr_read_b32 v34, a66\n"
"v_accvgpr_read_b32 v35, a67\n"
"v_mul_f32 v32, v32, v243\n"
"v_mfma_f32_16x16x32_bf16 a[76:79], v[200:203], v[88:91], a[76:79]\n"
"s_waitcnt lgkmcnt(5)\n"
"ds_read_b64_tr_b16 v[206:207], v235 offset:43136\n"
"ds_read_b64_tr_b16 v[200:201], v235 offset:24704\n"
"v_mul_f32 v33, v33, v243\n"
"v_mul_f32 v34, v34, v243\n"
"v_mul_f32 v35, v35, v243\n"
"v_cvt_pk_bf16_f32 v160, v32, v33\n"
"v_cvt_pk_bf16_f32 v161, v34, v35\n"
"v_mfma_f32_16x16x32_bf16 a[80:83], v[208:211], v[64:67], a[80:83]\n"
"ds_read_b64_tr_b16 v[202:203], v235 offset:30848\n"
"v_cmp_gt_u32 vcc, s59, v172\n"
"v_mfma_f32_16x16x32_bf16 a[80:83], v[212:215], v[96:99], a[80:83]\n"
"v_cndmask_b32 v239, v181, v172, vcc\n"
"v_mov_b32 v252, 0x53800000\n"
"v_mfma_f32_16x16x32_bf16 a[84:87], v[212:215], v[104:107], a[84:87]\n"
"v_cmp_nlt_f32 vcc, v242, v252\n"
"Ltp10%=:\n"
"s_cbranch_execz Ltrl0x3%=\n"
"v_accvgpr_read_b32 v40, a68\n"
"v_mfma_f32_16x16x32_bf16 a[84:87], v[208:211], v[72:75], a[84:87]\n"
"v_accvgpr_read_b32 v41, a69\n"
"v_accvgpr_read_b32 v42, a70\n"
"v_mfma_f32_16x16x32_bf16 a[88:91], v[208:211], v[80:83], a[88:91]\n"
"v_accvgpr_read_b32 v36, a80\n"
"v_accvgpr_read_b32 v37, a81\n"
"v_mfma_f32_16x16x32_bf16 a[88:91], v[212:215], v[112:115], a[88:91]\n"
"v_accvgpr_read_b32 v38, a82\n"
"v_accvgpr_read_b32 v39, a83\n"
"v_mfma_f32_16x16x32_bf16 a[92:95], v[212:215], v[120:123], a[92:95]\n"
"ds_read_b64_tr_b16 v[212:213], v232 offset:37120\n"
"v_mul_f32 v36, v36, v243\n"
"v_mfma_f32_16x16x32_bf16 a[92:95], v[208:211], v[88:91], a[92:95]\n"
"s_waitcnt lgkmcnt(5)\n"
"ds_read_b64_tr_b16 v[214:215], v232 offset:43264\n"
"ds_read_b64_tr_b16 v[208:209], v232 offset:24832\n"
"v_mfma_f32_16x16x32_bf16 a[96:99], v[192:195], v[64:67], a[96:99]\n"
"ds_read_b64_tr_b16 v[210:211], v232 offset:30976\n"
"v_mul_f32 v37, v37, v243\n"
"v_mfma_f32_16x16x32_bf16 a[96:99], v[196:199], v[96:99], a[96:99]\n"
"v_mul_f32 v38, v38, v243\n"
"v_mul_f32 v39, v39, v243\n"
"v_mfma_f32_16x16x32_bf16 a[100:103], v[196:199], v[104:107], a[100:103]\n"
"v_cvt_pk_bf16_f32 v162, v36, v37\n"
"v_cvt_pk_bf16_f32 v163, v38, v39\n"
"v_mfma_f32_16x16x32_bf16 a[100:103], v[192:195], v[72:75], a[100:103]\n"
"v_accvgpr_read_b32 v43, a71\n"
"v_accvgpr_read_b32 v44, a84\n"
"v_mfma_f32_16x16x32_bf16 a[104:107], v[192:195], v[80:83], a[104:107]\n"
"v_accvgpr_read_b32 v45, a85\n"
"v_accvgpr_read_b32 v46, a86\n"
"v_mfma_f32_16x16x32_bf16 a[104:107], v[196:199], v[112:115], a[104:107]\n"
"v_accvgpr_read_b32 v47, a87\n"
"v_mul_f32 v40, v40, v245\n"
"v_mfma_f32_16x16x32_bf16 a[108:111], v[196:199], v[120:123], a[108:111]\n"
"ds_read_b64_tr_b16 v[196:197], v233 offset:37120\n"
"v_mul_f32 v41, v41, v245\n"
"v_mfma_f32_16x16x32_bf16 a[108:111], v[192:195], v[88:91], a[108:111]\n"
"s_waitcnt lgkmcnt(5)\n"
"ds_read_b64_tr_b16 v[198:199], v233 offset:43264\n"
"ds_read_b64_tr_b16 v[192:193], v233 offset:24832\n"
"v_mfma_f32_16x16x32_bf16 a[112:115], v[200:203], v[64:67], a[112:115]\n"
"ds_read_b64_tr_b16 v[194:195], v233 offset:30976\n"
"v_mul_f32 v42, v42, v245\n"
"v_mfma_f32_16x16x32_bf16 a[112:115], v[204:207], v[96:99], a[112:115]\n"
"v_mul_f32 v43, v43, v245\n"
"v_mul_f32 v44, v44, v245\n"
"v_mfma_f32_16x16x32_bf16 a[116:119], v[204:207], v[104:107], a[116:119]\n"
"v_mul_f32 v45, v45, v245\n"
"v_mul_f32 v46, v46, v245\n"
"v_mfma_f32_16x16x32_bf16 a[116:119], v[200:203], v[72:75], a[116:119]\n"
"v_mul_f32 v47, v47, v245\n"
"v_cvt_pk_bf16_f32 v164, v40, v41\n"
"v_mfma_f32_16x16x32_bf16 a[120:123], v[200:203], v[80:83], a[120:123]\n"
"v_cvt_pk_bf16_f32 v165, v42, v43\n"
"v_cvt_pk_bf16_f32 v166, v44, v45\n"
"v_mfma_f32_16x16x32_bf16 a[120:123], v[204:207], v[112:115], a[120:123]\n"
"v_cvt_pk_bf16_f32 v167, v46, v47\n"
"v_accvgpr_read_b32 v48, a72\n"
"v_mfma_f32_16x16x32_bf16 a[124:127], v[204:207], v[120:123], a[124:127]\n"
"ds_read_b64_tr_b16 v[204:205], v234 offset:37120\n"
"v_accvgpr_read_b32 v49, a73\n"
"v_mfma_f32_16x16x32_bf16 a[124:127], v[200:203], v[88:91], a[124:127]\n"
"s_waitcnt lgkmcnt(5)\n"
"ds_read_b64_tr_b16 v[206:207], v234 offset:43264\n"
"ds_read_b64_tr_b16 v[200:201], v234 offset:24832\n"
"v_mfma_f32_16x16x32_bf16 a[128:131], v[208:211], v[64:67], a[128:131]\n"
"s_cmp_lg_u64 vcc, 0\n"
"ds_read_b64_tr_b16 v[202:203], v234 offset:30976\n"
"v_accvgpr_read_b32 v50, a74\n"
"v_mfma_f32_16x16x32_bf16 a[128:131], v[212:215], v[96:99], a[128:131]\n"
"s_cselect_b32 s80, 1, 0\n"
"v_mov_b32 v78, 0x27000\n"
"v_accvgpr_read_b32 v51, a75\n"
"v_mfma_f32_16x16x32_bf16 a[132:135], v[212:215], v[104:107], a[132:135]\n"
"s_lshl_b32 s82, %[w], 2\n"
"v_accvgpr_read_b32 v52, a88\n"
"v_accvgpr_read_b32 v53, a89\n"
"v_mfma_f32_16x16x32_bf16 a[132:135], v[208:211], v[72:75], a[132:135]\n"
"v_accvgpr_read_b32 v54, a90\n"
"v_accvgpr_read_b32 v55, a91\n"
"v_mfma_f32_16x16x32_bf16 a[136:139], v[208:211], v[80:83], a[136:139]\n"
"v_mul_f32 v48, v48, v247\n"
"v_mul_f32 v49, v49, v247\n"
"v_mfma_f32_16x16x32_bf16 a[136:139], v[212:215], v[112:115], a[136:139]\n"
"v_mov_b32 v77, s80\n"
"v_add_u32 v76, s82, v78\n"
"v_mfma_f32_16x16x32_bf16 a[140:143], v[212:215], v[120:123], a[140:143]\n"
"ds_read_b64_tr_b16 v[212:213], v235 offset:37120\n"
"v_mul_f32 v50, v50, v247\n"
"v_mfma_f32_16x16x32_bf16 a[140:143], v[208:211], v[88:91], a[140:143]\n"
"s_waitcnt lgkmcnt(5)\n"
"ds_read_b64_tr_b16 v[214:215], v235 offset:43264\n"
"ds_read_b64_tr_b16 v[208:209], v235 offset:24832\n"
"v_mfma_f32_16x16x32_bf16 a[144:147], v[192:195], v[64:67], a[144:147]\n"
"ds_read_b64_tr_b16 v[210:211], v235 offset:30976\n"
"v_mul_f32 v51, v51, v247\n"
"v_mfma_f32_16x16x32_bf16 a[144:147], v[196:199], v[96:99], a[144:147]\n"
"ds_write_b32 v76, v77\n"
"v_mul_f32 v52, v52, v247\n"
"v_mfma_f32_16x16x32_bf16 a[148:151], v[196:199], v[104:107], a[148:151]\n"
"v_mul_f32 v53, v53, v247\n"
"v_mul_f32 v54, v54, v247\n"
"v_mfma_f32_16x16x32_bf16 a[148:151], v[192:195], v[72:75], a[148:151]\n"
"v_mul_f32 v55, v55, v247\n"
"v_cvt_pk_bf16_f32 v168, v48, v49\n"
"v_mfma_f32_16x16x32_bf16 a[152:155], v[192:195], v[80:83], a[152:155]\n"
"v_cvt_pk_bf16_f32 v169, v50, v51\n"
"v_cvt_pk_bf16_f32 v170, v52, v53\n"
"v_cvt_pk_bf16_f32 v171, v54, v55\n"
"v_mfma_f32_16x16x32_bf16 a[152:155], v[196:199], v[112:115], a[152:155]\n"
"v_add_u32 v229, 0x18000, v227\n"
"v_lshl_add_u32 v236, v172, 4, s81\n"
"v_mfma_f32_16x16x32_bf16 a[156:159], v[196:199], v[120:123], a[156:159]\n"
"s_waitcnt vmcnt(63) lgkmcnt(0)\n"
"s_barrier\n"
"ds_read_b64_tr_b16 v[196:197], v229 offset:5120\n"
"v_mfma_f32_16x16x32_bf16 a[156:159], v[192:195], v[88:91], a[156:159]\n"
"ds_read_b64_tr_b16 v[198:199], v229 offset:7680\n"
"ds_read_b64_tr_b16 v[192:193], v229 offset:0\n"
"v_mfma_f32_16x16x32_bf16 a[160:163], v[200:203], v[64:67], a[160:163]\n"
"s_mul_i32 s58, s95, 0x3c000\n"
"s_add_u32 m0, s87, 0xf000\n"
"ds_read_b64_tr_b16 v[194:195], v229 offset:2560\n"
"v_mfma_f32_16x16x32_bf16 a[160:163], v[204:207], v[96:99], a[160:163]\n"
"s_add_u32 s58, s58, 0x14000\n"
"buffer_load_dwordx4 v222, s[40:43], s58 offen offset:0 lds\n"
"v_mfma_f32_16x16x32_bf16 a[164:167], v[204:207], v[104:107], a[164:167]\n"
"v_accvgpr_read_b32 v56, a76\n"
"v_accvgpr_read_b32 v57, a77\n"
"v_mfma_f32_16x16x32_bf16 a[164:167], v[200:203], v[72:75], a[164:167]\n"
"buffer_load_dwordx4 v223, s[40:43], s58 offen offset:1024 lds\n"
"v_mfma_f32_16x16x32_bf16 a[168:171], v[200:203], v[80:83], a[168:171]\n"
"v_accvgpr_read_b32 v58, a78\n"
"v_accvgpr_read_b32 v59, a79\n"
"v_mfma_f32_16x16x32_bf16 a[168:171], v[204:207], v[112:115], a[168:171]\n"
"ds_read_b128 v[68:71], v78 offset:0\n"
"v_accvgpr_read_b32 v60, a92\n"
"v_mfma_f32_16x16x32_bf16 a[172:175], v[204:207], v[120:123], a[172:175]\n"
"ds_read_b64_tr_b16 v[204:205], v229 offset:5152\n"
"v_accvgpr_read_b32 v61, a93\n"
"v_mfma_f32_16x16x32_bf16 a[172:175], v[200:203], v[88:91], a[172:175]\n"
"ds_read_b64_tr_b16 v[206:207], v229 offset:7712\n"
"ds_read_b64_tr_b16 v[200:201], v229 offset:32\n"
"v_mfma_f32_16x16x32_bf16 a[176:179], v[208:211], v[64:67], a[176:179]\n"
"ds_read_b64_tr_b16 v[202:203], v229 offset:2592\n"
"v_accvgpr_read_b32 v62, a94\n"
"v_mfma_f32_16x16x32_bf16 a[176:179], v[212:215], v[96:99], a[176:179]\n"
"buffer_load_dwordx4 v224, s[40:43], s58 offen offset:2048 lds\n"
"v_accvgpr_read_b32 v63, a95\n"
"v_mul_f32 v56, v56, v249\n"
"v_mul_f32 v57, v57, v249\n"
"v_mul_f32 v58, v58, v249\n"
"v_mul_f32 v59, v59, v249\n"
"v_mul_f32 v60, v60, v249\n"
"v_mul_f32 v61, v61, v249\n"
"v_mul_f32 v62, v62, v249\n"
"v_mul_f32 v63, v63, v249\n"
"v_cvt_pk_bf16_f32 v172, v56, v57\n"
"v_cvt_pk_bf16_f32 v173, v58, v59\n"
"v_cvt_pk_bf16_f32 v174, v60, v61\n"
"v_cvt_pk_bf16_f32 v175, v62, v63\n"
"v_mfma_f32_16x16x32_bf16 a[180:183], v[212:215], v[104:107], a[180:183]\n"
"v_accvgpr_read_b32 v32, a96\n"
"v_accvgpr_read_b32 v33, a97\n"
"v_mfma_f32_16x16x32_bf16 a[180:183], v[208:211], v[72:75], a[180:183]\n"
"buffer_load_dwordx4 v225, s[40:43], s58 offen offset:3072 lds\n"
"v_mfma_f32_16x16x32_bf16 a[184:187], v[208:211], v[80:83], a[184:187]\n"
"s_add_u32 m0, s87, 0x10000\n"
"v_accvgpr_read_b32 v34, a98\n"
"v_accvgpr_read_b32 v35, a99\n"
"v_mfma_f32_16x16x32_bf16 a[184:187], v[212:215], v[112:115], a[184:187]\n"
"buffer_load_dwordx4 v226, s[40:43], s58 offen offset:0 lds\n"
"v_mfma_f32_16x16x32_bf16 a[188:191], v[212:215], v[120:123], a[188:191]\n"
"ds_read_b64_tr_b16 v[212:213], v229 offset:5184\n"
"v_accvgpr_read_b32 v36, a112\n"
"v_mfma_f32_16x16x32_bf16 a[188:191], v[208:211], v[88:91], a[188:191]\n"
"s_waitcnt lgkmcnt(6)\n"
"ds_read_b64_tr_b16 v[214:215], v229 offset:7744\n"
"ds_read_b64_tr_b16 v[208:209], v229 offset:64\n"
"v_mfma_f32_16x16x32_bf16 a[0:3], v[192:195], v[128:131], 0\n"
"ds_read_b64_tr_b16 v[210:211], v229 offset:2624\n"
"v_accvgpr_read_b32 v37, a113\n"
"v_mfma_f32_16x16x32_bf16 a[0:3], v[196:199], v[144:147], a[0:3]\n"
"s_waitcnt lgkmcnt(8)\n"
"v_or3_b32 v68, v68, v69, v70\n"
"v_accvgpr_read_b32 v38, a114\n"
"v_mfma_f32_16x16x32_bf16 a[4:7], v[196:199], v[148:151], 0\n"
"v_or_b32 v68, v68, v71\n"
"v_accvgpr_read_b32 v39, a115\n"
"v_mfma_f32_16x16x32_bf16 a[4:7], v[192:195], v[132:135], a[4:7]\n"
"v_mul_f32 v32, v32, v243\n"
"v_mul_f32 v33, v33, v243\n"
"v_mfma_f32_16x16x32_bf16 a[8:11], v[192:195], v[136:139], 0\n"
"v_mul_f32 v34, v34, v243\n"
"v_mul_f32 v35, v35, v243\n"
"v_mfma_f32_16x16x32_bf16 a[8:11], v[196:199], v[152:155], a[8:11]\n"
"v_readfirstlane_b32 s80, v68\n"
"v_mul_f32 v36, v36, v243\n"
"v_mul_f32 v37, v37, v243\n"
"v_mul_f32 v38, v38, v243\n"
"v_mul_f32 v39, v39, v243\n"
"v_cvt_pk_bf16_f32 v176, v32, v33\n"
"v_cvt_pk_bf16_f32 v177, v34, v35\n"
"v_cvt_pk_bf16_f32 v178, v36, v37\n"
"v_cvt_pk_bf16_f32 v179, v38, v39\n"
"v_mfma_f32_16x16x32_bf16 a[12:15], v[196:199], v[156:159], 0\n"
"ds_read_b64_tr_b16 v[196:197], v229 offset:5216\n"
"v_accvgpr_read_b32 v40, a100\n"
"v_mfma_f32_16x16x32_bf16 a[12:15], v[192:195], v[140:143], a[12:15]\n"
"s_waitcnt lgkmcnt(5)\n"
"ds_read_b64_tr_b16 v[198:199], v229 offset:7776\n"
"ds_read_b64_tr_b16 v[192:193], v229 offset:96\n"
"v_mfma_f32_16x16x32_bf16 a[16:19], v[200:203], v[128:131], 0\n"
"ds_read_b64_tr_b16 v[194:195], v229 offset:2656\n"
"v_accvgpr_read_b32 v41, a101\n"
"v_mfma_f32_16x16x32_bf16 a[16:19], v[204:207], v[144:147], a[16:19]\n"
"s_cmp_gt_u32 s80, s63\n"
"v_accvgpr_read_b32 v42, a102\n"
"v_accvgpr_read_b32 v43, a103\n"
"v_mfma_f32_16x16x32_bf16 a[20:23], v[204:207], v[148:151], 0\n"
"s_cbranch_scc1 Lfail%=\n"
"v_accvgpr_read_b32 v44, a116\n"
"v_accvgpr_read_b32 v45, a117\n"
"v_mfma_f32_16x16x32_bf16 a[20:23], v[200:203], v[132:135], a[20:23]\n"
"v_accvgpr_read_b32 v46, a118\n"
"v_accvgpr_read_b32 v47, a119\n"
"v_mfma_f32_16x16x32_bf16 a[24:27], v[200:203], v[136:139], 0\n"
"v_mul_f32 v40, v40, v245\n"
"v_mul_f32 v41, v41, v245\n"
"v_mfma_f32_16x16x32_bf16 a[24:27], v[204:207], v[152:155], a[24:27]\n"
"v_mul_f32 v42, v42, v245\n"
"v_mul_f32 v43, v43, v245\n"
"v_mfma_f32_16x16x32_bf16 a[28:31], v[204:207], v[156:159], 0\n"
"ds_read_b64_tr_b16 v[204:205], v229 offset:5248\n"
"v_mul_f32 v44, v44, v245\n"
"v_mfma_f32_16x16x32_bf16 a[28:31], v[200:203], v[140:143], a[28:31]\n"
"s_waitcnt lgkmcnt(5)\n"
"ds_read_b64_tr_b16 v[206:207], v229 offset:7808\n"
"ds_read_b64_tr_b16 v[200:201], v229 offset:128\n"
"v_mfma_f32_16x16x32_bf16 a[192:195], v[208:211], v[128:131], 0\n"
"ds_read_b64_tr_b16 v[202:203], v229 offset:2688\n"
"v_mul_f32 v45, v45, v245\n"
"v_mfma_f32_16x16x32_bf16 a[192:195], v[212:215], v[144:147], a[192:195]\n"
"v_mul_f32 v46, v46, v245\n"
"v_mul_f32 v47, v47, v245\n"
"v_cvt_pk_bf16_f32 v180, v40, v41\n"
"v_cvt_pk_bf16_f32 v181, v42, v43\n"
"v_cvt_pk_bf16_f32 v182, v44, v45\n"
"v_cvt_pk_bf16_f32 v183, v46, v47\n"
"v_mfma_f32_16x16x32_bf16 a[196:199], v[212:215], v[148:151], 0\n"
"v_accvgpr_read_b32 v48, a104\n"
"v_accvgpr_read_b32 v49, a105\n"
"v_mfma_f32_16x16x32_bf16 a[196:199], v[208:211], v[132:135], a[196:199]\n"
"v_accvgpr_read_b32 v50, a106\n"
"v_accvgpr_read_b32 v51, a107\n"
"v_mfma_f32_16x16x32_bf16 a[200:203], v[208:211], v[136:139], 0\n"
"v_accvgpr_read_b32 v52, a120\n"
"v_accvgpr_read_b32 v53, a121\n"
"v_mfma_f32_16x16x32_bf16 a[200:203], v[212:215], v[152:155], a[200:203]\n"
"v_accvgpr_read_b32 v54, a122\n"
"v_accvgpr_read_b32 v55, a123\n"
"v_mfma_f32_16x16x32_bf16 a[204:207], v[212:215], v[156:159], 0\n"
"ds_read_b64_tr_b16 v[212:213], v229 offset:15360\n"
"v_mul_f32 v48, v48, v247\n"
"v_mfma_f32_16x16x32_bf16 a[204:207], v[208:211], v[140:143], a[204:207]\n"
"s_waitcnt lgkmcnt(5)\n"
"ds_read_b64_tr_b16 v[214:215], v229 offset:17920\n"
"ds_read_b64_tr_b16 v[208:209], v229 offset:10240\n"
"v_mfma_f32_16x16x32_bf16 a[208:211], v[192:195], v[128:131], 0\n"
"ds_read_b64_tr_b16 v[210:211], v229 offset:12800\n"
"v_mul_f32 v49, v49, v247\n"
"v_mfma_f32_16x16x32_bf16 a[208:211], v[196:199], v[144:147], a[208:211]\n"
"v_mul_f32 v50, v50, v247\n"
"v_mul_f32 v51, v51, v247\n"
"v_mfma_f32_16x16x32_bf16 a[212:215], v[196:199], v[148:151], 0\n"
"v_mul_f32 v52, v52, v247\n"
"v_mul_f32 v53, v53, v247\n"
"v_mfma_f32_16x16x32_bf16 a[212:215], v[192:195], v[132:135], a[212:215]\n"
"v_mul_f32 v54, v54, v247\n"
"v_mul_f32 v55, v55, v247\n"
"v_mfma_f32_16x16x32_bf16 a[216:219], v[192:195], v[136:139], 0\n"
"v_cvt_pk_bf16_f32 v184, v48, v49\n"
"v_cvt_pk_bf16_f32 v185, v50, v51\n"
"v_mfma_f32_16x16x32_bf16 a[216:219], v[196:199], v[152:155], a[216:219]\n"
"v_cvt_pk_bf16_f32 v186, v52, v53\n"
"v_cvt_pk_bf16_f32 v187, v54, v55\n"
"v_mfma_f32_16x16x32_bf16 a[220:223], v[196:199], v[156:159], 0\n"
"ds_read_b64_tr_b16 v[196:197], v229 offset:15392\n"
"v_accvgpr_read_b32 v56, a108\n"
"v_mfma_f32_16x16x32_bf16 a[220:223], v[192:195], v[140:143], a[220:223]\n"
"s_waitcnt lgkmcnt(5)\n"
"ds_read_b64_tr_b16 v[198:199], v229 offset:17952\n"
"ds_read_b64_tr_b16 v[192:193], v229 offset:10272\n"
"v_mfma_f32_16x16x32_bf16 a[224:227], v[200:203], v[128:131], 0\n"
"ds_read_b64_tr_b16 v[194:195], v229 offset:12832\n"
"v_accvgpr_read_b32 v57, a109\n"
"v_mfma_f32_16x16x32_bf16 a[224:227], v[204:207], v[144:147], a[224:227]\n"
"v_accvgpr_read_b32 v58, a110\n"
"v_accvgpr_read_b32 v59, a111\n"
"v_mfma_f32_16x16x32_bf16 a[228:231], v[204:207], v[148:151], 0\n"
"s_lshl_b32 s82, s23, 2\n"
"v_accvgpr_read_b32 v60, a124\n"
"v_accvgpr_read_b32 v61, a125\n"
"v_mfma_f32_16x16x32_bf16 a[228:231], v[200:203], v[132:135], a[228:231]\n"
"v_accvgpr_read_b32 v62, a126\n"
"v_accvgpr_read_b32 v63, a127\n"
"v_mfma_f32_16x16x32_bf16 a[232:235], v[200:203], v[136:139], 0\n"
"s_add_u32 s82, s82, s60\n"
"v_mul_f32 v56, v56, v249\n"
"v_mul_f32 v57, v57, v249\n"
"v_mfma_f32_16x16x32_bf16 a[232:235], v[204:207], v[152:155], a[232:235]\n"
"v_mul_f32 v58, v58, v249\n"
"v_mul_f32 v59, v59, v249\n"
"v_mfma_f32_16x16x32_bf16 a[236:239], v[204:207], v[156:159], 0\n"
"ds_read_b64_tr_b16 v[204:205], v229 offset:15424\n"
"v_mul_f32 v60, v60, v249\n"
"v_mfma_f32_16x16x32_bf16 a[236:239], v[200:203], v[140:143], a[236:239]\n"
"s_waitcnt lgkmcnt(5)\n"
"ds_read_b64_tr_b16 v[206:207], v229 offset:17984\n"
"ds_read_b64_tr_b16 v[200:201], v229 offset:10304\n"
"v_mfma_f32_16x16x32_bf16 a[240:243], v[208:211], v[128:131], 0\n"
"ds_read_b64_tr_b16 v[202:203], v229 offset:12864\n"
"v_mul_f32 v61, v61, v249\n"
"v_mfma_f32_16x16x32_bf16 a[240:243], v[212:215], v[144:147], a[240:243]\n"
"v_mul_f32 v62, v62, v249\n"
"v_mul_f32 v63, v63, v249\n"
"v_mfma_f32_16x16x32_bf16 a[244:247], v[212:215], v[148:151], 0\n"
"s_waitcnt vmcnt(0) lgkmcnt(0)\n"
"s_barrier\n"
"buffer_store_dword v253, v239, s[96:99], s82 offen offset:0 sc1\n"
"v_cvt_pk_bf16_f32 v188, v56, v57\n"
"v_cvt_pk_bf16_f32 v189, v58, v59\n"
"v_cvt_pk_bf16_f32 v190, v60, v61\n"
"v_cvt_pk_bf16_f32 v191, v62, v63\n"
"v_mfma_f32_16x16x32_bf16 a[244:247], v[208:211], v[132:135], a[244:247]\n"
"buffer_store_dwordx4 v[160:163], v250, s[52:55], s76 offen offset:0 sc1\n"
"v_mfma_f32_16x16x32_bf16 a[248:251], v[208:211], v[136:139], 0\n"
"v_accvgpr_read_b32 v32, a128\n"
"v_accvgpr_read_b32 v33, a129\n"
"v_mfma_f32_16x16x32_bf16 a[248:251], v[212:215], v[152:155], a[248:251]\n"
"v_accvgpr_read_b32 v34, a130\n"
"v_accvgpr_read_b32 v35, a131\n"
"v_mfma_f32_16x16x32_bf16 a[252:255], v[212:215], v[156:159], 0\n"
"ds_read_b64_tr_b16 v[212:213], v229 offset:15456\n"
"v_accvgpr_read_b32 v36, a144\n"
"v_mfma_f32_16x16x32_bf16 a[252:255], v[208:211], v[140:143], a[252:255]\n"
"ds_read_b64_tr_b16 v[214:215], v229 offset:18016\n"
"ds_read_b64_tr_b16 v[208:209], v229 offset:10336\n"
"v_mfma_f32_16x16x32_bf16 v[64:67], v[192:195], v[128:131], 0\n"
"ds_read_b64_tr_b16 v[210:211], v229 offset:12896\n"
"v_accvgpr_read_b32 v37, a145\n"
"v_mfma_f32_16x16x32_bf16 v[64:67], v[196:199], v[144:147], v[64:67]\n"
"v_accvgpr_read_b32 v38, a146\n"
"v_accvgpr_read_b32 v39, a147\n"
"v_mfma_f32_16x16x32_bf16 v[68:71], v[196:199], v[148:151], 0\n"
"v_mul_f32 v32, v32, v243\n"
"v_mul_f32 v33, v33, v243\n"
"v_mfma_f32_16x16x32_bf16 v[68:71], v[192:195], v[132:135], v[68:71]\n"
"v_mul_f32 v34, v34, v243\n"
"v_mul_f32 v35, v35, v243\n"
"v_mfma_f32_16x16x32_bf16 v[72:75], v[192:195], v[136:139], 0\n"
"v_mul_f32 v36, v36, v243\n"
"v_mul_f32 v37, v37, v243\n"
"v_mfma_f32_16x16x32_bf16 v[72:75], v[196:199], v[152:155], v[72:75]\n"
"v_mul_f32 v38, v38, v243\n"
"v_mul_f32 v39, v39, v243\n"
"v_mfma_f32_16x16x32_bf16 v[76:79], v[196:199], v[156:159], 0\n"
"ds_read_b64_tr_b16 v[196:197], v229 offset:15488\n"
"v_cvt_pk_bf16_f32 v0, v32, v33\n"
"v_cvt_pk_bf16_f32 v1, v34, v35\n"
"v_cvt_pk_bf16_f32 v2, v36, v37\n"
"v_cvt_pk_bf16_f32 v3, v38, v39\n"
"v_mfma_f32_16x16x32_bf16 v[76:79], v[192:195], v[140:143], v[76:79]\n"
"ds_read_b64_tr_b16 v[198:199], v229 offset:18048\n"
"ds_read_b64_tr_b16 v[192:193], v229 offset:10368\n"
"v_mfma_f32_16x16x32_bf16 v[80:83], v[200:203], v[128:131], 0\n"
"ds_read_b64_tr_b16 v[194:195], v229 offset:12928\n"
"v_accvgpr_read_b32 v40, a132\n"
"v_mfma_f32_16x16x32_bf16 v[80:83], v[204:207], v[144:147], v[80:83]\n"
"buffer_store_dwordx4 v[164:167], v250, s[52:55], s76 offen offset:1024 sc1\n"
"v_mfma_f32_16x16x32_bf16 v[84:87], v[204:207], v[148:151], 0\n"
"v_accvgpr_read_b32 v41, a133\n"
"v_accvgpr_read_b32 v42, a134\n"
"v_mfma_f32_16x16x32_bf16 v[84:87], v[200:203], v[132:135], v[84:87]\n"
"v_accvgpr_read_b32 v43, a135\n"
"v_accvgpr_read_b32 v44, a148\n"
"v_mfma_f32_16x16x32_bf16 v[88:91], v[200:203], v[136:139], 0\n"
"v_accvgpr_read_b32 v45, a149\n"
"v_accvgpr_read_b32 v46, a150\n"
"v_mfma_f32_16x16x32_bf16 v[88:91], v[204:207], v[152:155], v[88:91]\n"
"v_accvgpr_read_b32 v47, a151\n"
"v_mul_f32 v40, v40, v245\n"
"v_mfma_f32_16x16x32_bf16 v[92:95], v[204:207], v[156:159], 0\n"
"ds_read_b64_tr_b16 v[204:205], v229 offset:25600\n"
"v_mul_f32 v41, v41, v245\n"
"v_mfma_f32_16x16x32_bf16 v[92:95], v[200:203], v[140:143], v[92:95]\n"
"s_waitcnt lgkmcnt(5)\n"
"ds_read_b64_tr_b16 v[206:207], v229 offset:28160\n"
"ds_read_b64_tr_b16 v[200:201], v229 offset:20480\n"
"v_mfma_f32_16x16x32_bf16 v[96:99], v[208:211], v[128:131], 0\n"
"ds_read_b64_tr_b16 v[202:203], v229 offset:23040\n"
"v_mul_f32 v42, v42, v245\n"
"v_mfma_f32_16x16x32_bf16 v[96:99], v[212:215], v[144:147], v[96:99]\n"
"v_mul_f32 v43, v43, v245\n"
"v_mul_f32 v44, v44, v245\n"
"v_mfma_f32_16x16x32_bf16 v[100:103], v[212:215], v[148:151], 0\n"
"v_mul_f32 v45, v45, v245\n"
"v_mul_f32 v46, v46, v245\n"
"v_mul_f32 v47, v47, v245\n"
"v_cvt_pk_bf16_f32 v4, v40, v41\n"
"v_cvt_pk_bf16_f32 v5, v42, v43\n"
"v_cvt_pk_bf16_f32 v6, v44, v45\n"
"v_cvt_pk_bf16_f32 v7, v46, v47\n"
"v_mfma_f32_16x16x32_bf16 v[100:103], v[208:211], v[132:135], v[100:103]\n"
"buffer_store_dwordx4 v[168:171], v250, s[52:55], s76 offen offset:2048 sc1\n"
"v_mfma_f32_16x16x32_bf16 v[104:107], v[208:211], v[136:139], 0\n"
"v_accvgpr_read_b32 v48, a136\n"
"v_accvgpr_read_b32 v49, a137\n"
"v_mfma_f32_16x16x32_bf16 v[104:107], v[212:215], v[152:155], v[104:107]\n"
"v_accvgpr_read_b32 v50, a138\n"
"v_accvgpr_read_b32 v51, a139\n"
"v_mfma_f32_16x16x32_bf16 v[108:111], v[212:215], v[156:159], 0\n"
"ds_read_b64_tr_b16 v[212:213], v229 offset:25632\n"
"v_accvgpr_read_b32 v52, a152\n"
"v_mfma_f32_16x16x32_bf16 v[108:111], v[208:211], v[140:143], v[108:111]\n"
"s_waitcnt lgkmcnt(5)\n"
"ds_read_b64_tr_b16 v[214:215], v229 offset:28192\n"
"ds_read_b64_tr_b16 v[208:209], v229 offset:20512\n"
"v_mfma_f32_16x16x32_bf16 v[112:115], v[192:195], v[128:131], 0\n"
"ds_read_b64_tr_b16 v[210:211], v229 offset:23072\n"
"v_accvgpr_read_b32 v53, a153\n"
"v_mfma_f32_16x16x32_bf16 v[112:115], v[196:199], v[144:147], v[112:115]\n"
"v_accvgpr_read_b32 v54, a154\n"
"v_accvgpr_read_b32 v55, a155\n"
"v_mfma_f32_16x16x32_bf16 v[116:119], v[196:199], v[148:151], 0\n"
"v_mul_f32 v48, v48, v247\n"
"v_mul_f32 v49, v49, v247\n"
"v_mfma_f32_16x16x32_bf16 v[116:119], v[192:195], v[132:135], v[116:119]\n"
"v_mul_f32 v50, v50, v247\n"
"v_mul_f32 v51, v51, v247\n"
"v_mfma_f32_16x16x32_bf16 v[120:123], v[192:195], v[136:139], 0\n"
"v_mul_f32 v52, v52, v247\n"
"v_mul_f32 v53, v53, v247\n"
"v_mfma_f32_16x16x32_bf16 v[120:123], v[196:199], v[152:155], v[120:123]\n"
"v_mul_f32 v54, v54, v247\n"
"v_mul_f32 v55, v55, v247\n"
"v_mfma_f32_16x16x32_bf16 v[124:127], v[196:199], v[156:159], 0\n"
"ds_read_b64_tr_b16 v[196:197], v229 offset:25664\n"
"v_cvt_pk_bf16_f32 v8, v48, v49\n"
"v_cvt_pk_bf16_f32 v9, v50, v51\n"
"v_cvt_pk_bf16_f32 v10, v52, v53\n"
"v_cvt_pk_bf16_f32 v11, v54, v55\n"
"v_mfma_f32_16x16x32_bf16 v[124:127], v[192:195], v[140:143], v[124:127]\n"
"s_waitcnt lgkmcnt(5)\n"
"ds_read_b64_tr_b16 v[198:199], v229 offset:28224\n"
"ds_read_b64_tr_b16 v[192:193], v229 offset:20544\n"
"v_mfma_f32_16x16x32_bf16 a[0:3], v[200:203], v[160:163], a[0:3]\n"
"ds_read_b64_tr_b16 v[194:195], v229 offset:23104\n"
"v_accvgpr_read_b32 v56, a140\n"
"v_mfma_f32_16x16x32_bf16 a[0:3], v[204:207], v[176:179], a[0:3]\n"
"buffer_store_dwordx4 v[172:175], v250, s[52:55], s76 offen offset:3072 sc1\n"
"v_mfma_f32_16x16x32_bf16 a[4:7], v[204:207], v[180:183], a[4:7]\n"
"v_accvgpr_read_b32 v57, a141\n"
"v_accvgpr_read_b32 v58, a142\n"
"v_mfma_f32_16x16x32_bf16 a[4:7], v[200:203], v[164:167], a[4:7]\n"
"v_accvgpr_read_b32 v59, a143\n"
"v_accvgpr_read_b32 v60, a156\n"
"v_mfma_f32_16x16x32_bf16 a[8:11], v[200:203], v[168:171], a[8:11]\n"
"v_accvgpr_read_b32 v61, a157\n"
"v_accvgpr_read_b32 v62, a158\n"
"v_mfma_f32_16x16x32_bf16 a[8:11], v[204:207], v[184:187], a[8:11]\n"
"v_accvgpr_read_b32 v63, a159\n"
"v_mul_f32 v56, v56, v249\n"
"v_mfma_f32_16x16x32_bf16 a[12:15], v[204:207], v[188:191], a[12:15]\n"
"ds_read_b64_tr_b16 v[204:205], v229 offset:25696\n"
"v_mul_f32 v57, v57, v249\n"
"v_mfma_f32_16x16x32_bf16 a[12:15], v[200:203], v[172:175], a[12:15]\n"
"s_waitcnt lgkmcnt(5)\n"
"ds_read_b64_tr_b16 v[206:207], v229 offset:28256\n"
"ds_read_b64_tr_b16 v[200:201], v229 offset:20576\n"
"v_mfma_f32_16x16x32_bf16 a[16:19], v[208:211], v[160:163], a[16:19]\n"
"ds_read_b64_tr_b16 v[202:203], v229 offset:23136\n"
"v_mul_f32 v58, v58, v249\n"
"v_mfma_f32_16x16x32_bf16 a[16:19], v[212:215], v[176:179], a[16:19]\n"
"buffer_load_dword v255, v240, s[96:99], s60 offen offset:0 sc1\n"
"v_mfma_f32_16x16x32_bf16 a[20:23], v[212:215], v[180:183], a[20:23]\n"
"v_mul_f32 v59, v59, v249\n"
"v_mul_f32 v60, v60, v249\n"
"v_mfma_f32_16x16x32_bf16 a[20:23], v[208:211], v[164:167], a[20:23]\n"
"v_mul_f32 v61, v61, v249\n"
"v_mul_f32 v62, v62, v249\n"
"v_mul_f32 v63, v63, v249\n"
"v_cvt_pk_bf16_f32 v12, v56, v57\n"
"v_cvt_pk_bf16_f32 v13, v58, v59\n"
"v_cvt_pk_bf16_f32 v14, v60, v61\n"
"v_cvt_pk_bf16_f32 v15, v62, v63\n"
"v_mfma_f32_16x16x32_bf16 a[24:27], v[208:211], v[168:171], a[24:27]\n"
"buffer_store_dwordx4 v[176:179], v250, s[52:55], s77 offen offset:0 sc1\n"
"v_mfma_f32_16x16x32_bf16 a[24:27], v[212:215], v[184:187], a[24:27]\n"
"v_accvgpr_read_b32 v32, a160\n"
"v_accvgpr_read_b32 v33, a161\n"
"v_mfma_f32_16x16x32_bf16 a[28:31], v[212:215], v[188:191], a[28:31]\n"
"ds_read_b64_tr_b16 v[212:213], v229 offset:25728\n"
"v_accvgpr_read_b32 v34, a162\n"
"v_mfma_f32_16x16x32_bf16 a[28:31], v[208:211], v[172:175], a[28:31]\n"
"s_waitcnt lgkmcnt(5)\n"
"ds_read_b64_tr_b16 v[214:215], v229 offset:28288\n"
"ds_read_b64_tr_b16 v[208:209], v229 offset:20608\n"
"v_mfma_f32_16x16x32_bf16 a[192:195], v[192:195], v[160:163], a[192:195]\n"
"ds_read_b64_tr_b16 v[210:211], v229 offset:23168\n"
"v_accvgpr_read_b32 v35, a163\n"
"v_mfma_f32_16x16x32_bf16 a[192:195], v[196:199], v[176:179], a[192:195]\n"
"v_accvgpr_read_b32 v36, a176\n"
"v_accvgpr_read_b32 v37, a177\n"
"v_mfma_f32_16x16x32_bf16 a[196:199], v[196:199], v[180:183], a[196:199]\n"
"v_accvgpr_read_b32 v38, a178\n"
"v_accvgpr_read_b32 v39, a179\n"
"v_mfma_f32_16x16x32_bf16 a[196:199], v[192:195], v[164:167], a[196:199]\n"
"v_mul_f32 v32, v32, v243\n"
"v_mul_f32 v33, v33, v243\n"
"v_mfma_f32_16x16x32_bf16 a[200:203], v[192:195], v[168:171], a[200:203]\n"
"v_mul_f32 v34, v34, v243\n"
"v_mul_f32 v35, v35, v243\n"
"v_mfma_f32_16x16x32_bf16 a[200:203], v[196:199], v[184:187], a[200:203]\n"
"v_mul_f32 v36, v36, v243\n"
"v_mul_f32 v37, v37, v243\n"
"v_mfma_f32_16x16x32_bf16 a[204:207], v[196:199], v[188:191], a[204:207]\n"
"ds_read_b64_tr_b16 v[196:197], v229 offset:35840\n"
"v_mul_f32 v38, v38, v243\n"
"v_mfma_f32_16x16x32_bf16 a[204:207], v[192:195], v[172:175], a[204:207]\n"
"s_waitcnt lgkmcnt(5)\n"
"ds_read_b64_tr_b16 v[198:199], v229 offset:38400\n"
"ds_read_b64_tr_b16 v[192:193], v229 offset:30720\n"
"v_mul_f32 v39, v39, v243\n"
"v_cvt_pk_bf16_f32 v16, v32, v33\n"
"v_cvt_pk_bf16_f32 v17, v34, v35\n"
"v_cvt_pk_bf16_f32 v18, v36, v37\n"
"v_cvt_pk_bf16_f32 v19, v38, v39\n"
"v_mfma_f32_16x16x32_bf16 a[208:211], v[200:203], v[160:163], a[208:211]\n"
"ds_read_b64_tr_b16 v[194:195], v229 offset:33280\n"
"v_accvgpr_read_b32 v40, a164\n"
"v_mfma_f32_16x16x32_bf16 a[208:211], v[204:207], v[176:179], a[208:211]\n"
"buffer_store_dwordx4 v[180:183], v250, s[52:55], s77 offen offset:1024 sc1\n"
"v_mfma_f32_16x16x32_bf16 a[212:215], v[204:207], v[180:183], a[212:215]\n"
"v_accvgpr_read_b32 v41, a165\n"
"v_accvgpr_read_b32 v42, a166\n"
"v_mfma_f32_16x16x32_bf16 a[212:215], v[200:203], v[164:167], a[212:215]\n"
"v_accvgpr_read_b32 v43, a167\n"
"v_accvgpr_read_b32 v44, a180\n"
"v_mfma_f32_16x16x32_bf16 a[216:219], v[200:203], v[168:171], a[216:219]\n"
"v_accvgpr_read_b32 v45, a181\n"
"v_accvgpr_read_b32 v46, a182\n"
"v_mfma_f32_16x16x32_bf16 a[216:219], v[204:207], v[184:187], a[216:219]\n"
"v_accvgpr_read_b32 v47, a183\n"
"v_mul_f32 v40, v40, v245\n"
"v_mfma_f32_16x16x32_bf16 a[220:223], v[204:207], v[188:191], a[220:223]\n"
"ds_read_b64_tr_b16 v[204:205], v229 offset:35872\n"
"v_mul_f32 v41, v41, v245\n"
"v_mfma_f32_16x16x32_bf16 a[220:223], v[200:203], v[172:175], a[220:223]\n"
"s_waitcnt lgkmcnt(5)\n"
"ds_read_b64_tr_b16 v[206:207], v229 offset:38432\n"
"ds_read_b64_tr_b16 v[200:201], v229 offset:30752\n"
"v_mfma_f32_16x16x32_bf16 a[224:227], v[208:211], v[160:163], a[224:227]\n"
"ds_read_b64_tr_b16 v[202:203], v229 offset:33312\n"
"v_mul_f32 v42, v42, v245\n"
"v_mfma_f32_16x16x32_bf16 a[224:227], v[212:215], v[176:179], a[224:227]\n"
"v_mul_f32 v43, v43, v245\n"
"v_mul_f32 v44, v44, v245\n"
"v_mfma_f32_16x16x32_bf16 a[228:231], v[212:215], v[180:183], a[228:231]\n"
"v_mul_f32 v45, v45, v245\n"
"v_mul_f32 v46, v46, v245\n"
"v_mfma_f32_16x16x32_bf16 a[228:231], v[208:211], v[164:167], a[228:231]\n"
"v_mul_f32 v47, v47, v245\n"
"v_cvt_pk_bf16_f32 v20, v40, v41\n"
"v_cvt_pk_bf16_f32 v21, v42, v43\n"
"v_cvt_pk_bf16_f32 v22, v44, v45\n"
"v_cvt_pk_bf16_f32 v23, v46, v47\n"
"v_mfma_f32_16x16x32_bf16 a[232:235], v[208:211], v[168:171], a[232:235]\n"
"buffer_store_dwordx4 v[184:187], v250, s[52:55], s77 offen offset:2048 sc1\n"
"v_mfma_f32_16x16x32_bf16 a[232:235], v[212:215], v[184:187], a[232:235]\n"
"v_accvgpr_read_b32 v48, a168\n"
"v_accvgpr_read_b32 v49, a169\n"
"v_mfma_f32_16x16x32_bf16 a[236:239], v[212:215], v[188:191], a[236:239]\n"
"ds_read_b64_tr_b16 v[212:213], v229 offset:35904\n"
"v_accvgpr_read_b32 v50, a170\n"
"v_mfma_f32_16x16x32_bf16 a[236:239], v[208:211], v[172:175], a[236:239]\n"
"s_waitcnt lgkmcnt(5)\n"
"ds_read_b64_tr_b16 v[214:215], v229 offset:38464\n"
"ds_read_b64_tr_b16 v[208:209], v229 offset:30784\n"
"v_mfma_f32_16x16x32_bf16 a[240:243], v[192:195], v[160:163], a[240:243]\n"
"ds_read_b64_tr_b16 v[210:211], v229 offset:33344\n"
"v_accvgpr_read_b32 v51, a171\n"
"v_mfma_f32_16x16x32_bf16 a[240:243], v[196:199], v[176:179], a[240:243]\n"
"v_accvgpr_read_b32 v52, a184\n"
"v_accvgpr_read_b32 v53, a185\n"
"v_mfma_f32_16x16x32_bf16 a[244:247], v[196:199], v[180:183], a[244:247]\n"
"v_accvgpr_read_b32 v54, a186\n"
"v_accvgpr_read_b32 v55, a187\n"
"v_mfma_f32_16x16x32_bf16 a[244:247], v[192:195], v[164:167], a[244:247]\n"
"v_mul_f32 v48, v48, v247\n"
"v_mul_f32 v49, v49, v247\n"
"v_mfma_f32_16x16x32_bf16 a[248:251], v[192:195], v[168:171], a[248:251]\n"
"v_mul_f32 v50, v50, v247\n"
"v_mul_f32 v51, v51, v247\n"
"v_mfma_f32_16x16x32_bf16 a[248:251], v[196:199], v[184:187], a[248:251]\n"
"v_mul_f32 v52, v52, v247\n"
"v_mul_f32 v53, v53, v247\n"
"v_mfma_f32_16x16x32_bf16 a[252:255], v[196:199], v[188:191], a[252:255]\n"
"ds_read_b64_tr_b16 v[196:197], v229 offset:35936\n"
"v_mul_f32 v54, v54, v247\n"
"v_mfma_f32_16x16x32_bf16 a[252:255], v[192:195], v[172:175], a[252:255]\n"
"s_waitcnt lgkmcnt(5)\n"
"ds_read_b64_tr_b16 v[198:199], v229 offset:38496\n"
"ds_read_b64_tr_b16 v[192:193], v229 offset:30816\n"
"v_mul_f32 v55, v55, v247\n"
"v_cvt_pk_bf16_f32 v24, v48, v49\n"
"v_cvt_pk_bf16_f32 v25, v50, v51\n"
"v_cvt_pk_bf16_f32 v26, v52, v53\n"
"v_cvt_pk_bf16_f32 v27, v54, v55\n"
"v_mfma_f32_16x16x32_bf16 v[64:67], v[200:203], v[160:163], v[64:67]\n"
"ds_read_b64_tr_b16 v[194:195], v229 offset:33376\n"
"v_accvgpr_read_b32 v56, a172\n"
"v_mfma_f32_16x16x32_bf16 v[64:67], v[204:207], v[176:179], v[64:67]\n"
"buffer_store_dwordx4 v[188:191], v250, s[52:55], s77 offen offset:3072 sc1\n"
"v_mfma_f32_16x16x32_bf16 v[68:71], v[204:207], v[180:183], v[68:71]\n"
"v_accvgpr_read_b32 v57, a173\n"
"v_accvgpr_read_b32 v58, a174\n"
"v_mfma_f32_16x16x32_bf16 v[68:71], v[200:203], v[164:167], v[68:71]\n"
"v_accvgpr_read_b32 v59, a175\n"
"v_accvgpr_read_b32 v60, a188\n"
"v_mfma_f32_16x16x32_bf16 v[72:75], v[200:203], v[168:171], v[72:75]\n"
"v_accvgpr_read_b32 v61, a189\n"
"v_accvgpr_read_b32 v62, a190\n"
"v_mfma_f32_16x16x32_bf16 v[72:75], v[204:207], v[184:187], v[72:75]\n"
"v_accvgpr_read_b32 v63, a191\n"
"v_mul_f32 v56, v56, v249\n"
"v_mfma_f32_16x16x32_bf16 v[76:79], v[204:207], v[188:191], v[76:79]\n"
"ds_read_b64_tr_b16 v[204:205], v229 offset:35968\n"
"v_mul_f32 v57, v57, v249\n"
"v_mfma_f32_16x16x32_bf16 v[76:79], v[200:203], v[172:175], v[76:79]\n"
"s_waitcnt lgkmcnt(5)\n"
"ds_read_b64_tr_b16 v[206:207], v229 offset:38528\n"
"ds_read_b64_tr_b16 v[200:201], v229 offset:30848\n"
"v_mfma_f32_16x16x32_bf16 v[80:83], v[208:211], v[160:163], v[80:83]\n"
"ds_read_b64_tr_b16 v[202:203], v229 offset:33408\n"
"v_mul_f32 v58, v58, v249\n"
"v_mfma_f32_16x16x32_bf16 v[80:83], v[212:215], v[176:179], v[80:83]\n"
"s_waitcnt vmcnt(4)\n"
"v_cmp_ne_u32 vcc, v255, v253\n"
"v_mul_f32 v59, v59, v249\n"
"v_mfma_f32_16x16x32_bf16 v[84:87], v[212:215], v[180:183], v[84:87]\n"
"v_mul_f32 v60, v60, v249\n"
"v_mul_f32 v61, v61, v249\n"
"v_mfma_f32_16x16x32_bf16 v[84:87], v[208:211], v[164:167], v[84:87]\n"
"s_lshr_b32 s66, vcc_lo, 0\n"
"v_mul_f32 v62, v62, v249\n"
"v_mul_f32 v63, v63, v249\n"
"v_mfma_f32_16x16x32_bf16 v[88:91], v[208:211], v[168:171], v[88:91]\n"
"v_cvt_pk_bf16_f32 v28, v56, v57\n"
"v_cvt_pk_bf16_f32 v29, v58, v59\n"
"v_cvt_pk_bf16_f32 v30, v60, v61\n"
"v_cvt_pk_bf16_f32 v31, v62, v63\n"
"v_mfma_f32_16x16x32_bf16 v[88:91], v[212:215], v[184:187], v[88:91]\n"
"s_bitcmp1_b32 s66, s95\n"
"s_cbranch_scc1 Lspx1%=\n"
"Lrtx1%=:\n"
"v_add_u32 v228, 0xf000, v227\n"
"v_mfma_f32_16x16x32_bf16 v[92:95], v[212:215], v[188:191], v[92:95]\n"
"ds_read_b64_tr_b16 v[212:213], v229 offset:46080\n"
"buffer_store_dwordx4 v[0:3], v250, s[52:55], s78 offen offset:0 sc1\n"
"v_mfma_f32_16x16x32_bf16 v[92:95], v[208:211], v[172:175], v[92:95]\n"
"s_waitcnt lgkmcnt(5)\n"
"ds_read_b64_tr_b16 v[214:215], v229 offset:48640\n"
"ds_read_b64_tr_b16 v[208:209], v229 offset:40960\n"
"v_mfma_f32_16x16x32_bf16 v[96:99], v[192:195], v[160:163], v[96:99]\n"
"ds_read_b64_tr_b16 v[210:211], v229 offset:43520\n"
"v_and_b32 v220, 0x3f0, v250\n"
"v_mfma_f32_16x16x32_bf16 v[96:99], v[196:199], v[176:179], v[96:99]\n"
"s_mul_i32 s57, s95, 0x18000\n"
"buffer_load_dwordx4 a[32:35], v250, s[52:55], s57 offen offset:0 sc1\n"
"v_mfma_f32_16x16x32_bf16 v[100:103], v[196:199], v[180:183], v[100:103]\n"
"buffer_load_dwordx4 a[36:39], v250, s[52:55], s57 offen offset:1024 sc1\n"
"v_mfma_f32_16x16x32_bf16 v[100:103], v[192:195], v[164:167], v[100:103]\n"
"v_mfma_f32_16x16x32_bf16 v[104:107], v[192:195], v[168:171], v[104:107]\n"
"buffer_load_dwordx4 a[40:43], v250, s[52:55], s57 offen offset:2048 sc1\n"
"v_mfma_f32_16x16x32_bf16 v[104:107], v[196:199], v[184:187], v[104:107]\n"
"v_mfma_f32_16x16x32_bf16 v[108:111], v[196:199], v[188:191], v[108:111]\n"
"ds_read_b64_tr_b16 v[196:197], v229 offset:46112\n"
"v_mfma_f32_16x16x32_bf16 v[108:111], v[192:195], v[172:175], v[108:111]\n"
"s_waitcnt lgkmcnt(5)\n"
"ds_read_b64_tr_b16 v[198:199], v229 offset:48672\n"
"ds_read_b64_tr_b16 v[192:193], v229 offset:40992\n"
"v_mfma_f32_16x16x32_bf16 v[112:115], v[200:203], v[160:163], v[112:115]\n"
"ds_read_b64_tr_b16 v[194:195], v229 offset:43552\n"
"v_mfma_f32_16x16x32_bf16 v[112:115], v[204:207], v[176:179], v[112:115]\n"
"buffer_load_dwordx4 a[44:47], v250, s[52:55], s57 offen offset:3072 sc1\n"
"v_mfma_f32_16x16x32_bf16 v[116:119], v[204:207], v[180:183], v[116:119]\n"
"s_add_u32 s57, s57, 0x4000\n"
"buffer_load_dwordx4 a[48:51], v250, s[52:55], s57 offen offset:0 sc1\n"
"buffer_store_dwordx4 v[4:7], v250, s[52:55], s78 offen offset:1024 sc1\n"
"v_mfma_f32_16x16x32_bf16 v[116:119], v[200:203], v[164:167], v[116:119]\n"
"v_mfma_f32_16x16x32_bf16 v[120:123], v[200:203], v[168:171], v[120:123]\n"
"v_mfma_f32_16x16x32_bf16 v[120:123], v[204:207], v[184:187], v[120:123]\n"
"buffer_load_dwordx4 a[52:55], v250, s[52:55], s57 offen offset:1024 sc1\n"
"v_mfma_f32_16x16x32_bf16 v[124:127], v[204:207], v[188:191], v[124:127]\n"
"ds_read_b64_tr_b16 v[204:205], v229 offset:46144\n"
"v_mfma_f32_16x16x32_bf16 v[124:127], v[200:203], v[172:175], v[124:127]\n"
"s_waitcnt lgkmcnt(5)\n"
"ds_read_b64_tr_b16 v[206:207], v229 offset:48704\n"
"ds_read_b64_tr_b16 v[200:201], v229 offset:41024\n"
"v_mfma_f32_16x16x32_bf16 a[0:3], v[208:211], v[0:3], a[0:3]\n"
"ds_read_b64_tr_b16 v[202:203], v229 offset:43584\n"
"v_mfma_f32_16x16x32_bf16 a[0:3], v[212:215], v[16:19], a[0:3]\n"
"buffer_load_dwordx4 a[56:59], v250, s[52:55], s57 offen offset:2048 sc1\n"
"v_mfma_f32_16x16x32_bf16 a[4:7], v[212:215], v[20:23], a[4:7]\n"
"v_mfma_f32_16x16x32_bf16 a[4:7], v[208:211], v[4:7], a[4:7]\n"
"buffer_load_dwordx4 a[60:63], v250, s[52:55], s57 offen offset:3072 sc1\n"
"v_mfma_f32_16x16x32_bf16 a[8:11], v[208:211], v[8:11], a[8:11]\n"
"buffer_store_dwordx4 v[8:11], v250, s[52:55], s78 offen offset:2048 sc1\n"
"v_mfma_f32_16x16x32_bf16 a[8:11], v[212:215], v[24:27], a[8:11]\n"
"v_mfma_f32_16x16x32_bf16 a[12:15], v[212:215], v[28:31], a[12:15]\n"
"ds_read_b64_tr_b16 v[212:213], v229 offset:46176\n"
"v_mfma_f32_16x16x32_bf16 a[12:15], v[208:211], v[12:15], a[12:15]\n"
"s_waitcnt lgkmcnt(5)\n"
"ds_read_b64_tr_b16 v[214:215], v229 offset:48736\n"
"ds_read_b64_tr_b16 v[208:209], v229 offset:41056\n"
"v_mfma_f32_16x16x32_bf16 a[16:19], v[192:195], v[0:3], a[16:19]\n"
"ds_read_b64_tr_b16 v[210:211], v229 offset:43616\n"
"v_mfma_f32_16x16x32_bf16 a[16:19], v[196:199], v[16:19], a[16:19]\n"
"v_mfma_f32_16x16x32_bf16 a[20:23], v[196:199], v[20:23], a[20:23]\n"
"v_mfma_f32_16x16x32_bf16 a[20:23], v[192:195], v[4:7], a[20:23]\n"
"v_mfma_f32_16x16x32_bf16 a[24:27], v[192:195], v[8:11], a[24:27]\n"
"v_mfma_f32_16x16x32_bf16 a[24:27], v[196:199], v[24:27], a[24:27]\n"
"v_mfma_f32_16x16x32_bf16 a[28:31], v[196:199], v[28:31], a[28:31]\n"
"ds_read_b64_tr_b16 v[196:197], v229 offset:46208\n"
"v_mfma_f32_16x16x32_bf16 a[28:31], v[192:195], v[12:15], a[28:31]\n"
"s_waitcnt lgkmcnt(5)\n"
"ds_read_b64_tr_b16 v[198:199], v229 offset:48768\n"
"ds_read_b64_tr_b16 v[192:193], v229 offset:41088\n"
"v_mfma_f32_16x16x32_bf16 a[192:195], v[200:203], v[0:3], a[192:195]\n"
"ds_read_b64_tr_b16 v[194:195], v229 offset:43648\n"
"v_mfma_f32_16x16x32_bf16 a[192:195], v[204:207], v[16:19], a[192:195]\n"
"buffer_store_dwordx4 v[12:15], v250, s[52:55], s78 offen offset:3072 sc1\n"
"v_mfma_f32_16x16x32_bf16 a[196:199], v[204:207], v[20:23], a[196:199]\n"
"v_mfma_f32_16x16x32_bf16 a[196:199], v[200:203], v[4:7], a[196:199]\n"
"v_mfma_f32_16x16x32_bf16 a[200:203], v[200:203], v[8:11], a[200:203]\n"
"v_mfma_f32_16x16x32_bf16 a[200:203], v[204:207], v[24:27], a[200:203]\n"
"v_mfma_f32_16x16x32_bf16 a[204:207], v[204:207], v[28:31], a[204:207]\n"
"ds_read_b64_tr_b16 v[204:205], v229 offset:56320\n"
"v_mfma_f32_16x16x32_bf16 a[204:207], v[200:203], v[12:15], a[204:207]\n"
"s_waitcnt lgkmcnt(5)\n"
"ds_read_b64_tr_b16 v[206:207], v229 offset:58880\n"
"ds_read_b64_tr_b16 v[200:201], v229 offset:51200\n"
"v_mfma_f32_16x16x32_bf16 a[208:211], v[208:211], v[0:3], a[208:211]\n"
"ds_read_b64_tr_b16 v[202:203], v229 offset:53760\n"
"v_mfma_f32_16x16x32_bf16 a[208:211], v[212:215], v[16:19], a[208:211]\n"
"v_mfma_f32_16x16x32_bf16 a[212:215], v[212:215], v[20:23], a[212:215]\n"
"v_mfma_f32_16x16x32_bf16 a[212:215], v[208:211], v[4:7], a[212:215]\n"
"buffer_store_dwordx4 v[16:19], v250, s[52:55], s79 offen offset:0 sc1\n"
"v_mfma_f32_16x16x32_bf16 a[216:219], v[208:211], v[8:11], a[216:219]\n"
"v_mfma_f32_16x16x32_bf16 a[216:219], v[212:215], v[24:27], a[216:219]\n"
"v_mfma_f32_16x16x32_bf16 a[220:223], v[212:215], v[28:31], a[220:223]\n"
"ds_read_b64_tr_b16 v[212:213], v229 offset:56352\n"
"v_mfma_f32_16x16x32_bf16 a[220:223], v[208:211], v[12:15], a[220:223]\n"
"s_waitcnt lgkmcnt(5)\n"
"ds_read_b64_tr_b16 v[214:215], v229 offset:58912\n"
"ds_read_b64_tr_b16 v[208:209], v229 offset:51232\n"
"v_mfma_f32_16x16x32_bf16 a[224:227], v[192:195], v[0:3], a[224:227]\n"
"ds_read_b64_tr_b16 v[210:211], v229 offset:53792\n"
"v_mfma_f32_16x16x32_bf16 a[224:227], v[196:199], v[16:19], a[224:227]\n"
"v_mfma_f32_16x16x32_bf16 a[228:231], v[196:199], v[20:23], a[228:231]\n"
"v_mfma_f32_16x16x32_bf16 a[228:231], v[192:195], v[4:7], a[228:231]\n"
"v_mfma_f32_16x16x32_bf16 a[232:235], v[192:195], v[8:11], a[232:235]\n"
"v_mfma_f32_16x16x32_bf16 a[232:235], v[196:199], v[24:27], a[232:235]\n"
"v_mfma_f32_16x16x32_bf16 a[236:239], v[196:199], v[28:31], a[236:239]\n"
"ds_read_b64_tr_b16 v[196:197], v229 offset:56384\n"
"v_mfma_f32_16x16x32_bf16 a[236:239], v[192:195], v[12:15], a[236:239]\n"
"s_waitcnt lgkmcnt(5)\n"
"ds_read_b64_tr_b16 v[198:199], v229 offset:58944\n"
"ds_read_b64_tr_b16 v[192:193], v229 offset:51264\n"
"v_mfma_f32_16x16x32_bf16 a[240:243], v[200:203], v[0:3], a[240:243]\n"
"ds_read_b64_tr_b16 v[194:195], v229 offset:53824\n"
"v_mfma_f32_16x16x32_bf16 a[240:243], v[204:207], v[16:19], a[240:243]\n"
"buffer_store_dwordx4 v[20:23], v250, s[52:55], s79 offen offset:1024 sc1\n"
"v_mfma_f32_16x16x32_bf16 a[244:247], v[204:207], v[20:23], a[244:247]\n"
"v_mfma_f32_16x16x32_bf16 a[244:247], v[200:203], v[4:7], a[244:247]\n"
"v_mfma_f32_16x16x32_bf16 a[248:251], v[200:203], v[8:11], a[248:251]\n"
"v_mfma_f32_16x16x32_bf16 a[248:251], v[204:207], v[24:27], a[248:251]\n"
"v_mfma_f32_16x16x32_bf16 a[252:255], v[204:207], v[28:31], a[252:255]\n"
"ds_read_b64_tr_b16 v[204:205], v229 offset:56416\n"
"v_mfma_f32_16x16x32_bf16 a[252:255], v[200:203], v[12:15], a[252:255]\n"
"s_waitcnt lgkmcnt(5)\n"
"ds_read_b64_tr_b16 v[206:207], v229 offset:58976\n"
"ds_read_b64_tr_b16 v[200:201], v229 offset:51296\n"
"v_mfma_f32_16x16x32_bf16 v[64:67], v[208:211], v[0:3], v[64:67]\n"
"ds_read_b64_tr_b16 v[202:203], v229 offset:53856\n"
"v_mfma_f32_16x16x32_bf16 v[64:67], v[212:215], v[16:19], v[64:67]\n"
"v_mfma_f32_16x16x32_bf16 v[68:71], v[212:215], v[20:23], v[68:71]\n"
"v_mfma_f32_16x16x32_bf16 v[68:71], v[208:211], v[4:7], v[68:71]\n"
"buffer_store_dwordx4 v[24:27], v250, s[52:55], s79 offen offset:2048 sc1\n"
"v_mfma_f32_16x16x32_bf16 v[72:75], v[208:211], v[8:11], v[72:75]\n"
"v_mfma_f32_16x16x32_bf16 v[72:75], v[212:215], v[24:27], v[72:75]\n"
"v_mfma_f32_16x16x32_bf16 v[76:79], v[212:215], v[28:31], v[76:79]\n"
"ds_read_b64_tr_b16 v[212:213], v229 offset:56448\n"
"v_mfma_f32_16x16x32_bf16 v[76:79], v[208:211], v[12:15], v[76:79]\n"
"s_waitcnt lgkmcnt(5)\n"
"ds_read_b64_tr_b16 v[214:215], v229 offset:59008\n"
"ds_read_b64_tr_b16 v[208:209], v229 offset:51328\n"
"v_mfma_f32_16x16x32_bf16 v[80:83], v[192:195], v[0:3], v[80:83]\n"
"ds_read_b64_tr_b16 v[210:211], v229 offset:53888\n"
"v_mfma_f32_16x16x32_bf16 v[80:83], v[196:199], v[16:19], v[80:83]\n"
"v_mfma_f32_16x16x32_bf16 v[84:87], v[196:199], v[20:23], v[84:87]\n"
"v_mfma_f32_16x16x32_bf16 v[84:87], v[192:195], v[4:7], v[84:87]\n"
"v_mfma_f32_16x16x32_bf16 v[88:91], v[192:195], v[8:11], v[88:91]\n"
"v_mfma_f32_16x16x32_bf16 v[88:91], v[196:199], v[24:27], v[88:91]\n"
"v_mfma_f32_16x16x32_bf16 v[92:95], v[196:199], v[28:31], v[92:95]\n"
"ds_read_b64_tr_b16 v[196:197], v227 offset:5120\n"
"v_mfma_f32_16x16x32_bf16 v[92:95], v[192:195], v[12:15], v[92:95]\n"
"s_waitcnt lgkmcnt(5)\n"
"ds_read_b64_tr_b16 v[198:199], v227 offset:7680\n"
"ds_read_b64_tr_b16 v[192:193], v227 offset:0\n"
"v_mfma_f32_16x16x32_bf16 v[96:99], v[200:203], v[0:3], v[96:99]\n"
"ds_read_b64_tr_b16 v[194:195], v227 offset:2560\n"
"v_mfma_f32_16x16x32_bf16 v[96:99], v[204:207], v[16:19], v[96:99]\n"
"buffer_store_dwordx4 v[28:31], v250, s[52:55], s79 offen offset:3072 sc1\n"
"v_mfma_f32_16x16x32_bf16 v[100:103], v[204:207], v[20:23], v[100:103]\n"
"v_mfma_f32_16x16x32_bf16 v[100:103], v[200:203], v[4:7], v[100:103]\n"
"v_mfma_f32_16x16x32_bf16 v[104:107], v[200:203], v[8:11], v[104:107]\n"
"v_mfma_f32_16x16x32_bf16 v[104:107], v[204:207], v[24:27], v[104:107]\n"
"v_mfma_f32_16x16x32_bf16 v[108:111], v[204:207], v[28:31], v[108:111]\n"
"ds_read_b64_tr_b16 v[204:205], v227 offset:5152\n"
"v_mfma_f32_16x16x32_bf16 v[108:111], v[200:203], v[12:15], v[108:111]\n"
"s_waitcnt lgkmcnt(5)\n"
"ds_read_b64_tr_b16 v[206:207], v227 offset:7712\n"
"ds_read_b64_tr_b16 v[200:201], v227 offset:32\n"
"v_mfma_f32_16x16x32_bf16 v[112:115], v[208:211], v[0:3], v[112:115]\n"
"ds_read_b64_tr_b16 v[202:203], v227 offset:2592\n"
"v_mfma_f32_16x16x32_bf16 v[112:115], v[212:215], v[16:19], v[112:115]\n"
"v_mfma_f32_16x16x32_bf16 v[116:119], v[212:215], v[20:23], v[116:119]\n"
"v_mfma_f32_16x16x32_bf16 v[116:119], v[208:211], v[4:7], v[116:119]\n"
"v_mfma_f32_16x16x32_bf16 v[120:123], v[208:211], v[8:11], v[120:123]\n"
"v_mfma_f32_16x16x32_bf16 v[120:123], v[212:215], v[24:27], v[120:123]\n"
"s_waitcnt vmcnt(6)\n"
"v_mfma_f32_16x16x32_bf16 v[124:127], v[212:215], v[28:31], v[124:127]\n"
"ds_read_b64_tr_b16 v[212:213], v227 offset:5184\n"
"v_mfma_f32_16x16x32_bf16 v[124:127], v[208:211], v[12:15], v[124:127]\n"
"s_waitcnt lgkmcnt(5)\n"
"ds_read_b64_tr_b16 v[214:215], v227 offset:7744\n"
"ds_read_b64_tr_b16 v[208:209], v227 offset:64\n"
"v_mfma_f32_16x16x32_bf16 a[0:3], v[192:195], a[32:35], a[0:3]\n"
"ds_read_b64_tr_b16 v[210:211], v227 offset:2624\n"
"v_mfma_f32_16x16x32_bf16 a[0:3], v[196:199], a[48:51], a[0:3]\n"
"v_mfma_f32_16x16x32_bf16 a[4:7], v[196:199], a[52:55], a[4:7]\n"
"v_mfma_f32_16x16x32_bf16 a[4:7], v[192:195], a[36:39], a[4:7]\n"
"v_mfma_f32_16x16x32_bf16 a[8:11], v[192:195], a[40:43], a[8:11]\n"
"v_mfma_f32_16x16x32_bf16 a[8:11], v[196:199], a[56:59], a[8:11]\n"
"v_mfma_f32_16x16x32_bf16 a[12:15], v[196:199], a[60:63], a[12:15]\n"
"ds_read_b64_tr_b16 v[196:197], v227 offset:5216\n"
"v_mfma_f32_16x16x32_bf16 a[12:15], v[192:195], a[44:47], a[12:15]\n"
"s_waitcnt lgkmcnt(5)\n"
"ds_read_b64_tr_b16 v[198:199], v227 offset:7776\n"
"ds_read_b64_tr_b16 v[192:193], v227 offset:96\n"
"v_mfma_f32_16x16x32_bf16 a[16:19], v[200:203], a[32:35], a[16:19]\n"
"ds_read_b64_tr_b16 v[194:195], v227 offset:2656\n"
"v_mfma_f32_16x16x32_bf16 a[16:19], v[204:207], a[48:51], a[16:19]\n"
"v_mfma_f32_16x16x32_bf16 a[20:23], v[204:207], a[52:55], a[20:23]\n"
"v_mfma_f32_16x16x32_bf16 a[20:23], v[200:203], a[36:39], a[20:23]\n"
"v_mfma_f32_16x16x32_bf16 a[24:27], v[200:203], a[40:43], a[24:27]\n"
"v_mfma_f32_16x16x32_bf16 a[24:27], v[204:207], a[56:59], a[24:27]\n"
"v_mfma_f32_16x16x32_bf16 a[28:31], v[204:207], a[60:63], a[28:31]\n"
"s_waitcnt vmcnt(0) lgkmcnt(0)\n"
"s_barrier\n"
"ds_read_b64_tr_b16 v[204:205], v227 offset:5248\n"
"buffer_store_dword v253, v239, s[96:99], s82 offen offset:16 sc1\n"
"v_mfma_f32_16x16x32_bf16 a[28:31], v[200:203], a[44:47], a[28:31]\n"
"ds_read_b64_tr_b16 v[206:207], v227 offset:7808\n"
"ds_read_b64_tr_b16 v[200:201], v227 offset:128\n"
"v_mfma_f32_16x16x32_bf16 a[192:195], v[208:211], a[32:35], a[192:195]\n"
"ds_read_b64_tr_b16 v[202:203], v227 offset:2688\n"
"v_mfma_f32_16x16x32_bf16 a[192:195], v[212:215], a[48:51], a[192:195]\n"
"s_bitcmp1_b32 s66, s67\n"
"s_cbranch_scc1 Lspx2%=\n"
"Lrtx2%=:\n"
"v_mfma_f32_16x16x32_bf16 a[196:199], v[212:215], a[52:55], a[196:199]\n"
"s_mul_i32 s57, s67, 0x18000\n"
"buffer_load_dwordx4 a[64:67], v250, s[52:55], s57 offen offset:0 sc1\n"
"v_mfma_f32_16x16x32_bf16 a[196:199], v[208:211], a[36:39], a[196:199]\n"
"v_mfma_f32_16x16x32_bf16 a[200:203], v[208:211], a[40:43], a[200:203]\n"
"v_mfma_f32_16x16x32_bf16 a[200:203], v[212:215], a[56:59], a[200:203]\n"
"buffer_load_dwordx4 a[68:71], v250, s[52:55], s57 offen offset:1024 sc1\n"
"v_mfma_f32_16x16x32_bf16 a[204:207], v[212:215], a[60:63], a[204:207]\n"
"ds_read_b64_tr_b16 v[212:213], v227 offset:15360\n"
"v_mfma_f32_16x16x32_bf16 a[204:207], v[208:211], a[44:47], a[204:207]\n"
"ds_read_b64_tr_b16 v[214:215], v227 offset:17920\n"
"ds_read_b64_tr_b16 v[208:209], v227 offset:10240\n"
"v_mfma_f32_16x16x32_bf16 a[208:211], v[192:195], a[32:35], a[208:211]\n"
"ds_read_b64_tr_b16 v[210:211], v227 offset:12800\n"
"v_mfma_f32_16x16x32_bf16 a[208:211], v[196:199], a[48:51], a[208:211]\n"
"buffer_load_dwordx4 a[72:75], v250, s[52:55], s57 offen offset:2048 sc1\n"
"v_mfma_f32_16x16x32_bf16 a[212:215], v[196:199], a[52:55], a[212:215]\n"
"v_mfma_f32_16x16x32_bf16 a[212:215], v[192:195], a[36:39], a[212:215]\n"
"buffer_load_dwordx4 a[76:79], v250, s[52:55], s57 offen offset:3072 sc1\n"
"v_mfma_f32_16x16x32_bf16 a[216:219], v[192:195], a[40:43], a[216:219]\n"
"v_mfma_f32_16x16x32_bf16 a[216:219], v[196:199], a[56:59], a[216:219]\n"
"v_mfma_f32_16x16x32_bf16 a[220:223], v[196:199], a[60:63], a[220:223]\n"
"ds_read_b64_tr_b16 v[196:197], v227 offset:15392\n"
"v_mfma_f32_16x16x32_bf16 a[220:223], v[192:195], a[44:47], a[220:223]\n"
"s_waitcnt lgkmcnt(5)\n"
"ds_read_b64_tr_b16 v[198:199], v227 offset:17952\n"
"ds_read_b64_tr_b16 v[192:193], v227 offset:10272\n"
"v_mfma_f32_16x16x32_bf16 a[224:227], v[200:203], a[32:35], a[224:227]\n"
"ds_read_b64_tr_b16 v[194:195], v227 offset:12832\n"
"v_mfma_f32_16x16x32_bf16 a[224:227], v[204:207], a[48:51], a[224:227]\n"
"s_add_u32 s57, s57, 0x4000\n"
"buffer_load_dwordx4 a[80:83], v250, s[52:55], s57 offen offset:0 sc1\n"
"v_mfma_f32_16x16x32_bf16 a[228:231], v[204:207], a[52:55], a[228:231]\n"
"buffer_load_dwordx4 a[84:87], v250, s[52:55], s57 offen offset:1024 sc1\n"
"v_mfma_f32_16x16x32_bf16 a[228:231], v[200:203], a[36:39], a[228:231]\n"
"v_mfma_f32_16x16x32_bf16 a[232:235], v[200:203], a[40:43], a[232:235]\n"
"buffer_load_dwordx4 a[88:91], v250, s[52:55], s57 offen offset:2048 sc1\n"
"v_mfma_f32_16x16x32_bf16 a[232:235], v[204:207], a[56:59], a[232:235]\n"
"v_mfma_f32_16x16x32_bf16 a[236:239], v[204:207], a[60:63], a[236:239]\n"
"ds_read_b64_tr_b16 v[204:205], v227 offset:15424\n"
"v_mfma_f32_16x16x32_bf16 a[236:239], v[200:203], a[44:47], a[236:239]\n"
"s_waitcnt lgkmcnt(5)\n"
"ds_read_b64_tr_b16 v[206:207], v227 offset:17984\n"
"ds_read_b64_tr_b16 v[200:201], v227 offset:10304\n"
"v_mfma_f32_16x16x32_bf16 a[240:243], v[208:211], a[32:35], a[240:243]\n"
"ds_read_b64_tr_b16 v[202:203], v227 offset:12864\n"
"v_mfma_f32_16x16x32_bf16 a[240:243], v[212:215], a[48:51], a[240:243]\n"
"buffer_load_dwordx4 a[92:95], v250, s[52:55], s57 offen offset:3072 sc1\n"
"v_mfma_f32_16x16x32_bf16 a[244:247], v[212:215], a[52:55], a[244:247]\n"
"v_mfma_f32_16x16x32_bf16 a[244:247], v[208:211], a[36:39], a[244:247]\n"
"v_mfma_f32_16x16x32_bf16 a[248:251], v[208:211], a[40:43], a[248:251]\n"
"v_mfma_f32_16x16x32_bf16 a[248:251], v[212:215], a[56:59], a[248:251]\n"
"s_bitcmp1_b32 s66, s69\n"
"s_cbranch_scc1 Lspx3%=\n"
"Lrtx3%=:\n"
"v_mfma_f32_16x16x32_bf16 a[252:255], v[212:215], a[60:63], a[252:255]\n"
"ds_read_b64_tr_b16 v[212:213], v227 offset:15456\n"
"v_mfma_f32_16x16x32_bf16 a[252:255], v[208:211], a[44:47], a[252:255]\n"
"s_waitcnt lgkmcnt(5)\n"
"ds_read_b64_tr_b16 v[214:215], v227 offset:18016\n"
"ds_read_b64_tr_b16 v[208:209], v227 offset:10336\n"
"v_mfma_f32_16x16x32_bf16 v[64:67], v[192:195], a[32:35], v[64:67]\n"
"ds_read_b64_tr_b16 v[210:211], v227 offset:12896\n"
"v_mfma_f32_16x16x32_bf16 v[64:67], v[196:199], a[48:51], v[64:67]\n"
"s_mul_i32 s57, s69, 0x18000\n"
"buffer_load_dwordx4 a[96:99], v250, s[52:55], s57 offen offset:0 sc1\n"
"v_mfma_f32_16x16x32_bf16 v[68:71], v[196:199], a[52:55], v[68:71]\n"
"buffer_load_dwordx4 a[100:103], v250, s[52:55], s57 offen offset:1024 sc1\n"
"v_mfma_f32_16x16x32_bf16 v[68:71], v[192:195], a[36:39], v[68:71]\n"
"v_mfma_f32_16x16x32_bf16 v[72:75], v[192:195], a[40:43], v[72:75]\n"
"buffer_load_dwordx4 a[104:107], v250, s[52:55], s57 offen offset:2048 sc1\n"
"v_mfma_f32_16x16x32_bf16 v[72:75], v[196:199], a[56:59], v[72:75]\n"
"v_mfma_f32_16x16x32_bf16 v[76:79], v[196:199], a[60:63], v[76:79]\n"
"ds_read_b64_tr_b16 v[196:197], v227 offset:15488\n"
"v_mfma_f32_16x16x32_bf16 v[76:79], v[192:195], a[44:47], v[76:79]\n"
"s_waitcnt lgkmcnt(5)\n"
"ds_read_b64_tr_b16 v[198:199], v227 offset:18048\n"
"ds_read_b64_tr_b16 v[192:193], v227 offset:10368\n"
"v_mfma_f32_16x16x32_bf16 v[80:83], v[200:203], a[32:35], v[80:83]\n"
"ds_read_b64_tr_b16 v[194:195], v227 offset:12928\n"
"v_mfma_f32_16x16x32_bf16 v[80:83], v[204:207], a[48:51], v[80:83]\n"
"buffer_load_dwordx4 a[108:111], v250, s[52:55], s57 offen offset:3072 sc1\n"
"v_mfma_f32_16x16x32_bf16 v[84:87], v[204:207], a[52:55], v[84:87]\n"
"s_add_u32 s57, s57, 0x4000\n"
"buffer_load_dwordx4 a[112:115], v250, s[52:55], s57 offen offset:0 sc1\n"
"v_mfma_f32_16x16x32_bf16 v[84:87], v[200:203], a[36:39], v[84:87]\n"
"v_mfma_f32_16x16x32_bf16 v[88:91], v[200:203], a[40:43], v[88:91]\n"
"v_mfma_f32_16x16x32_bf16 v[88:91], v[204:207], a[56:59], v[88:91]\n"
"buffer_load_dwordx4 a[116:119], v250, s[52:55], s57 offen offset:1024 sc1\n"
"v_mfma_f32_16x16x32_bf16 v[92:95], v[204:207], a[60:63], v[92:95]\n"
"ds_read_b64_tr_b16 v[204:205], v227 offset:25600\n"
"v_mfma_f32_16x16x32_bf16 v[92:95], v[200:203], a[44:47], v[92:95]\n"
"s_waitcnt lgkmcnt(5)\n"
"ds_read_b64_tr_b16 v[206:207], v227 offset:28160\n"
"ds_read_b64_tr_b16 v[200:201], v227 offset:20480\n"
"v_mfma_f32_16x16x32_bf16 v[96:99], v[208:211], a[32:35], v[96:99]\n"
"ds_read_b64_tr_b16 v[202:203], v227 offset:23040\n"
"v_mfma_f32_16x16x32_bf16 v[96:99], v[212:215], a[48:51], v[96:99]\n"
"buffer_load_dwordx4 a[120:123], v250, s[52:55], s57 offen offset:2048 sc1\n"
"v_mfma_f32_16x16x32_bf16 v[100:103], v[212:215], a[52:55], v[100:103]\n"
"v_mfma_f32_16x16x32_bf16 v[100:103], v[208:211], a[36:39], v[100:103]\n"
"buffer_load_dwordx4 a[124:127], v250, s[52:55], s57 offen offset:3072 sc1\n"
"v_mfma_f32_16x16x32_bf16 v[104:107], v[208:211], a[40:43], v[104:107]\n"
"v_mfma_f32_16x16x32_bf16 v[104:107], v[212:215], a[56:59], v[104:107]\n"
"v_mfma_f32_16x16x32_bf16 v[108:111], v[212:215], a[60:63], v[108:111]\n"
"ds_read_b64_tr_b16 v[212:213], v227 offset:25632\n"
"v_mfma_f32_16x16x32_bf16 v[108:111], v[208:211], a[44:47], v[108:111]\n"
"s_waitcnt lgkmcnt(5)\n"
"ds_read_b64_tr_b16 v[214:215], v227 offset:28192\n"
"ds_read_b64_tr_b16 v[208:209], v227 offset:20512\n"
"v_mfma_f32_16x16x32_bf16 v[112:115], v[192:195], a[32:35], v[112:115]\n"
"s_mul_i32 s58, s67, 0x3c000\n"
"s_add_u32 m0, s87, 0x18000\n"
"ds_read_b64_tr_b16 v[210:211], v227 offset:23072\n"
"v_mfma_f32_16x16x32_bf16 v[112:115], v[196:199], a[48:51], v[112:115]\n"
"s_add_u32 s58, s58, 0x14000\n"
"buffer_load_dwordx4 v222, s[40:43], s58 offen offset:0 lds\n"
"v_mfma_f32_16x16x32_bf16 v[116:119], v[196:199], a[52:55], v[116:119]\n"
"buffer_load_dwordx4 v223, s[40:43], s58 offen offset:1024 lds\n"
"v_mfma_f32_16x16x32_bf16 v[116:119], v[192:195], a[36:39], v[116:119]\n"
"v_mfma_f32_16x16x32_bf16 v[120:123], v[192:195], a[40:43], v[120:123]\n"
"v_mfma_f32_16x16x32_bf16 v[120:123], v[196:199], a[56:59], v[120:123]\n"
"s_waitcnt vmcnt(10)\n"
"v_mfma_f32_16x16x32_bf16 v[124:127], v[196:199], a[60:63], v[124:127]\n"
"ds_read_b64_tr_b16 v[196:197], v227 offset:25664\n"
"v_mfma_f32_16x16x32_bf16 v[124:127], v[192:195], a[44:47], v[124:127]\n"
"s_waitcnt lgkmcnt(5)\n"
"ds_read_b64_tr_b16 v[198:199], v227 offset:28224\n"
"ds_read_b64_tr_b16 v[192:193], v227 offset:20544\n"
"v_mfma_f32_16x16x32_bf16 a[0:3], v[200:203], a[64:67], a[0:3]\n"
"ds_read_b64_tr_b16 v[194:195], v227 offset:23104\n"
"v_mfma_f32_16x16x32_bf16 a[0:3], v[204:207], a[80:83], a[0:3]\n"
"buffer_load_dwordx4 v224, s[40:43], s58 offen offset:2048 lds\n"
"v_mfma_f32_16x16x32_bf16 a[4:7], v[204:207], a[84:87], a[4:7]\n"
"buffer_load_dwordx4 v225, s[40:43], s58 offen offset:3072 lds\n"
"v_mfma_f32_16x16x32_bf16 a[4:7], v[200:203], a[68:71], a[4:7]\n"
"v_mfma_f32_16x16x32_bf16 a[8:11], v[200:203], a[72:75], a[8:11]\n"
"v_mfma_f32_16x16x32_bf16 a[8:11], v[204:207], a[88:91], a[8:11]\n"
"v_mfma_f32_16x16x32_bf16 a[12:15], v[204:207], a[92:95], a[12:15]\n"
"ds_read_b64_tr_b16 v[204:205], v227 offset:25696\n"
"v_mfma_f32_16x16x32_bf16 a[12:15], v[200:203], a[76:79], a[12:15]\n"
"s_waitcnt lgkmcnt(5)\n"
"ds_read_b64_tr_b16 v[206:207], v227 offset:28256\n"
"ds_read_b64_tr_b16 v[200:201], v227 offset:20576\n"
"v_mfma_f32_16x16x32_bf16 a[16:19], v[208:211], a[64:67], a[16:19]\n"
"s_add_u32 m0, s87, 0x19000\n"
"ds_read_b64_tr_b16 v[202:203], v227 offset:23136\n"
"v_mfma_f32_16x16x32_bf16 a[16:19], v[212:215], a[80:83], a[16:19]\n"
"buffer_load_dwordx4 v226, s[40:43], s58 offen offset:0 lds\n"
"v_mfma_f32_16x16x32_bf16 a[20:23], v[212:215], a[84:87], a[20:23]\n"
"buffer_load_dword v255, v240, s[96:99], s60 offen offset:0 sc1\n"
"v_mfma_f32_16x16x32_bf16 a[20:23], v[208:211], a[68:71], a[20:23]\n"
"s_mul_i32 s58, s69, 0x3c000\n"
"s_add_u32 m0, s87, 0x1d000\n"
"v_mfma_f32_16x16x32_bf16 a[24:27], v[208:211], a[72:75], a[24:27]\n"
"s_add_u32 s58, s58, 0x14000\n"
"buffer_load_dwordx4 v222, s[40:43], s58 offen offset:0 lds\n"
"v_mfma_f32_16x16x32_bf16 a[24:27], v[212:215], a[88:91], a[24:27]\n"
"v_mfma_f32_16x16x32_bf16 a[28:31], v[212:215], a[92:95], a[28:31]\n"
"ds_read_b64_tr_b16 v[212:213], v227 offset:25728\n"
"v_mfma_f32_16x16x32_bf16 a[28:31], v[208:211], a[76:79], a[28:31]\n"
"s_waitcnt lgkmcnt(5)\n"
"ds_read_b64_tr_b16 v[214:215], v227 offset:28288\n"
"ds_read_b64_tr_b16 v[208:209], v227 offset:20608\n"
"v_mfma_f32_16x16x32_bf16 a[192:195], v[192:195], a[64:67], a[192:195]\n"
"ds_read_b64_tr_b16 v[210:211], v227 offset:23168\n"
"v_mfma_f32_16x16x32_bf16 a[192:195], v[196:199], a[80:83], a[192:195]\n"
"buffer_load_dwordx4 v223, s[40:43], s58 offen offset:1024 lds\n"
"v_mfma_f32_16x16x32_bf16 a[196:199], v[196:199], a[84:87], a[196:199]\n"
"v_mfma_f32_16x16x32_bf16 a[196:199], v[192:195], a[68:71], a[196:199]\n"
"buffer_load_dwordx4 v224, s[40:43], s58 offen offset:2048 lds\n"
"v_mfma_f32_16x16x32_bf16 a[200:203], v[192:195], a[72:75], a[200:203]\n"
"v_mfma_f32_16x16x32_bf16 a[200:203], v[196:199], a[88:91], a[200:203]\n"
"v_mfma_f32_16x16x32_bf16 a[204:207], v[196:199], a[92:95], a[204:207]\n"
"ds_read_b64_tr_b16 v[196:197], v227 offset:35840\n"
"v_mfma_f32_16x16x32_bf16 a[204:207], v[192:195], a[76:79], a[204:207]\n"
"s_waitcnt lgkmcnt(5)\n"
"ds_read_b64_tr_b16 v[198:199], v227 offset:38400\n"
"ds_read_b64_tr_b16 v[192:193], v227 offset:30720\n"
"v_mfma_f32_16x16x32_bf16 a[208:211], v[200:203], a[64:67], a[208:211]\n"
"ds_read_b64_tr_b16 v[194:195], v227 offset:33280\n"
"v_mfma_f32_16x16x32_bf16 a[208:211], v[204:207], a[80:83], a[208:211]\n"
"v_mfma_f32_16x16x32_bf16 a[212:215], v[204:207], a[84:87], a[212:215]\n"
"v_mfma_f32_16x16x32_bf16 a[212:215], v[200:203], a[68:71], a[212:215]\n"
"v_mfma_f32_16x16x32_bf16 a[216:219], v[200:203], a[72:75], a[216:219]\n"
"v_mfma_f32_16x16x32_bf16 a[216:219], v[204:207], a[88:91], a[216:219]\n"
"v_mfma_f32_16x16x32_bf16 a[220:223], v[204:207], a[92:95], a[220:223]\n"
"ds_read_b64_tr_b16 v[204:205], v227 offset:35872\n"
"v_mfma_f32_16x16x32_bf16 a[220:223], v[200:203], a[76:79], a[220:223]\n"
"s_waitcnt lgkmcnt(5)\n"
"ds_read_b64_tr_b16 v[206:207], v227 offset:38432\n"
"ds_read_b64_tr_b16 v[200:201], v227 offset:30752\n"
"v_mfma_f32_16x16x32_bf16 a[224:227], v[208:211], a[64:67], a[224:227]\n"
"ds_read_b64_tr_b16 v[202:203], v227 offset:33312\n"
"v_mfma_f32_16x16x32_bf16 a[224:227], v[212:215], a[80:83], a[224:227]\n"
"buffer_load_dwordx4 v225, s[40:43], s58 offen offset:3072 lds\n"
"v_mfma_f32_16x16x32_bf16 a[228:231], v[212:215], a[84:87], a[228:231]\n"
"s_add_u32 m0, s87, 0x1e000\n"
"v_mfma_f32_16x16x32_bf16 a[228:231], v[208:211], a[68:71], a[228:231]\n"
"buffer_load_dwordx4 v226, s[40:43], s58 offen offset:0 lds\n"
"v_mfma_f32_16x16x32_bf16 a[232:235], v[208:211], a[72:75], a[232:235]\n"
"s_mul_i32 s58, s95, 0x3c000\n"
"s_add_u32 m0, s87, 0x22000\n"
"v_mfma_f32_16x16x32_bf16 a[232:235], v[212:215], a[88:91], a[232:235]\n"
"s_add_u32 s58, s58, 0x28000\n"
"buffer_load_dwordx4 v222, s[40:43], s58 offen offset:0 lds\n"
"v_mfma_f32_16x16x32_bf16 a[236:239], v[212:215], a[92:95], a[236:239]\n"
"ds_read_b64_tr_b16 v[212:213], v227 offset:35904\n"
"v_mfma_f32_16x16x32_bf16 a[236:239], v[208:211], a[76:79], a[236:239]\n"
"s_waitcnt lgkmcnt(5)\n"
"ds_read_b64_tr_b16 v[214:215], v227 offset:38464\n"
"ds_read_b64_tr_b16 v[208:209], v227 offset:30784\n"
"v_mfma_f32_16x16x32_bf16 a[240:243], v[192:195], a[64:67], a[240:243]\n"
"ds_read_b64_tr_b16 v[210:211], v227 offset:33344\n"
"v_mfma_f32_16x16x32_bf16 a[240:243], v[196:199], a[80:83], a[240:243]\n"
"buffer_load_dwordx4 v223, s[40:43], s58 offen offset:1024 lds\n"
"v_mfma_f32_16x16x32_bf16 a[244:247], v[196:199], a[84:87], a[244:247]\n"
"v_mfma_f32_16x16x32_bf16 a[244:247], v[192:195], a[68:71], a[244:247]\n"
"v_mfma_f32_16x16x32_bf16 a[248:251], v[192:195], a[72:75], a[248:251]\n"
"buffer_load_dwordx4 v224, s[40:43], s58 offen offset:2048 lds\n"
"v_mfma_f32_16x16x32_bf16 a[248:251], v[196:199], a[88:91], a[248:251]\n"
"v_mfma_f32_16x16x32_bf16 a[252:255], v[196:199], a[92:95], a[252:255]\n"
"ds_read_b64_tr_b16 v[196:197], v227 offset:35936\n"
"v_mfma_f32_16x16x32_bf16 a[252:255], v[192:195], a[76:79], a[252:255]\n"
"s_waitcnt lgkmcnt(5)\n"
"ds_read_b64_tr_b16 v[198:199], v227 offset:38496\n"
"ds_read_b64_tr_b16 v[192:193], v227 offset:30816\n"
"v_mfma_f32_16x16x32_bf16 v[64:67], v[200:203], a[64:67], v[64:67]\n"
"ds_read_b64_tr_b16 v[194:195], v227 offset:33376\n"
"v_mfma_f32_16x16x32_bf16 v[64:67], v[204:207], a[80:83], v[64:67]\n"
"buffer_load_dwordx4 v225, s[40:43], s58 offen offset:3072 lds\n"
"v_mfma_f32_16x16x32_bf16 v[68:71], v[204:207], a[84:87], v[68:71]\n"
"v_mfma_f32_16x16x32_bf16 v[68:71], v[200:203], a[68:71], v[68:71]\n"
"s_add_u32 m0, s87, 0x23000\n"
"v_mfma_f32_16x16x32_bf16 v[72:75], v[200:203], a[72:75], v[72:75]\n"
"buffer_load_dwordx4 v226, s[40:43], s58 offen offset:0 lds\n"
"v_mfma_f32_16x16x32_bf16 v[72:75], v[204:207], a[88:91], v[72:75]\n"
"v_mfma_f32_16x16x32_bf16 v[76:79], v[204:207], a[92:95], v[76:79]\n"
"ds_read_b64_tr_b16 v[204:205], v227 offset:35968\n"
"v_mfma_f32_16x16x32_bf16 v[76:79], v[200:203], a[76:79], v[76:79]\n"
"s_waitcnt lgkmcnt(5)\n"
"ds_read_b64_tr_b16 v[206:207], v227 offset:38528\n"
"ds_read_b64_tr_b16 v[200:201], v227 offset:30848\n"
"v_mfma_f32_16x16x32_bf16 v[80:83], v[208:211], a[64:67], v[80:83]\n"
"ds_read_b64_tr_b16 v[202:203], v227 offset:33408\n"
"v_mfma_f32_16x16x32_bf16 v[80:83], v[212:215], a[80:83], v[80:83]\n"
"v_mfma_f32_16x16x32_bf16 v[84:87], v[212:215], a[84:87], v[84:87]\n"
"s_waitcnt vmcnt(10)\n"
"v_cmp_ne_u32 vcc, v255, v253\n"
"v_mfma_f32_16x16x32_bf16 v[84:87], v[208:211], a[68:71], v[84:87]\n"
"v_mfma_f32_16x16x32_bf16 v[88:91], v[208:211], a[72:75], v[88:91]\n"
"s_lshr_b32 s66, vcc_lo, 4\n"
"v_mfma_f32_16x16x32_bf16 v[88:91], v[212:215], a[88:91], v[88:91]\n"
"s_bitcmp1_b32 s66, s95\n"
"s_cbranch_scc1 Lspy1%=\n"
"Lrty1%=:\n"
"v_mfma_f32_16x16x32_bf16 v[92:95], v[212:215], a[92:95], v[92:95]\n"
"ds_read_b64_tr_b16 v[212:213], v227 offset:46080\n"
"v_mfma_f32_16x16x32_bf16 v[92:95], v[208:211], a[76:79], v[92:95]\n"
"s_waitcnt lgkmcnt(5)\n"
"ds_read_b64_tr_b16 v[214:215], v227 offset:48640\n"
"ds_read_b64_tr_b16 v[208:209], v227 offset:40960\n"
"v_mfma_f32_16x16x32_bf16 v[96:99], v[192:195], a[64:67], v[96:99]\n"
"s_mul_i32 s57, s95, 0x18000\n"
"ds_read_b64_tr_b16 v[210:211], v227 offset:43520\n"
"v_mfma_f32_16x16x32_bf16 v[96:99], v[196:199], a[80:83], v[96:99]\n"
"s_add_u32 s57, s57, 0x8000\n"
"buffer_load_dwordx4 a[128:131], v250, s[52:55], s57 offen offset:0 sc1\n"
"v_mfma_f32_16x16x32_bf16 v[100:103], v[196:199], a[84:87], v[100:103]\n"
"v_mfma_f32_16x16x32_bf16 v[100:103], v[192:195], a[68:71], v[100:103]\n"
"buffer_load_dwordx4 a[132:135], v250, s[52:55], s57 offen offset:1024 sc1\n"
"v_mfma_f32_16x16x32_bf16 v[104:107], v[192:195], a[72:75], v[104:107]\n"
"v_mfma_f32_16x16x32_bf16 v[104:107], v[196:199], a[88:91], v[104:107]\n"
"v_mfma_f32_16x16x32_bf16 v[108:111], v[196:199], a[92:95], v[108:111]\n"
"ds_read_b64_tr_b16 v[196:197], v227 offset:46112\n"
"v_mfma_f32_16x16x32_bf16 v[108:111], v[192:195], a[76:79], v[108:111]\n"
"s_waitcnt lgkmcnt(5)\n"
"ds_read_b64_tr_b16 v[198:199], v227 offset:48672\n"
"ds_read_b64_tr_b16 v[192:193], v227 offset:40992\n"
"v_mfma_f32_16x16x32_bf16 v[112:115], v[200:203], a[64:67], v[112:115]\n"
"ds_read_b64_tr_b16 v[194:195], v227 offset:43552\n"
"v_mfma_f32_16x16x32_bf16 v[112:115], v[204:207], a[80:83], v[112:115]\n"
"buffer_load_dwordx4 a[136:139], v250, s[52:55], s57 offen offset:2048 sc1\n"
"v_mfma_f32_16x16x32_bf16 v[116:119], v[204:207], a[84:87], v[116:119]\n"
"v_mfma_f32_16x16x32_bf16 v[116:119], v[200:203], a[68:71], v[116:119]\n"
"v_mfma_f32_16x16x32_bf16 v[120:123], v[200:203], a[72:75], v[120:123]\n"
"v_mfma_f32_16x16x32_bf16 v[120:123], v[204:207], a[88:91], v[120:123]\n"
"s_waitcnt vmcnt(19)\n"
"buffer_load_dwordx4 a[140:143], v250, s[52:55], s57 offen offset:3072 sc1\n"
"v_mfma_f32_16x16x32_bf16 v[124:127], v[204:207], a[92:95], v[124:127]\n"
"ds_read_b64_tr_b16 v[204:205], v227 offset:46144\n"
"v_mfma_f32_16x16x32_bf16 v[124:127], v[200:203], a[76:79], v[124:127]\n"
"s_waitcnt lgkmcnt(5)\n"
"ds_read_b64_tr_b16 v[206:207], v227 offset:48704\n"
"ds_read_b64_tr_b16 v[200:201], v227 offset:41024\n"
"v_mfma_f32_16x16x32_bf16 a[0:3], v[208:211], a[96:99], a[0:3]\n"
"ds_read_b64_tr_b16 v[202:203], v227 offset:43584\n"
"v_mfma_f32_16x16x32_bf16 a[0:3], v[212:215], a[112:115], a[0:3]\n"
"v_mfma_f32_16x16x32_bf16 a[4:7], v[212:215], a[116:119], a[4:7]\n"
"s_add_u32 s57, s57, 0x4000\n"
"buffer_load_dwordx4 a[144:147], v250, s[52:55], s57 offen offset:0 sc1\n"
"v_mfma_f32_16x16x32_bf16 a[4:7], v[208:211], a[100:103], a[4:7]\n"
"v_mfma_f32_16x16x32_bf16 a[8:11], v[208:211], a[104:107], a[8:11]\n"
"v_mfma_f32_16x16x32_bf16 a[8:11], v[212:215], a[120:123], a[8:11]\n"
"v_mfma_f32_16x16x32_bf16 a[12:15], v[212:215], a[124:127], a[12:15]\n"
"ds_read_b64_tr_b16 v[212:213], v227 offset:46176\n"
"v_mfma_f32_16x16x32_bf16 a[12:15], v[208:211], a[108:111], a[12:15]\n"
"s_waitcnt lgkmcnt(5)\n"
"ds_read_b64_tr_b16 v[214:215], v227 offset:48736\n"
"ds_read_b64_tr_b16 v[208:209], v227 offset:41056\n"
"v_mfma_f32_16x16x32_bf16 a[16:19], v[192:195], a[96:99], a[16:19]\n"
"ds_read_b64_tr_b16 v[210:211], v227 offset:43616\n"
"v_mfma_f32_16x16x32_bf16 a[16:19], v[196:199], a[112:115], a[16:19]\n"
"buffer_load_dwordx4 a[148:151], v250, s[52:55], s57 offen offset:1024 sc1\n"
"v_mfma_f32_16x16x32_bf16 a[20:23], v[196:199], a[116:119], a[20:23]\n"
"v_mfma_f32_16x16x32_bf16 a[20:23], v[192:195], a[100:103], a[20:23]\n"
"v_mfma_f32_16x16x32_bf16 a[24:27], v[192:195], a[104:107], a[24:27]\n"
"buffer_load_dwordx4 a[152:155], v250, s[52:55], s57 offen offset:2048 sc1\n"
"v_mfma_f32_16x16x32_bf16 a[24:27], v[196:199], a[120:123], a[24:27]\n"
"v_mfma_f32_16x16x32_bf16 a[28:31], v[196:199], a[124:127], a[28:31]\n"
"ds_read_b64_tr_b16 v[196:197], v227 offset:46208\n"
"v_mfma_f32_16x16x32_bf16 a[28:31], v[192:195], a[108:111], a[28:31]\n"
"s_waitcnt lgkmcnt(5)\n"
"ds_read_b64_tr_b16 v[198:199], v227 offset:48768\n"
"ds_read_b64_tr_b16 v[192:193], v227 offset:41088\n"
"v_mfma_f32_16x16x32_bf16 a[192:195], v[200:203], a[96:99], a[192:195]\n"
"ds_read_b64_tr_b16 v[194:195], v227 offset:43648\n"
"v_mfma_f32_16x16x32_bf16 a[192:195], v[204:207], a[112:115], a[192:195]\n"
"buffer_load_dwordx4 a[156:159], v250, s[52:55], s57 offen offset:3072 sc1\n"
"v_mfma_f32_16x16x32_bf16 a[196:199], v[204:207], a[116:119], a[196:199]\n"
"v_mfma_f32_16x16x32_bf16 a[196:199], v[200:203], a[100:103], a[196:199]\n"
"v_mfma_f32_16x16x32_bf16 a[200:203], v[200:203], a[104:107], a[200:203]\n"
"v_mfma_f32_16x16x32_bf16 a[200:203], v[204:207], a[120:123], a[200:203]\n"
"v_mfma_f32_16x16x32_bf16 a[204:207], v[204:207], a[124:127], a[204:207]\n"
"ds_read_b64_tr_b16 v[204:205], v227 offset:56320\n"
"v_mfma_f32_16x16x32_bf16 a[204:207], v[200:203], a[108:111], a[204:207]\n"
"s_waitcnt lgkmcnt(5)\n"
"ds_read_b64_tr_b16 v[206:207], v227 offset:58880\n"
"ds_read_b64_tr_b16 v[200:201], v227 offset:51200\n"
"v_mfma_f32_16x16x32_bf16 a[208:211], v[208:211], a[96:99], a[208:211]\n"
"ds_read_b64_tr_b16 v[202:203], v227 offset:53760\n"
"v_mfma_f32_16x16x32_bf16 a[208:211], v[212:215], a[112:115], a[208:211]\n"
"s_bitcmp1_b32 s66, s67\n"
"s_cbranch_scc1 Lspy2%=\n"
"Lrty2%=:\n"
"s_mul_i32 s57, s67, 0x18000\n"
"v_mfma_f32_16x16x32_bf16 a[212:215], v[212:215], a[116:119], a[212:215]\n"
"s_add_u32 s57, s57, 0x8000\n"
"buffer_load_dwordx4 a[160:163], v250, s[52:55], s57 offen offset:0 sc1\n"
"v_mfma_f32_16x16x32_bf16 a[212:215], v[208:211], a[100:103], a[212:215]\n"
"v_mfma_f32_16x16x32_bf16 a[216:219], v[208:211], a[104:107], a[216:219]\n"
"v_mfma_f32_16x16x32_bf16 a[216:219], v[212:215], a[120:123], a[216:219]\n"
"v_mfma_f32_16x16x32_bf16 a[220:223], v[212:215], a[124:127], a[220:223]\n"
"ds_read_b64_tr_b16 v[212:213], v227 offset:56352\n"
"v_mfma_f32_16x16x32_bf16 a[220:223], v[208:211], a[108:111], a[220:223]\n"
"s_waitcnt lgkmcnt(5)\n"
"ds_read_b64_tr_b16 v[214:215], v227 offset:58912\n"
"ds_read_b64_tr_b16 v[208:209], v227 offset:51232\n"
"v_mfma_f32_16x16x32_bf16 a[224:227], v[192:195], a[96:99], a[224:227]\n"
"ds_read_b64_tr_b16 v[210:211], v227 offset:53792\n"
"v_mfma_f32_16x16x32_bf16 a[224:227], v[196:199], a[112:115], a[224:227]\n"
"buffer_load_dwordx4 a[164:167], v250, s[52:55], s57 offen offset:1024 sc1\n"
"v_mfma_f32_16x16x32_bf16 a[228:231], v[196:199], a[116:119], a[228:231]\n"
"v_mfma_f32_16x16x32_bf16 a[228:231], v[192:195], a[100:103], a[228:231]\n"
"v_mfma_f32_16x16x32_bf16 a[232:235], v[192:195], a[104:107], a[232:235]\n"
"buffer_load_dwordx4 a[168:171], v250, s[52:55], s57 offen offset:2048 sc1\n"
"v_mfma_f32_16x16x32_bf16 a[232:235], v[196:199], a[120:123], a[232:235]\n"
"v_mfma_f32_16x16x32_bf16 a[236:239], v[196:199], a[124:127], a[236:239]\n"
"ds_read_b64_tr_b16 v[196:197], v227 offset:56384\n"
"v_mfma_f32_16x16x32_bf16 a[236:239], v[192:195], a[108:111], a[236:239]\n"
"s_waitcnt lgkmcnt(5)\n"
"ds_read_b64_tr_b16 v[198:199], v227 offset:58944\n"
"ds_read_b64_tr_b16 v[192:193], v227 offset:51264\n"
"v_mfma_f32_16x16x32_bf16 a[240:243], v[200:203], a[96:99], a[240:243]\n"
"ds_read_b64_tr_b16 v[194:195], v227 offset:53824\n"
"v_mfma_f32_16x16x32_bf16 a[240:243], v[204:207], a[112:115], a[240:243]\n"
"buffer_load_dwordx4 a[172:175], v250, s[52:55], s57 offen offset:3072 sc1\n"
"v_mfma_f32_16x16x32_bf16 a[244:247], v[204:207], a[116:119], a[244:247]\n"
"v_mfma_f32_16x16x32_bf16 a[244:247], v[200:203], a[100:103], a[244:247]\n"
"v_mfma_f32_16x16x32_bf16 a[248:251], v[200:203], a[104:107], a[248:251]\n"
"v_mfma_f32_16x16x32_bf16 a[248:251], v[204:207], a[120:123], a[248:251]\n"
"v_mfma_f32_16x16x32_bf16 a[252:255], v[204:207], a[124:127], a[252:255]\n"
"ds_read_b64_tr_b16 v[204:205], v227 offset:56416\n"
"v_mfma_f32_16x16x32_bf16 a[252:255], v[200:203], a[108:111], a[252:255]\n"
"s_waitcnt lgkmcnt(5)\n"
"ds_read_b64_tr_b16 v[206:207], v227 offset:58976\n"
"ds_read_b64_tr_b16 v[200:201], v227 offset:51296\n"
"v_mfma_f32_16x16x32_bf16 v[64:67], v[208:211], a[96:99], v[64:67]\n"
"ds_read_b64_tr_b16 v[202:203], v227 offset:53856\n"
"v_mfma_f32_16x16x32_bf16 v[64:67], v[212:215], a[112:115], v[64:67]\n"
"s_add_u32 s57, s57, 0x4000\n"
"buffer_load_dwordx4 a[176:179], v250, s[52:55], s57 offen offset:0 sc1\n"
"v_mfma_f32_16x16x32_bf16 v[68:71], v[212:215], a[116:119], v[68:71]\n"
"v_mfma_f32_16x16x32_bf16 v[68:71], v[208:211], a[100:103], v[68:71]\n"
"buffer_load_dwordx4 a[180:183], v250, s[52:55], s57 offen offset:1024 sc1\n"
"v_mfma_f32_16x16x32_bf16 v[72:75], v[208:211], a[104:107], v[72:75]\n"
"v_mfma_f32_16x16x32_bf16 v[72:75], v[212:215], a[120:123], v[72:75]\n"
"v_mfma_f32_16x16x32_bf16 v[76:79], v[212:215], a[124:127], v[76:79]\n"
"ds_read_b64_tr_b16 v[212:213], v227 offset:56448\n"
"v_mfma_f32_16x16x32_bf16 v[76:79], v[208:211], a[108:111], v[76:79]\n"
"s_waitcnt lgkmcnt(5)\n"
"ds_read_b64_tr_b16 v[214:215], v227 offset:59008\n"
"ds_read_b64_tr_b16 v[208:209], v227 offset:51328\n"
"v_mfma_f32_16x16x32_bf16 v[80:83], v[192:195], a[96:99], v[80:83]\n"
"ds_read_b64_tr_b16 v[210:211], v227 offset:53888\n"
"v_mfma_f32_16x16x32_bf16 v[80:83], v[196:199], a[112:115], v[80:83]\n"
"buffer_load_dwordx4 a[184:187], v250, s[52:55], s57 offen offset:2048 sc1\n"
"v_mfma_f32_16x16x32_bf16 v[84:87], v[196:199], a[116:119], v[84:87]\n"
"v_mfma_f32_16x16x32_bf16 v[84:87], v[192:195], a[100:103], v[84:87]\n"
"v_mfma_f32_16x16x32_bf16 v[88:91], v[192:195], a[104:107], v[88:91]\n"
"v_mfma_f32_16x16x32_bf16 v[88:91], v[196:199], a[120:123], v[88:91]\n"
"v_mfma_f32_16x16x32_bf16 v[92:95], v[196:199], a[124:127], v[92:95]\n"
"s_waitcnt vmcnt(20) lgkmcnt(0)\n"
"s_barrier\n"
"ds_read_b64_tr_b16 v[196:197], v228 offset:5120\n"
"v_mfma_f32_16x16x32_bf16 v[92:95], v[192:195], a[108:111], v[92:95]\n"
"ds_read_b64_tr_b16 v[198:199], v228 offset:7680\n"
"ds_read_b64_tr_b16 v[192:193], v228 offset:0\n"
"v_mfma_f32_16x16x32_bf16 v[96:99], v[200:203], a[96:99], v[96:99]\n"
"ds_read_b64_tr_b16 v[194:195], v228 offset:2560\n"
"v_mfma_f32_16x16x32_bf16 v[96:99], v[204:207], a[112:115], v[96:99]\n"
"v_mfma_f32_16x16x32_bf16 v[100:103], v[204:207], a[116:119], v[100:103]\n"
"v_mfma_f32_16x16x32_bf16 v[100:103], v[200:203], a[100:103], v[100:103]\n"
"v_mfma_f32_16x16x32_bf16 v[104:107], v[200:203], a[104:107], v[104:107]\n"
"v_mfma_f32_16x16x32_bf16 v[104:107], v[204:207], a[120:123], v[104:107]\n"
"v_mfma_f32_16x16x32_bf16 v[108:111], v[204:207], a[124:127], v[108:111]\n"
"ds_read_b64_tr_b16 v[204:205], v228 offset:5152\n"
"v_mfma_f32_16x16x32_bf16 v[108:111], v[200:203], a[108:111], v[108:111]\n"
"ds_read_b64_tr_b16 v[206:207], v228 offset:7712\n"
"ds_read_b64_tr_b16 v[200:201], v228 offset:32\n"
"v_mfma_f32_16x16x32_bf16 v[112:115], v[208:211], a[96:99], v[112:115]\n"
"ds_read_b64_tr_b16 v[202:203], v228 offset:2592\n"
"v_mfma_f32_16x16x32_bf16 v[112:115], v[212:215], a[112:115], v[112:115]\n"
"buffer_load_dwordx4 a[188:191], v250, s[52:55], s57 offen offset:3072 sc1\n"
"v_mfma_f32_16x16x32_bf16 v[116:119], v[212:215], a[116:119], v[116:119]\n"
"v_mfma_f32_16x16x32_bf16 v[116:119], v[208:211], a[100:103], v[116:119]\n"
"v_mfma_f32_16x16x32_bf16 v[120:123], v[208:211], a[104:107], v[120:123]\n"
"v_mfma_f32_16x16x32_bf16 v[120:123], v[212:215], a[120:123], v[120:123]\n"
"s_bitcmp1_b32 s66, s69\n"
"s_cbranch_scc1 Lspy3%=\n"
"Lrty3%=:\n"
"s_waitcnt vmcnt(8)\n"
"v_mfma_f32_16x16x32_bf16 v[124:127], v[212:215], a[124:127], v[124:127]\n"
"ds_read_b64_tr_b16 v[212:213], v228 offset:5184\n"
"v_mfma_f32_16x16x32_bf16 v[124:127], v[208:211], a[108:111], v[124:127]\n"
"s_waitcnt lgkmcnt(5)\n"
"ds_read_b64_tr_b16 v[214:215], v228 offset:7744\n"
"ds_read_b64_tr_b16 v[208:209], v228 offset:64\n"
"v_mfma_f32_16x16x32_bf16 a[0:3], v[192:195], a[128:131], a[0:3]\n"
"s_mul_i32 s57, s69, 0x18000\n"
"ds_read_b64_tr_b16 v[210:211], v228 offset:2624\n"
"v_mfma_f32_16x16x32_bf16 a[0:3], v[196:199], a[144:147], a[0:3]\n"
"s_add_u32 s57, s57, 0x8000\n"
"buffer_load_dwordx4 v[32:35], v250, s[52:55], s57 offen offset:0 sc1\n"
"v_mfma_f32_16x16x32_bf16 a[4:7], v[196:199], a[148:151], a[4:7]\n"
"v_mfma_f32_16x16x32_bf16 a[4:7], v[192:195], a[132:135], a[4:7]\n"
"buffer_load_dwordx4 v[36:39], v250, s[52:55], s57 offen offset:1024 sc1\n"
"v_mfma_f32_16x16x32_bf16 a[8:11], v[192:195], a[136:139], a[8:11]\n"
"v_mfma_f32_16x16x32_bf16 a[8:11], v[196:199], a[152:155], a[8:11]\n"
"v_mfma_f32_16x16x32_bf16 a[12:15], v[196:199], a[156:159], a[12:15]\n"
"ds_read_b64_tr_b16 v[196:197], v228 offset:5216\n"
"v_mfma_f32_16x16x32_bf16 a[12:15], v[192:195], a[140:143], a[12:15]\n"
"s_waitcnt lgkmcnt(5)\n"
"ds_read_b64_tr_b16 v[198:199], v228 offset:7776\n"
"ds_read_b64_tr_b16 v[192:193], v228 offset:96\n"
"v_mfma_f32_16x16x32_bf16 a[16:19], v[200:203], a[128:131], a[16:19]\n"
"ds_read_b64_tr_b16 v[194:195], v228 offset:2656\n"
"v_mfma_f32_16x16x32_bf16 a[16:19], v[204:207], a[144:147], a[16:19]\n"
"buffer_load_dwordx4 v[40:43], v250, s[52:55], s57 offen offset:2048 sc1\n"
"v_mfma_f32_16x16x32_bf16 a[20:23], v[204:207], a[148:151], a[20:23]\n"
"v_mfma_f32_16x16x32_bf16 a[20:23], v[200:203], a[132:135], a[20:23]\n"
"v_mfma_f32_16x16x32_bf16 a[24:27], v[200:203], a[136:139], a[24:27]\n"
"v_mfma_f32_16x16x32_bf16 a[24:27], v[204:207], a[152:155], a[24:27]\n"
"buffer_load_dwordx4 v[44:47], v250, s[52:55], s57 offen offset:3072 sc1\n"
"v_mfma_f32_16x16x32_bf16 a[28:31], v[204:207], a[156:159], a[28:31]\n"
"ds_read_b64_tr_b16 v[204:205], v228 offset:5248\n"
"v_mfma_f32_16x16x32_bf16 a[28:31], v[200:203], a[140:143], a[28:31]\n"
"s_waitcnt lgkmcnt(5)\n"
"ds_read_b64_tr_b16 v[206:207], v228 offset:7808\n"
"ds_read_b64_tr_b16 v[200:201], v228 offset:128\n"
"v_mfma_f32_16x16x32_bf16 a[192:195], v[208:211], a[128:131], a[192:195]\n"
"ds_read_b64_tr_b16 v[202:203], v228 offset:2688\n"
"v_mfma_f32_16x16x32_bf16 a[192:195], v[212:215], a[144:147], a[192:195]\n"
"v_mfma_f32_16x16x32_bf16 a[196:199], v[212:215], a[148:151], a[196:199]\n"
"s_add_u32 s57, s57, 0x4000\n"
"buffer_load_dwordx4 v[48:51], v250, s[52:55], s57 offen offset:0 sc1\n"
"v_mfma_f32_16x16x32_bf16 a[196:199], v[208:211], a[132:135], a[196:199]\n"
"v_mfma_f32_16x16x32_bf16 a[200:203], v[208:211], a[136:139], a[200:203]\n"
"v_mfma_f32_16x16x32_bf16 a[200:203], v[212:215], a[152:155], a[200:203]\n"
"v_mfma_f32_16x16x32_bf16 a[204:207], v[212:215], a[156:159], a[204:207]\n"
"ds_read_b64_tr_b16 v[212:213], v228 offset:15360\n"
"v_mfma_f32_16x16x32_bf16 a[204:207], v[208:211], a[140:143], a[204:207]\n"
"s_waitcnt lgkmcnt(5)\n"
"ds_read_b64_tr_b16 v[214:215], v228 offset:17920\n"
"ds_read_b64_tr_b16 v[208:209], v228 offset:10240\n"
"v_mfma_f32_16x16x32_bf16 a[208:211], v[192:195], a[128:131], a[208:211]\n"
"ds_read_b64_tr_b16 v[210:211], v228 offset:12800\n"
"v_mfma_f32_16x16x32_bf16 a[208:211], v[196:199], a[144:147], a[208:211]\n"
"buffer_load_dwordx4 v[52:55], v250, s[52:55], s57 offen offset:1024 sc1\n"
"v_mfma_f32_16x16x32_bf16 a[212:215], v[196:199], a[148:151], a[212:215]\n"
"v_mfma_f32_16x16x32_bf16 a[212:215], v[192:195], a[132:135], a[212:215]\n"
"v_mfma_f32_16x16x32_bf16 a[216:219], v[192:195], a[136:139], a[216:219]\n"
"buffer_load_dwordx4 v[56:59], v250, s[52:55], s57 offen offset:2048 sc1\n"
"v_mfma_f32_16x16x32_bf16 a[216:219], v[196:199], a[152:155], a[216:219]\n"
"v_mfma_f32_16x16x32_bf16 a[220:223], v[196:199], a[156:159], a[220:223]\n"
"ds_read_b64_tr_b16 v[196:197], v228 offset:15392\n"
"v_mfma_f32_16x16x32_bf16 a[220:223], v[192:195], a[140:143], a[220:223]\n"
"s_waitcnt lgkmcnt(5)\n"
"ds_read_b64_tr_b16 v[198:199], v228 offset:17952\n"
"ds_read_b64_tr_b16 v[192:193], v228 offset:10272\n"
"v_mfma_f32_16x16x32_bf16 a[224:227], v[200:203], a[128:131], a[224:227]\n"
"ds_read_b64_tr_b16 v[194:195], v228 offset:12832\n"
"v_mfma_f32_16x16x32_bf16 a[224:227], v[204:207], a[144:147], a[224:227]\n"
"buffer_load_dwordx4 v[60:63], v250, s[52:55], s57 offen offset:3072 sc1\n"
"v_mfma_f32_16x16x32_bf16 a[228:231], v[204:207], a[148:151], a[228:231]\n"
"v_mfma_f32_16x16x32_bf16 a[228:231], v[200:203], a[132:135], a[228:231]\n"
"v_mfma_f32_16x16x32_bf16 a[232:235], v[200:203], a[136:139], a[232:235]\n"
"v_mfma_f32_16x16x32_bf16 a[232:235], v[204:207], a[152:155], a[232:235]\n"
"v_mfma_f32_16x16x32_bf16 a[236:239], v[204:207], a[156:159], a[236:239]\n"
"ds_read_b64_tr_b16 v[204:205], v228 offset:15424\n"
"v_mfma_f32_16x16x32_bf16 a[236:239], v[200:203], a[140:143], a[236:239]\n"
"s_waitcnt lgkmcnt(5)\n"
"ds_read_b64_tr_b16 v[206:207], v228 offset:17984\n"
"ds_read_b64_tr_b16 v[200:201], v228 offset:10304\n"
"v_mfma_f32_16x16x32_bf16 a[240:243], v[208:211], a[128:131], a[240:243]\n"
"s_mul_i32 s58, s67, 0x3c000\n"
"s_add_u32 m0, s87, 0\n"
"ds_read_b64_tr_b16 v[202:203], v228 offset:12864\n"
"v_mfma_f32_16x16x32_bf16 a[240:243], v[212:215], a[144:147], a[240:243]\n"
"s_add_u32 s58, s58, 0x28000\n"
"buffer_load_dwordx4 v222, s[40:43], s58 offen offset:0 lds\n"
"v_mfma_f32_16x16x32_bf16 a[244:247], v[212:215], a[148:151], a[244:247]\n"
"v_mfma_f32_16x16x32_bf16 a[244:247], v[208:211], a[132:135], a[244:247]\n"
"buffer_load_dwordx4 v223, s[40:43], s58 offen offset:1024 lds\n"
"v_mfma_f32_16x16x32_bf16 a[248:251], v[208:211], a[136:139], a[248:251]\n"
"v_mfma_f32_16x16x32_bf16 a[248:251], v[212:215], a[152:155], a[248:251]\n"
"v_mfma_f32_16x16x32_bf16 a[252:255], v[212:215], a[156:159], a[252:255]\n"
"ds_read_b64_tr_b16 v[212:213], v228 offset:15456\n"
"v_mfma_f32_16x16x32_bf16 a[252:255], v[208:211], a[140:143], a[252:255]\n"
"s_waitcnt lgkmcnt(5)\n"
"ds_read_b64_tr_b16 v[214:215], v228 offset:18016\n"
"ds_read_b64_tr_b16 v[208:209], v228 offset:10336\n"
"v_mfma_f32_16x16x32_bf16 v[64:67], v[192:195], a[128:131], v[64:67]\n"
"ds_read_b64_tr_b16 v[210:211], v228 offset:12896\n"
"v_mfma_f32_16x16x32_bf16 v[64:67], v[196:199], a[144:147], v[64:67]\n"
"buffer_load_dwordx4 v224, s[40:43], s58 offen offset:2048 lds\n"
"v_mfma_f32_16x16x32_bf16 v[68:71], v[196:199], a[148:151], v[68:71]\n"
"v_mfma_f32_16x16x32_bf16 v[68:71], v[192:195], a[132:135], v[68:71]\n"
"v_mfma_f32_16x16x32_bf16 v[72:75], v[192:195], a[136:139], v[72:75]\n"
"v_mfma_f32_16x16x32_bf16 v[72:75], v[196:199], a[152:155], v[72:75]\n"
"buffer_load_dwordx4 v225, s[40:43], s58 offen offset:3072 lds\n"
"v_mfma_f32_16x16x32_bf16 v[76:79], v[196:199], a[156:159], v[76:79]\n"
"ds_read_b64_tr_b16 v[196:197], v228 offset:15488\n"
"v_mfma_f32_16x16x32_bf16 v[76:79], v[192:195], a[140:143], v[76:79]\n"
"s_waitcnt lgkmcnt(5)\n"
"ds_read_b64_tr_b16 v[198:199], v228 offset:18048\n"
"ds_read_b64_tr_b16 v[192:193], v228 offset:10368\n"
"v_mfma_f32_16x16x32_bf16 v[80:83], v[200:203], a[128:131], v[80:83]\n"
"ds_read_b64_tr_b16 v[194:195], v228 offset:12928\n"
"v_mfma_f32_16x16x32_bf16 v[80:83], v[204:207], a[144:147], v[80:83]\n"
"s_add_u32 m0, s87, 0x1000\n"
"v_mfma_f32_16x16x32_bf16 v[84:87], v[204:207], a[148:151], v[84:87]\n"
"buffer_load_dwordx4 v226, s[40:43], s58 offen offset:0 lds\n"
"v_mfma_f32_16x16x32_bf16 v[84:87], v[200:203], a[132:135], v[84:87]\n"
"v_mfma_f32_16x16x32_bf16 v[88:91], v[200:203], a[136:139], v[88:91]\n"
"v_mfma_f32_16x16x32_bf16 v[88:91], v[204:207], a[152:155], v[88:91]\n"
"v_mfma_f32_16x16x32_bf16 v[92:95], v[204:207], a[156:159], v[92:95]\n"
"ds_read_b64_tr_b16 v[204:205], v229 offset:5120\n"
"v_mfma_f32_16x16x32_bf16 v[92:95], v[200:203], a[140:143], v[92:95]\n"
"s_waitcnt lgkmcnt(5)\n"
"ds_read_b64_tr_b16 v[206:207], v229 offset:7680\n"
"ds_read_b64_tr_b16 v[200:201], v229 offset:0\n"
"v_mfma_f32_16x16x32_bf16 v[96:99], v[208:211], a[128:131], v[96:99]\n"
"s_mul_i32 s58, s69, 0x3c000\n"
"s_add_u32 m0, s87, 0x5000\n"
"ds_read_b64_tr_b16 v[202:203], v229 offset:2560\n"
"v_mfma_f32_16x16x32_bf16 v[96:99], v[212:215], a[144:147], v[96:99]\n"
"s_add_u32 s58, s58, 0x28000\n"
"buffer_load_dwordx4 v222, s[40:43], s58 offen offset:0 lds\n"
"v_mfma_f32_16x16x32_bf16 v[100:103], v[212:215], a[148:151], v[100:103]\n"
"v_mfma_f32_16x16x32_bf16 v[100:103], v[208:211], a[132:135], v[100:103]\n"
"v_mfma_f32_16x16x32_bf16 v[104:107], v[208:211], a[136:139], v[104:107]\n"
"buffer_load_dwordx4 v223, s[40:43], s58 offen offset:1024 lds\n"
"v_mfma_f32_16x16x32_bf16 v[104:107], v[212:215], a[152:155], v[104:107]\n"
"v_mfma_f32_16x16x32_bf16 v[108:111], v[212:215], a[156:159], v[108:111]\n"
"ds_read_b64_tr_b16 v[212:213], v229 offset:5152\n"
"v_mfma_f32_16x16x32_bf16 v[108:111], v[208:211], a[140:143], v[108:111]\n"
"s_waitcnt lgkmcnt(5)\n"
"ds_read_b64_tr_b16 v[214:215], v229 offset:7712\n"
"ds_read_b64_tr_b16 v[208:209], v229 offset:32\n"
"v_mfma_f32_16x16x32_bf16 v[112:115], v[192:195], a[128:131], v[112:115]\n"
"ds_read_b64_tr_b16 v[210:211], v229 offset:2592\n"
"v_mfma_f32_16x16x32_bf16 v[112:115], v[196:199], a[144:147], v[112:115]\n"
"buffer_load_dwordx4 v224, s[40:43], s58 offen offset:2048 lds\n"
"v_mfma_f32_16x16x32_bf16 v[116:119], v[196:199], a[148:151], v[116:119]\n"
"v_mfma_f32_16x16x32_bf16 v[116:119], v[192:195], a[132:135], v[116:119]\n"
"v_mfma_f32_16x16x32_bf16 v[120:123], v[192:195], a[136:139], v[120:123]\n"
"v_mfma_f32_16x16x32_bf16 v[120:123], v[196:199], a[152:155], v[120:123]\n"
"s_waitcnt vmcnt(16)\n"
"v_mfma_f32_16x16x32_bf16 v[124:127], v[196:199], a[156:159], v[124:127]\n"
"ds_read_b64_tr_b16 v[196:197], v229 offset:5184\n"
"v_mfma_f32_16x16x32_bf16 v[124:127], v[192:195], a[140:143], v[124:127]\n"
"s_waitcnt lgkmcnt(5)\n"
"ds_read_b64_tr_b16 v[198:199], v229 offset:7744\n"
"ds_read_b64_tr_b16 v[192:193], v229 offset:64\n"
"v_mfma_f32_16x16x32_bf16 a[0:3], v[200:203], a[160:163], a[0:3]\n"
"ds_read_b64_tr_b16 v[194:195], v229 offset:2624\n"
"v_mfma_f32_16x16x32_bf16 a[0:3], v[204:207], a[176:179], a[0:3]\n"
"buffer_load_dwordx4 v225, s[40:43], s58 offen offset:3072 lds\n"
"v_mfma_f32_16x16x32_bf16 a[4:7], v[204:207], a[180:183], a[4:7]\n"
"s_add_u32 m0, s87, 0x6000\n"
"v_mfma_f32_16x16x32_bf16 a[4:7], v[200:203], a[164:167], a[4:7]\n"
"buffer_load_dwordx4 v226, s[40:43], s58 offen offset:0 lds\n"
"v_mfma_f32_16x16x32_bf16 a[8:11], v[200:203], a[168:171], a[8:11]\n"
"v_mfma_f32_16x16x32_bf16 a[8:11], v[204:207], a[184:187], a[8:11]\n"
"v_mfma_f32_16x16x32_bf16 a[12:15], v[204:207], a[188:191], a[12:15]\n"
"ds_read_b64_tr_b16 v[204:205], v229 offset:5216\n"
"v_mfma_f32_16x16x32_bf16 a[12:15], v[200:203], a[172:175], a[12:15]\n"
"s_waitcnt lgkmcnt(5)\n"
"ds_read_b64_tr_b16 v[206:207], v229 offset:7776\n"
"ds_read_b64_tr_b16 v[200:201], v229 offset:96\n"
"v_mfma_f32_16x16x32_bf16 a[16:19], v[208:211], a[160:163], a[16:19]\n"
"s_mul_i32 s57, s95, 0x18000\n"
"ds_read_b64_tr_b16 v[202:203], v229 offset:2656\n"
"v_mfma_f32_16x16x32_bf16 a[16:19], v[212:215], a[176:179], a[16:19]\n"
"s_add_u32 s57, s57, 0x10000\n"
"buffer_load_dwordx4 v[128:131], v250, s[52:55], s57 offen offset:0 sc1\n"
"v_mfma_f32_16x16x32_bf16 a[20:23], v[212:215], a[180:183], a[20:23]\n"
"v_mfma_f32_16x16x32_bf16 a[20:23], v[208:211], a[164:167], a[20:23]\n"
"v_mfma_f32_16x16x32_bf16 a[24:27], v[208:211], a[168:171], a[24:27]\n"
"v_mfma_f32_16x16x32_bf16 a[24:27], v[212:215], a[184:187], a[24:27]\n"
"buffer_load_dwordx4 v[132:135], v250, s[52:55], s57 offen offset:1024 sc1\n"
"v_mfma_f32_16x16x32_bf16 a[28:31], v[212:215], a[188:191], a[28:31]\n"
"ds_read_b64_tr_b16 v[212:213], v229 offset:5248\n"
"v_mfma_f32_16x16x32_bf16 a[28:31], v[208:211], a[172:175], a[28:31]\n"
"s_waitcnt lgkmcnt(5)\n"
"ds_read_b64_tr_b16 v[214:215], v229 offset:7808\n"
"ds_read_b64_tr_b16 v[208:209], v229 offset:128\n"
"v_mfma_f32_16x16x32_bf16 a[192:195], v[192:195], a[160:163], a[192:195]\n"
"ds_read_b64_tr_b16 v[210:211], v229 offset:2688\n"
"v_mfma_f32_16x16x32_bf16 a[192:195], v[196:199], a[176:179], a[192:195]\n"
"v_mfma_f32_16x16x32_bf16 a[196:199], v[196:199], a[180:183], a[196:199]\n"
"buffer_load_dwordx4 v[136:139], v250, s[52:55], s57 offen offset:2048 sc1\n"
"v_mfma_f32_16x16x32_bf16 a[196:199], v[192:195], a[164:167], a[196:199]\n"
"v_mfma_f32_16x16x32_bf16 a[200:203], v[192:195], a[168:171], a[200:203]\n"
"v_mfma_f32_16x16x32_bf16 a[200:203], v[196:199], a[184:187], a[200:203]\n"
"v_mfma_f32_16x16x32_bf16 a[204:207], v[196:199], a[188:191], a[204:207]\n"
"ds_read_b64_tr_b16 v[196:197], v229 offset:15360\n"
"v_mfma_f32_16x16x32_bf16 a[204:207], v[192:195], a[172:175], a[204:207]\n"
"s_waitcnt lgkmcnt(5)\n"
"ds_read_b64_tr_b16 v[198:199], v229 offset:17920\n"
"ds_read_b64_tr_b16 v[192:193], v229 offset:10240\n"
"v_mfma_f32_16x16x32_bf16 a[208:211], v[200:203], a[160:163], a[208:211]\n"
"ds_read_b64_tr_b16 v[194:195], v229 offset:12800\n"
"v_mfma_f32_16x16x32_bf16 a[208:211], v[204:207], a[176:179], a[208:211]\n"
"v_mfma_f32_16x16x32_bf16 a[212:215], v[204:207], a[180:183], a[212:215]\n"
"v_mfma_f32_16x16x32_bf16 a[212:215], v[200:203], a[164:167], a[212:215]\n"
"v_mfma_f32_16x16x32_bf16 a[216:219], v[200:203], a[168:171], a[216:219]\n"
"v_mfma_f32_16x16x32_bf16 a[216:219], v[204:207], a[184:187], a[216:219]\n"
"v_mfma_f32_16x16x32_bf16 a[220:223], v[204:207], a[188:191], a[220:223]\n"
"ds_read_b64_tr_b16 v[204:205], v229 offset:15392\n"
"v_mfma_f32_16x16x32_bf16 a[220:223], v[200:203], a[172:175], a[220:223]\n"
"s_waitcnt lgkmcnt(5)\n"
"ds_read_b64_tr_b16 v[206:207], v229 offset:17952\n"
"ds_read_b64_tr_b16 v[200:201], v229 offset:10272\n"
"v_mfma_f32_16x16x32_bf16 a[224:227], v[208:211], a[160:163], a[224:227]\n"
"ds_read_b64_tr_b16 v[202:203], v229 offset:12832\n"
"v_mfma_f32_16x16x32_bf16 a[224:227], v[212:215], a[176:179], a[224:227]\n"
"buffer_load_dwordx4 v[140:143], v250, s[52:55], s57 offen offset:3072 sc1\n"
"v_mfma_f32_16x16x32_bf16 a[228:231], v[212:215], a[180:183], a[228:231]\n"
"s_add_u32 s57, s57, 0x4000\n"
"buffer_load_dwordx4 v[144:147], v250, s[52:55], s57 offen offset:0 sc1\n"
"v_mfma_f32_16x16x32_bf16 a[228:231], v[208:211], a[164:167], a[228:231]\n"
"buffer_load_dwordx4 v[148:151], v250, s[52:55], s57 offen offset:1024 sc1\n"
"v_mfma_f32_16x16x32_bf16 a[232:235], v[208:211], a[168:171], a[232:235]\n"
"v_mfma_f32_16x16x32_bf16 a[232:235], v[212:215], a[184:187], a[232:235]\n"
"v_mfma_f32_16x16x32_bf16 a[236:239], v[212:215], a[188:191], a[236:239]\n"
"ds_read_b64_tr_b16 v[212:213], v229 offset:15424\n"
"v_mfma_f32_16x16x32_bf16 a[236:239], v[208:211], a[172:175], a[236:239]\n"
"s_waitcnt lgkmcnt(5)\n"
"ds_read_b64_tr_b16 v[214:215], v229 offset:17984\n"
"ds_read_b64_tr_b16 v[208:209], v229 offset:10304\n"
"v_mfma_f32_16x16x32_bf16 a[240:243], v[192:195], a[160:163], a[240:243]\n"
"s_lshl_b32 s71, %[w], 1\n"
"ds_read_b64_tr_b16 v[210:211], v229 offset:12864\n"
"v_mfma_f32_16x16x32_bf16 a[240:243], v[196:199], a[176:179], a[240:243]\n"
"buffer_load_dwordx4 v[152:155], v250, s[52:55], s57 offen offset:2048 sc1\n"
"v_mfma_f32_16x16x32_bf16 a[244:247], v[196:199], a[180:183], a[244:247]\n"
"s_cmp_eq_u32 s71, s24\n"
"s_cselect_b32 s74, 1, 0\n"
"v_mfma_f32_16x16x32_bf16 a[244:247], v[192:195], a[164:167], a[244:247]\n"
"buffer_load_dwordx4 v[156:159], v250, s[52:55], s57 offen offset:3072 sc1\n"
"v_mfma_f32_16x16x32_bf16 a[248:251], v[192:195], a[168:171], a[248:251]\n"
"s_lshl_b32 s74, s74, 29\n"
"v_mfma_f32_16x16x32_bf16 a[248:251], v[196:199], a[184:187], a[248:251]\n"
"v_mfma_f32_16x16x32_bf16 a[252:255], v[196:199], a[188:191], a[252:255]\n"
"s_add_u32 s75, s74, 0x1000\n"
"ds_read_b64_tr_b16 v[196:197], v229 offset:15456\n"
"v_mfma_f32_16x16x32_bf16 a[252:255], v[192:195], a[172:175], a[252:255]\n"
"s_waitcnt lgkmcnt(5)\n"
"ds_read_b64_tr_b16 v[198:199], v229 offset:18016\n"
"ds_read_b64_tr_b16 v[192:193], v229 offset:10336\n"
"v_mfma_f32_16x16x32_bf16 v[64:67], v[200:203], a[160:163], v[64:67]\n"
"s_add_u32 s76, s74, 0x2000\n"
"s_mul_i32 s57, s67, 0x18000\n"
"ds_read_b64_tr_b16 v[194:195], v229 offset:12896\n"
"v_mfma_f32_16x16x32_bf16 v[64:67], v[204:207], a[176:179], v[64:67]\n"
"s_add_u32 s57, s57, 0x10000\n"
"buffer_load_dwordx4 v[160:163], v250, s[52:55], s57 offen offset:0 sc1\n"
"v_mfma_f32_16x16x32_bf16 v[68:71], v[204:207], a[180:183], v[68:71]\n"
"s_add_u32 s71, s71, 1\n"
"v_mfma_f32_16x16x32_bf16 v[68:71], v[200:203], a[164:167], v[68:71]\n"
"v_mfma_f32_16x16x32_bf16 v[72:75], v[200:203], a[168:171], v[72:75]\n"
"s_cmp_eq_u32 s71, s24\n"
"s_cselect_b32 s77, 1, 0\n"
"v_mfma_f32_16x16x32_bf16 v[72:75], v[204:207], a[184:187], v[72:75]\n"
"buffer_load_dwordx4 v[164:167], v250, s[52:55], s57 offen offset:1024 sc1\n"
"v_mfma_f32_16x16x32_bf16 v[76:79], v[204:207], a[188:191], v[76:79]\n"
"s_lshl_b32 s77, s77, 29\n"
"ds_read_b64_tr_b16 v[204:205], v229 offset:15488\n"
"v_mfma_f32_16x16x32_bf16 v[76:79], v[200:203], a[172:175], v[76:79]\n"
"s_waitcnt lgkmcnt(5)\n"
"ds_read_b64_tr_b16 v[206:207], v229 offset:18048\n"
"ds_read_b64_tr_b16 v[200:201], v229 offset:10368\n"
"v_mfma_f32_16x16x32_bf16 v[80:83], v[208:211], a[160:163], v[80:83]\n"
"s_add_u32 s77, s77, 0x14000\n"
"ds_read_b64_tr_b16 v[202:203], v229 offset:12928\n"
"v_mfma_f32_16x16x32_bf16 v[80:83], v[212:215], a[176:179], v[80:83]\n"
"v_mfma_f32_16x16x32_bf16 v[84:87], v[212:215], a[180:183], v[84:87]\n"
"s_add_u32 s78, s77, 0x1000\n"
"buffer_load_dwordx4 v[168:171], v250, s[52:55], s57 offen offset:2048 sc1\n"
"v_mfma_f32_16x16x32_bf16 v[84:87], v[208:211], a[164:167], v[84:87]\n"
"v_mfma_f32_16x16x32_bf16 v[88:91], v[208:211], a[168:171], v[88:91]\n"
"s_add_u32 s79, s77, 0x2000\n"
"v_mfma_f32_16x16x32_bf16 v[88:91], v[212:215], a[184:187], v[88:91]\n"
"v_mfma_f32_16x16x32_bf16 v[92:95], v[212:215], a[188:191], v[92:95]\n"
"ds_read_b64_tr_b16 v[212:213], v229 offset:25600\n"
"v_mfma_f32_16x16x32_bf16 v[92:95], v[208:211], a[172:175], v[92:95]\n"
"s_waitcnt lgkmcnt(5)\n"
"ds_read_b64_tr_b16 v[214:215], v229 offset:28160\n"
"ds_read_b64_tr_b16 v[208:209], v229 offset:20480\n"
"v_mfma_f32_16x16x32_bf16 v[96:99], v[192:195], a[160:163], v[96:99]\n"
"ds_read_b64_tr_b16 v[210:211], v229 offset:23040\n"
"v_mfma_f32_16x16x32_bf16 v[96:99], v[196:199], a[176:179], v[96:99]\n"
"buffer_load_dwordx4 v[172:175], v250, s[52:55], s57 offen offset:3072 sc1\n"
"v_mfma_f32_16x16x32_bf16 v[100:103], v[196:199], a[180:183], v[100:103]\n"
"v_mfma_f32_16x16x32_bf16 v[100:103], v[192:195], a[164:167], v[100:103]\n"
"v_mfma_f32_16x16x32_bf16 v[104:107], v[192:195], a[168:171], v[104:107]\n"
"s_add_u32 s57, s57, 0x4000\n"
"buffer_load_dwordx4 v[176:179], v250, s[52:55], s57 offen offset:0 sc1\n"
"v_mfma_f32_16x16x32_bf16 v[104:107], v[196:199], a[184:187], v[104:107]\n"
"v_mfma_f32_16x16x32_bf16 v[108:111], v[196:199], a[188:191], v[108:111]\n"
"ds_read_b64_tr_b16 v[196:197], v229 offset:25632\n"
"v_mfma_f32_16x16x32_bf16 v[108:111], v[192:195], a[172:175], v[108:111]\n"
"s_waitcnt lgkmcnt(5)\n"
"ds_read_b64_tr_b16 v[198:199], v229 offset:28192\n"
"ds_read_b64_tr_b16 v[192:193], v229 offset:20512\n"
"v_mfma_f32_16x16x32_bf16 v[112:115], v[200:203], a[160:163], v[112:115]\n"
"ds_read_b64_tr_b16 v[194:195], v229 offset:23072\n"
"v_mfma_f32_16x16x32_bf16 v[112:115], v[204:207], a[176:179], v[112:115]\n"
"buffer_load_dwordx4 v[180:183], v250, s[52:55], s57 offen offset:1024 sc1\n"
"v_mfma_f32_16x16x32_bf16 v[116:119], v[204:207], a[180:183], v[116:119]\n"
"v_mfma_f32_16x16x32_bf16 v[116:119], v[200:203], a[164:167], v[116:119]\n"
"v_mfma_f32_16x16x32_bf16 v[120:123], v[200:203], a[168:171], v[120:123]\n"
"v_mfma_f32_16x16x32_bf16 v[120:123], v[204:207], a[184:187], v[120:123]\n"
"s_waitcnt vmcnt(24)\n"
"v_mfma_f32_16x16x32_bf16 v[124:127], v[204:207], a[188:191], v[124:127]\n"
"ds_read_b64_tr_b16 v[204:205], v229 offset:25664\n"
"v_mfma_f32_16x16x32_bf16 v[124:127], v[200:203], a[172:175], v[124:127]\n"
"s_waitcnt lgkmcnt(5)\n"
"ds_read_b64_tr_b16 v[206:207], v229 offset:28224\n"
"ds_read_b64_tr_b16 v[200:201], v229 offset:20544\n"
"v_mfma_f32_16x16x32_bf16 a[0:3], v[208:211], v[32:35], a[0:3]\n"
"ds_read_b64_tr_b16 v[202:203], v229 offset:23104\n"
"v_mfma_f32_16x16x32_bf16 a[0:3], v[212:215], v[48:51], a[0:3]\n"
"buffer_load_dwordx4 v[184:187], v250, s[52:55], s57 offen offset:2048 sc1\n"
"v_mfma_f32_16x16x32_bf16 a[4:7], v[212:215], v[52:55], a[4:7]\n"
"v_mfma_f32_16x16x32_bf16 a[4:7], v[208:211], v[36:39], a[4:7]\n"
"buffer_load_dwordx4 v[188:191], v250, s[52:55], s57 offen offset:3072 sc1\n"
"v_mfma_f32_16x16x32_bf16 a[8:11], v[208:211], v[40:43], a[8:11]\n"
"v_mfma_f32_16x16x32_bf16 a[8:11], v[212:215], v[56:59], a[8:11]\n"
"v_mfma_f32_16x16x32_bf16 a[12:15], v[212:215], v[60:63], a[12:15]\n"
"ds_read_b64_tr_b16 v[212:213], v229 offset:25696\n"
"v_mfma_f32_16x16x32_bf16 a[12:15], v[208:211], v[44:47], a[12:15]\n"
"s_waitcnt lgkmcnt(5)\n"
"Ltp13%=:\n"
"s_cbranch_execz Ltrl0x3%=\n"
"ds_read_b64_tr_b16 v[214:215], v229 offset:28256\n"
"ds_read_b64_tr_b16 v[208:209], v229 offset:20576\n"
"v_mfma_f32_16x16x32_bf16 a[16:19], v[192:195], v[32:35], a[16:19]\n"
"s_mul_i32 s57, s69, 0x18000\n"
"ds_read_b64_tr_b16 v[210:211], v229 offset:23136\n"
"v_mfma_f32_16x16x32_bf16 a[16:19], v[196:199], v[48:51], a[16:19]\n"
"s_add_u32 s57, s57, 0x10000\n"
"buffer_load_dwordx4 v[0:3], v250, s[52:55], s57 offen offset:0 sc1\n"
"v_mfma_f32_16x16x32_bf16 a[20:23], v[196:199], v[52:55], a[20:23]\n"
"v_mfma_f32_16x16x32_bf16 a[20:23], v[192:195], v[36:39], a[20:23]\n"
"v_mfma_f32_16x16x32_bf16 a[24:27], v[192:195], v[40:43], a[24:27]\n"
"v_mfma_f32_16x16x32_bf16 a[24:27], v[196:199], v[56:59], a[24:27]\n"
"buffer_load_dwordx4 v[4:7], v250, s[52:55], s57 offen offset:1024 sc1\n"
"v_mfma_f32_16x16x32_bf16 a[28:31], v[196:199], v[60:63], a[28:31]\n"
"ds_read_b64_tr_b16 v[196:197], v229 offset:25728\n"
"v_mfma_f32_16x16x32_bf16 a[28:31], v[192:195], v[44:47], a[28:31]\n"
"s_waitcnt lgkmcnt(5)\n"
"ds_read_b64_tr_b16 v[198:199], v229 offset:28288\n"
"ds_read_b64_tr_b16 v[192:193], v229 offset:20608\n"
"v_mfma_f32_16x16x32_bf16 a[192:195], v[200:203], v[32:35], a[192:195]\n"
"ds_read_b64_tr_b16 v[194:195], v229 offset:23168\n"
"v_mfma_f32_16x16x32_bf16 a[192:195], v[204:207], v[48:51], a[192:195]\n"
"v_mfma_f32_16x16x32_bf16 a[196:199], v[204:207], v[52:55], a[196:199]\n"
"buffer_load_dwordx4 v[8:11], v250, s[52:55], s57 offen offset:2048 sc1\n"
"v_mfma_f32_16x16x32_bf16 a[196:199], v[200:203], v[36:39], a[196:199]\n"
"v_mfma_f32_16x16x32_bf16 a[200:203], v[200:203], v[40:43], a[200:203]\n"
"v_mfma_f32_16x16x32_bf16 a[200:203], v[204:207], v[56:59], a[200:203]\n"
"v_mfma_f32_16x16x32_bf16 a[204:207], v[204:207], v[60:63], a[204:207]\n"
"ds_read_b64_tr_b16 v[204:205], v229 offset:35840\n"
"v_mfma_f32_16x16x32_bf16 a[204:207], v[200:203], v[44:47], a[204:207]\n"
"s_waitcnt lgkmcnt(5)\n"
"ds_read_b64_tr_b16 v[206:207], v229 offset:38400\n"
"ds_read_b64_tr_b16 v[200:201], v229 offset:30720\n"
"v_mfma_f32_16x16x32_bf16 a[208:211], v[208:211], v[32:35], a[208:211]\n"
"ds_read_b64_tr_b16 v[202:203], v229 offset:33280\n"
"v_mfma_f32_16x16x32_bf16 a[208:211], v[212:215], v[48:51], a[208:211]\n"
"buffer_load_dwordx4 v[12:15], v250, s[52:55], s57 offen offset:3072 sc1\n"
"v_mfma_f32_16x16x32_bf16 a[212:215], v[212:215], v[52:55], a[212:215]\n"
"v_mfma_f32_16x16x32_bf16 a[212:215], v[208:211], v[36:39], a[212:215]\n"
"v_mfma_f32_16x16x32_bf16 a[216:219], v[208:211], v[40:43], a[216:219]\n"
"s_add_u32 s57, s57, 0x4000\n"
"buffer_load_dwordx4 v[16:19], v250, s[52:55], s57 offen offset:0 sc1\n"
"v_mfma_f32_16x16x32_bf16 a[216:219], v[212:215], v[56:59], a[216:219]\n"
"v_mfma_f32_16x16x32_bf16 a[220:223], v[212:215], v[60:63], a[220:223]\n"
"ds_read_b64_tr_b16 v[212:213], v229 offset:35872\n"
"v_mfma_f32_16x16x32_bf16 a[220:223], v[208:211], v[44:47], a[220:223]\n"
"s_waitcnt lgkmcnt(5)\n"
"ds_read_b64_tr_b16 v[214:215], v229 offset:38432\n"
"ds_read_b64_tr_b16 v[208:209], v229 offset:30752\n"
"v_mfma_f32_16x16x32_bf16 a[224:227], v[192:195], v[32:35], a[224:227]\n"
"ds_read_b64_tr_b16 v[210:211], v229 offset:33312\n"
"v_mfma_f32_16x16x32_bf16 a[224:227], v[196:199], v[48:51], a[224:227]\n"
"buffer_load_dwordx4 v[20:23], v250, s[52:55], s57 offen offset:1024 sc1\n"
"v_mfma_f32_16x16x32_bf16 a[228:231], v[196:199], v[52:55], a[228:231]\n"
"v_mfma_f32_16x16x32_bf16 a[228:231], v[192:195], v[36:39], a[228:231]\n"
"v_mfma_f32_16x16x32_bf16 a[232:235], v[192:195], v[40:43], a[232:235]\n"
"v_mfma_f32_16x16x32_bf16 a[232:235], v[196:199], v[56:59], a[232:235]\n"
"v_mfma_f32_16x16x32_bf16 a[236:239], v[196:199], v[60:63], a[236:239]\n"
"ds_read_b64_tr_b16 v[196:197], v229 offset:35904\n"
"v_mfma_f32_16x16x32_bf16 a[236:239], v[192:195], v[44:47], a[236:239]\n"
"s_waitcnt lgkmcnt(5)\n"
"ds_read_b64_tr_b16 v[198:199], v229 offset:38464\n"
"ds_read_b64_tr_b16 v[192:193], v229 offset:30784\n"
"v_mfma_f32_16x16x32_bf16 a[240:243], v[200:203], v[32:35], a[240:243]\n"
"ds_read_b64_tr_b16 v[194:195], v229 offset:33344\n"
"v_mfma_f32_16x16x32_bf16 a[240:243], v[204:207], v[48:51], a[240:243]\n"
"buffer_load_dwordx4 v[24:27], v250, s[52:55], s57 offen offset:2048 sc1\n"
"v_mfma_f32_16x16x32_bf16 a[244:247], v[204:207], v[52:55], a[244:247]\n"
"v_mfma_f32_16x16x32_bf16 a[244:247], v[200:203], v[36:39], a[244:247]\n"
"buffer_load_dwordx4 v[28:31], v250, s[52:55], s57 offen offset:3072 sc1\n"
"v_mfma_f32_16x16x32_bf16 a[248:251], v[200:203], v[40:43], a[248:251]\n"
"v_mfma_f32_16x16x32_bf16 a[248:251], v[204:207], v[56:59], a[248:251]\n"
"v_mfma_f32_16x16x32_bf16 a[252:255], v[204:207], v[60:63], a[252:255]\n"
"ds_read_b64_tr_b16 v[204:205], v229 offset:35936\n"
"v_mfma_f32_16x16x32_bf16 a[252:255], v[200:203], v[44:47], a[252:255]\n"
"s_waitcnt lgkmcnt(5)\n"
"ds_read_b64_tr_b16 v[206:207], v229 offset:38496\n"
"ds_read_b64_tr_b16 v[200:201], v229 offset:30816\n"
"v_mfma_f32_16x16x32_bf16 v[64:67], v[208:211], v[32:35], v[64:67]\n"
"ds_read_b64_tr_b16 v[202:203], v229 offset:33376\n"
"v_mfma_f32_16x16x32_bf16 v[64:67], v[212:215], v[48:51], v[64:67]\n"
"v_mfma_f32_16x16x32_bf16 v[68:71], v[212:215], v[52:55], v[68:71]\n"
"v_mfma_f32_16x16x32_bf16 v[68:71], v[208:211], v[36:39], v[68:71]\n"
"v_mfma_f32_16x16x32_bf16 v[72:75], v[208:211], v[40:43], v[72:75]\n"
"v_mfma_f32_16x16x32_bf16 v[72:75], v[212:215], v[56:59], v[72:75]\n"
"v_mfma_f32_16x16x32_bf16 v[76:79], v[212:215], v[60:63], v[76:79]\n"
"ds_read_b64_tr_b16 v[212:213], v229 offset:35968\n"
"v_mfma_f32_16x16x32_bf16 v[76:79], v[208:211], v[44:47], v[76:79]\n"
"s_waitcnt lgkmcnt(5)\n"
"ds_read_b64_tr_b16 v[214:215], v229 offset:38528\n"
"ds_read_b64_tr_b16 v[208:209], v229 offset:30848\n"
"v_mfma_f32_16x16x32_bf16 v[80:83], v[192:195], v[32:35], v[80:83]\n"
"ds_read_b64_tr_b16 v[210:211], v229 offset:33408\n"
"v_mfma_f32_16x16x32_bf16 v[80:83], v[196:199], v[48:51], v[80:83]\n"
"v_mfma_f32_16x16x32_bf16 v[84:87], v[196:199], v[52:55], v[84:87]\n"
"v_mfma_f32_16x16x32_bf16 v[84:87], v[192:195], v[36:39], v[84:87]\n"
"v_mfma_f32_16x16x32_bf16 v[88:91], v[192:195], v[40:43], v[88:91]\n"
"v_mfma_f32_16x16x32_bf16 v[88:91], v[196:199], v[56:59], v[88:91]\n"
"v_mfma_f32_16x16x32_bf16 v[92:95], v[196:199], v[60:63], v[92:95]\n"
"s_waitcnt vmcnt(24) lgkmcnt(0)\n"
"s_barrier\n"
"ds_read_b64_tr_b16 v[196:197], v229 offset:46080\n"
"v_mfma_f32_16x16x32_bf16 v[92:95], v[192:195], v[44:47], v[92:95]\n"
"ds_read_b64_tr_b16 v[198:199], v229 offset:48640\n"
"ds_read_b64_tr_b16 v[192:193], v229 offset:40960\n"
"v_mfma_f32_16x16x32_bf16 v[96:99], v[200:203], v[32:35], v[96:99]\n"
"ds_read_b64_tr_b16 v[194:195], v229 offset:43520\n"
"v_mfma_f32_16x16x32_bf16 v[96:99], v[204:207], v[48:51], v[96:99]\n"
"v_mfma_f32_16x16x32_bf16 v[100:103], v[204:207], v[52:55], v[100:103]\n"
"v_mfma_f32_16x16x32_bf16 v[100:103], v[200:203], v[36:39], v[100:103]\n"
"v_mfma_f32_16x16x32_bf16 v[104:107], v[200:203], v[40:43], v[104:107]\n"
"v_mfma_f32_16x16x32_bf16 v[104:107], v[204:207], v[56:59], v[104:107]\n"
"v_mfma_f32_16x16x32_bf16 v[108:111], v[204:207], v[60:63], v[108:111]\n"
"ds_read_b64_tr_b16 v[204:205], v227 offset:5120\n"
"v_mfma_f32_16x16x32_bf16 v[108:111], v[200:203], v[44:47], v[108:111]\n"
"ds_read_b64_tr_b16 v[206:207], v227 offset:7680\n"
"ds_read_b64_tr_b16 v[200:201], v227 offset:0\n"
"v_mfma_f32_16x16x32_bf16 v[112:115], v[208:211], v[32:35], v[112:115]\n"
"ds_read_b64_tr_b16 v[202:203], v227 offset:2560\n"
"v_mfma_f32_16x16x32_bf16 v[112:115], v[212:215], v[48:51], v[112:115]\n"
"v_mfma_f32_16x16x32_bf16 v[116:119], v[212:215], v[52:55], v[116:119]\n"
"v_mfma_f32_16x16x32_bf16 v[116:119], v[208:211], v[36:39], v[116:119]\n"
"v_mfma_f32_16x16x32_bf16 v[120:123], v[208:211], v[40:43], v[120:123]\n"
"v_mfma_f32_16x16x32_bf16 v[120:123], v[212:215], v[56:59], v[120:123]\n"
"s_waitcnt vmcnt(0)\n"
"v_mfma_f32_16x16x32_bf16 v[124:127], v[212:215], v[60:63], v[124:127]\n"
"ds_read_b64_tr_b16 v[212:213], v227 offset:25600\n"
"v_mfma_f32_16x16x32_bf16 v[124:127], v[208:211], v[44:47], v[124:127]\n"
"s_waitcnt lgkmcnt(5)\n"
"ds_read_b64_tr_b16 v[214:215], v227 offset:28160\n"
"ds_read_b64_tr_b16 v[208:209], v227 offset:20480\n"
"v_mfma_f32_16x16x32_bf16 a[0:3], v[192:195], v[128:131], a[0:3]\n"
"ds_read_b64_tr_b16 v[210:211], v227 offset:23040\n"
"v_mfma_f32_16x16x32_bf16 a[0:3], v[196:199], v[144:147], a[0:3]\n"
"v_mfma_f32_16x16x32_bf16 a[4:7], v[196:199], v[148:151], a[4:7]\n"
"v_mfma_f32_16x16x32_bf16 a[4:7], v[192:195], v[132:135], a[4:7]\n"
"v_mfma_f32_16x16x32_bf16 a[8:11], v[192:195], v[136:139], a[8:11]\n"
"v_mfma_f32_16x16x32_bf16 a[8:11], v[196:199], v[152:155], a[8:11]\n"
"v_mfma_f32_16x16x32_bf16 a[12:15], v[196:199], v[156:159], a[12:15]\n"
"ds_read_b64_tr_b16 v[196:197], v229 offset:46112\n"
"v_mfma_f32_16x16x32_bf16 a[12:15], v[192:195], v[140:143], a[12:15]\n"
"s_waitcnt lgkmcnt(5)\n"
"ds_read_b64_tr_b16 v[198:199], v229 offset:48672\n"
"ds_read_b64_tr_b16 v[192:193], v229 offset:40992\n"
"v_mfma_f32_16x16x32_bf16 a[0:3], v[200:203], v[160:163], a[0:3]\n"
"ds_read_b64_tr_b16 v[194:195], v229 offset:43552\n"
"v_mfma_f32_16x16x32_bf16 a[0:3], v[204:207], v[176:179], a[0:3]\n"
"v_mfma_f32_16x16x32_bf16 a[4:7], v[204:207], v[180:183], a[4:7]\n"
"v_mfma_f32_16x16x32_bf16 a[4:7], v[200:203], v[164:167], a[4:7]\n"
"v_mfma_f32_16x16x32_bf16 a[8:11], v[200:203], v[168:171], a[8:11]\n"
"v_mfma_f32_16x16x32_bf16 a[8:11], v[204:207], v[184:187], a[8:11]\n"
"v_mfma_f32_16x16x32_bf16 a[12:15], v[204:207], v[188:191], a[12:15]\n"
"ds_read_b64_tr_b16 v[204:205], v227 offset:5152\n"
"v_mfma_f32_16x16x32_bf16 a[12:15], v[200:203], v[172:175], a[12:15]\n"
"s_waitcnt lgkmcnt(5)\n"
"ds_read_b64_tr_b16 v[206:207], v227 offset:7712\n"
"ds_read_b64_tr_b16 v[200:201], v227 offset:32\n"
"v_mfma_f32_16x16x32_bf16 a[0:3], v[208:211], v[0:3], a[0:3]\n"
"ds_read_b64_tr_b16 v[202:203], v227 offset:2592\n"
"v_mfma_f32_16x16x32_bf16 a[0:3], v[212:215], v[16:19], a[0:3]\n"
"v_mfma_f32_16x16x32_bf16 a[4:7], v[212:215], v[20:23], a[4:7]\n"
"v_mfma_f32_16x16x32_bf16 a[4:7], v[208:211], v[4:7], a[4:7]\n"
"v_mfma_f32_16x16x32_bf16 a[8:11], v[208:211], v[8:11], a[8:11]\n"
"v_accvgpr_read_b32 v32, a0\n"
"v_accvgpr_read_b32 v33, a1\n"
"v_mfma_f32_16x16x32_bf16 a[8:11], v[212:215], v[24:27], a[8:11]\n"
"v_accvgpr_read_b32 v34, a2\n"
"v_accvgpr_read_b32 v35, a3\n"
"v_mfma_f32_16x16x32_bf16 a[12:15], v[212:215], v[28:31], a[12:15]\n"
"ds_read_b64_tr_b16 v[212:213], v227 offset:25632\n"
"v_cvt_pk_bf16_f32 v32, v32, v33\n"
"v_mfma_f32_16x16x32_bf16 a[12:15], v[208:211], v[12:15], a[12:15]\n"
"s_waitcnt lgkmcnt(5)\n"
"ds_read_b64_tr_b16 v[214:215], v227 offset:28192\n"
"ds_read_b64_tr_b16 v[208:209], v227 offset:20512\n"
"v_mfma_f32_16x16x32_bf16 a[16:19], v[192:195], v[128:131], a[16:19]\n"
"ds_read_b64_tr_b16 v[210:211], v227 offset:23072\n"
"v_cvt_pk_bf16_f32 v33, v34, v35\n"
"v_mfma_f32_16x16x32_bf16 a[16:19], v[196:199], v[144:147], a[16:19]\n"
"v_accvgpr_read_b32 v40, a4\n"
"v_accvgpr_read_b32 v41, a5\n"
"v_mfma_f32_16x16x32_bf16 a[20:23], v[196:199], v[148:151], a[20:23]\n"
"v_accvgpr_read_b32 v42, a6\n"
"v_accvgpr_read_b32 v43, a7\n"
"v_mfma_f32_16x16x32_bf16 a[20:23], v[192:195], v[132:135], a[20:23]\n"
"v_cvt_pk_bf16_f32 v40, v40, v41\n"
"v_cvt_pk_bf16_f32 v41, v42, v43\n"
"v_mfma_f32_16x16x32_bf16 a[24:27], v[192:195], v[136:139], a[24:27]\n"
"v_accvgpr_read_b32 v48, a8\n"
"v_accvgpr_read_b32 v49, a9\n"
"v_mfma_f32_16x16x32_bf16 a[24:27], v[196:199], v[152:155], a[24:27]\n"
"v_accvgpr_read_b32 v50, a10\n"
"v_accvgpr_read_b32 v51, a11\n"
"v_mfma_f32_16x16x32_bf16 a[28:31], v[196:199], v[156:159], a[28:31]\n"
"ds_read_b64_tr_b16 v[196:197], v229 offset:46144\n"
"v_cvt_pk_bf16_f32 v48, v48, v49\n"
"v_mfma_f32_16x16x32_bf16 a[28:31], v[192:195], v[140:143], a[28:31]\n"
"s_waitcnt lgkmcnt(5)\n"
"ds_read_b64_tr_b16 v[198:199], v229 offset:48704\n"
"ds_read_b64_tr_b16 v[192:193], v229 offset:41024\n"
"v_mfma_f32_16x16x32_bf16 a[16:19], v[200:203], v[160:163], a[16:19]\n"
"ds_read_b64_tr_b16 v[194:195], v229 offset:43584\n"
"v_cvt_pk_bf16_f32 v49, v50, v51\n"
"v_mfma_f32_16x16x32_bf16 a[16:19], v[204:207], v[176:179], a[16:19]\n"
"v_accvgpr_read_b32 v56, a12\n"
"v_accvgpr_read_b32 v57, a13\n"
"v_mfma_f32_16x16x32_bf16 a[20:23], v[204:207], v[180:183], a[20:23]\n"
"v_accvgpr_read_b32 v58, a14\n"
"v_accvgpr_read_b32 v59, a15\n"
"v_mfma_f32_16x16x32_bf16 a[20:23], v[200:203], v[164:167], a[20:23]\n"
"v_cvt_pk_bf16_f32 v56, v56, v57\n"
"v_cvt_pk_bf16_f32 v57, v58, v59\n"
"v_mfma_f32_16x16x32_bf16 a[24:27], v[200:203], v[168:171], a[24:27]\n"
"v_mfma_f32_16x16x32_bf16 a[24:27], v[204:207], v[184:187], a[24:27]\n"
"v_mfma_f32_16x16x32_bf16 a[28:31], v[204:207], v[188:191], a[28:31]\n"
"ds_read_b64_tr_b16 v[204:205], v227 offset:5184\n"
"v_mfma_f32_16x16x32_bf16 a[28:31], v[200:203], v[172:175], a[28:31]\n"
"s_waitcnt lgkmcnt(5)\n"
"ds_read_b64_tr_b16 v[206:207], v227 offset:7744\n"
"ds_read_b64_tr_b16 v[200:201], v227 offset:64\n"
"v_mfma_f32_16x16x32_bf16 a[16:19], v[208:211], v[0:3], a[16:19]\n"
"ds_read_b64_tr_b16 v[202:203], v227 offset:2624\n"
"v_mfma_f32_16x16x32_bf16 a[16:19], v[212:215], v[16:19], a[16:19]\n"
"v_mfma_f32_16x16x32_bf16 a[20:23], v[212:215], v[20:23], a[20:23]\n"
"v_mfma_f32_16x16x32_bf16 a[20:23], v[208:211], v[4:7], a[20:23]\n"
"v_mfma_f32_16x16x32_bf16 a[24:27], v[208:211], v[8:11], a[24:27]\n"
"v_accvgpr_read_b32 v36, a16\n"
"v_accvgpr_read_b32 v37, a17\n"
"v_mfma_f32_16x16x32_bf16 a[24:27], v[212:215], v[24:27], a[24:27]\n"
"v_accvgpr_read_b32 v38, a18\n"
"v_accvgpr_read_b32 v39, a19\n"
"v_mfma_f32_16x16x32_bf16 a[28:31], v[212:215], v[28:31], a[28:31]\n"
"ds_read_b64_tr_b16 v[212:213], v227 offset:25664\n"
"v_cvt_pk_bf16_f32 v34, v36, v37\n"
"v_mfma_f32_16x16x32_bf16 a[28:31], v[208:211], v[12:15], a[28:31]\n"
"s_waitcnt lgkmcnt(5)\n"
"ds_read_b64_tr_b16 v[214:215], v227 offset:28224\n"
"ds_read_b64_tr_b16 v[208:209], v227 offset:20544\n"
"v_mfma_f32_16x16x32_bf16 a[192:195], v[192:195], v[128:131], a[192:195]\n"
"ds_read_b64_tr_b16 v[210:211], v227 offset:23104\n"
"v_cvt_pk_bf16_f32 v35, v38, v39\n"
"v_mfma_f32_16x16x32_bf16 a[192:195], v[196:199], v[144:147], a[192:195]\n"
"buffer_store_dwordx4 v[32:35], v236, s[36:39], s74 offen offset:0 sc1\n"
"v_mfma_f32_16x16x32_bf16 a[196:199], v[196:199], v[148:151], a[196:199]\n"
"v_accvgpr_read_b32 v44, a20\n"
"v_accvgpr_read_b32 v45, a21\n"
"v_mfma_f32_16x16x32_bf16 a[196:199], v[192:195], v[132:135], a[196:199]\n"
"v_accvgpr_read_b32 v46, a22\n"
"v_accvgpr_read_b32 v47, a23\n"
"v_mfma_f32_16x16x32_bf16 a[200:203], v[192:195], v[136:139], a[200:203]\n"
"v_cvt_pk_bf16_f32 v42, v44, v45\n"
"v_cvt_pk_bf16_f32 v43, v46, v47\n"
"v_mfma_f32_16x16x32_bf16 a[200:203], v[196:199], v[152:155], a[200:203]\n"
"buffer_store_dwordx4 v[40:43], v236, s[36:39], s74 offen offset:1024 sc1\n"
"v_mfma_f32_16x16x32_bf16 a[204:207], v[196:199], v[156:159], a[204:207]\n"
"ds_read_b64_tr_b16 v[196:197], v229 offset:46176\n"
"v_accvgpr_read_b32 v52, a24\n"
"v_mfma_f32_16x16x32_bf16 a[204:207], v[192:195], v[140:143], a[204:207]\n"
"s_waitcnt lgkmcnt(5)\n"
"ds_read_b64_tr_b16 v[198:199], v229 offset:48736\n"
"ds_read_b64_tr_b16 v[192:193], v229 offset:41056\n"
"v_mfma_f32_16x16x32_bf16 a[192:195], v[200:203], v[160:163], a[192:195]\n"
"ds_read_b64_tr_b16 v[194:195], v229 offset:43616\n"
"v_accvgpr_read_b32 v53, a25\n"
"v_mfma_f32_16x16x32_bf16 a[192:195], v[204:207], v[176:179], a[192:195]\n"
"v_accvgpr_read_b32 v54, a26\n"
"v_accvgpr_read_b32 v55, a27\n"
"v_mfma_f32_16x16x32_bf16 a[196:199], v[204:207], v[180:183], a[196:199]\n"
"v_cvt_pk_bf16_f32 v50, v52, v53\n"
"v_cvt_pk_bf16_f32 v51, v54, v55\n"
"v_mfma_f32_16x16x32_bf16 a[196:199], v[200:203], v[164:167], a[196:199]\n"
"buffer_store_dwordx4 v[48:51], v236, s[36:39], s77 offen offset:0 sc1\n"
"v_mfma_f32_16x16x32_bf16 a[200:203], v[200:203], v[168:171], a[200:203]\n"
"v_accvgpr_read_b32 v60, a28\n"
"v_accvgpr_read_b32 v61, a29\n"
"v_mfma_f32_16x16x32_bf16 a[200:203], v[204:207], v[184:187], a[200:203]\n"
"v_accvgpr_read_b32 v62, a30\n"
"v_accvgpr_read_b32 v63, a31\n"
"v_mfma_f32_16x16x32_bf16 a[204:207], v[204:207], v[188:191], a[204:207]\n"
"ds_read_b64_tr_b16 v[204:205], v227 offset:5216\n"
"v_cvt_pk_bf16_f32 v58, v60, v61\n"
"v_mfma_f32_16x16x32_bf16 a[204:207], v[200:203], v[172:175], a[204:207]\n"
"s_waitcnt lgkmcnt(5)\n"
"ds_read_b64_tr_b16 v[206:207], v227 offset:7776\n"
"ds_read_b64_tr_b16 v[200:201], v227 offset:96\n"
"v_mfma_f32_16x16x32_bf16 a[192:195], v[208:211], v[0:3], a[192:195]\n"
"ds_read_b64_tr_b16 v[202:203], v227 offset:2656\n"
"v_cvt_pk_bf16_f32 v59, v62, v63\n"
"v_mfma_f32_16x16x32_bf16 a[192:195], v[212:215], v[16:19], a[192:195]\n"
"buffer_store_dwordx4 v[56:59], v236, s[36:39], s77 offen offset:1024 sc1\n"
"v_mfma_f32_16x16x32_bf16 a[196:199], v[212:215], v[20:23], a[196:199]\n"
"v_mfma_f32_16x16x32_bf16 a[196:199], v[208:211], v[4:7], a[196:199]\n"
"v_mfma_f32_16x16x32_bf16 a[200:203], v[208:211], v[8:11], a[200:203]\n"
"v_accvgpr_read_b32 v32, a192\n"
"v_accvgpr_read_b32 v33, a193\n"
"v_mfma_f32_16x16x32_bf16 a[200:203], v[212:215], v[24:27], a[200:203]\n"
"v_accvgpr_read_b32 v34, a194\n"
"v_accvgpr_read_b32 v35, a195\n"
"v_mfma_f32_16x16x32_bf16 a[204:207], v[212:215], v[28:31], a[204:207]\n"
"ds_read_b64_tr_b16 v[212:213], v227 offset:25696\n"
"v_cvt_pk_bf16_f32 v32, v32, v33\n"
"v_mfma_f32_16x16x32_bf16 a[204:207], v[208:211], v[12:15], a[204:207]\n"
"s_waitcnt lgkmcnt(5)\n"
"ds_read_b64_tr_b16 v[214:215], v227 offset:28256\n"
"ds_read_b64_tr_b16 v[208:209], v227 offset:20576\n"
"v_mfma_f32_16x16x32_bf16 a[208:211], v[192:195], v[128:131], a[208:211]\n"
"ds_read_b64_tr_b16 v[210:211], v227 offset:23136\n"
"v_cvt_pk_bf16_f32 v33, v34, v35\n"
"v_mfma_f32_16x16x32_bf16 a[208:211], v[196:199], v[144:147], a[208:211]\n"
"v_accvgpr_read_b32 v40, a196\n"
"v_accvgpr_read_b32 v41, a197\n"
"v_mfma_f32_16x16x32_bf16 a[212:215], v[196:199], v[148:151], a[212:215]\n"
"v_accvgpr_read_b32 v42, a198\n"
"v_accvgpr_read_b32 v43, a199\n"
"v_mfma_f32_16x16x32_bf16 a[212:215], v[192:195], v[132:135], a[212:215]\n"
"v_cvt_pk_bf16_f32 v40, v40, v41\n"
"v_cvt_pk_bf16_f32 v41, v42, v43\n"
"v_mfma_f32_16x16x32_bf16 a[216:219], v[192:195], v[136:139], a[216:219]\n"
"v_accvgpr_read_b32 v48, a200\n"
"v_accvgpr_read_b32 v49, a201\n"
"v_mfma_f32_16x16x32_bf16 a[216:219], v[196:199], v[152:155], a[216:219]\n"
"v_accvgpr_read_b32 v50, a202\n"
"v_accvgpr_read_b32 v51, a203\n"
"v_mfma_f32_16x16x32_bf16 a[220:223], v[196:199], v[156:159], a[220:223]\n"
"ds_read_b64_tr_b16 v[196:197], v229 offset:46208\n"
"v_cvt_pk_bf16_f32 v48, v48, v49\n"
"v_mfma_f32_16x16x32_bf16 a[220:223], v[192:195], v[140:143], a[220:223]\n"
"s_waitcnt lgkmcnt(5)\n"
"ds_read_b64_tr_b16 v[198:199], v229 offset:48768\n"
"ds_read_b64_tr_b16 v[192:193], v229 offset:41088\n"
"v_mfma_f32_16x16x32_bf16 a[208:211], v[200:203], v[160:163], a[208:211]\n"
"ds_read_b64_tr_b16 v[194:195], v229 offset:43648\n"
"v_cvt_pk_bf16_f32 v49, v50, v51\n"
"v_mfma_f32_16x16x32_bf16 a[208:211], v[204:207], v[176:179], a[208:211]\n"
"v_accvgpr_read_b32 v56, a204\n"
"v_accvgpr_read_b32 v57, a205\n"
"v_mfma_f32_16x16x32_bf16 a[212:215], v[204:207], v[180:183], a[212:215]\n"
"v_accvgpr_read_b32 v58, a206\n"
"v_accvgpr_read_b32 v59, a207\n"
"v_mfma_f32_16x16x32_bf16 a[212:215], v[200:203], v[164:167], a[212:215]\n"
"v_cvt_pk_bf16_f32 v56, v56, v57\n"
"v_cvt_pk_bf16_f32 v57, v58, v59\n"
"v_mfma_f32_16x16x32_bf16 a[216:219], v[200:203], v[168:171], a[216:219]\n"
"v_mfma_f32_16x16x32_bf16 a[216:219], v[204:207], v[184:187], a[216:219]\n"
"v_mfma_f32_16x16x32_bf16 a[220:223], v[204:207], v[188:191], a[220:223]\n"
"ds_read_b64_tr_b16 v[204:205], v227 offset:5248\n"
"v_mfma_f32_16x16x32_bf16 a[220:223], v[200:203], v[172:175], a[220:223]\n"
"s_waitcnt lgkmcnt(5)\n"
"ds_read_b64_tr_b16 v[206:207], v227 offset:7808\n"
"ds_read_b64_tr_b16 v[200:201], v227 offset:128\n"
"v_mfma_f32_16x16x32_bf16 a[208:211], v[208:211], v[0:3], a[208:211]\n"
"ds_read_b64_tr_b16 v[202:203], v227 offset:2688\n"
"v_mfma_f32_16x16x32_bf16 a[208:211], v[212:215], v[16:19], a[208:211]\n"
"v_mfma_f32_16x16x32_bf16 a[212:215], v[212:215], v[20:23], a[212:215]\n"
"v_mfma_f32_16x16x32_bf16 a[212:215], v[208:211], v[4:7], a[212:215]\n"
"v_mfma_f32_16x16x32_bf16 a[216:219], v[208:211], v[8:11], a[216:219]\n"
"v_accvgpr_read_b32 v36, a208\n"
"v_accvgpr_read_b32 v37, a209\n"
"v_mfma_f32_16x16x32_bf16 a[216:219], v[212:215], v[24:27], a[216:219]\n"
"v_accvgpr_read_b32 v38, a210\n"
"v_accvgpr_read_b32 v39, a211\n"
"v_mfma_f32_16x16x32_bf16 a[220:223], v[212:215], v[28:31], a[220:223]\n"
"ds_read_b64_tr_b16 v[212:213], v227 offset:25728\n"
"v_cvt_pk_bf16_f32 v34, v36, v37\n"
"v_mfma_f32_16x16x32_bf16 a[220:223], v[208:211], v[12:15], a[220:223]\n"
"s_waitcnt lgkmcnt(5)\n"
"ds_read_b64_tr_b16 v[214:215], v227 offset:28288\n"
"ds_read_b64_tr_b16 v[208:209], v227 offset:20608\n"
"v_mfma_f32_16x16x32_bf16 a[224:227], v[192:195], v[128:131], a[224:227]\n"
"ds_read_b64_tr_b16 v[210:211], v227 offset:23168\n"
"v_cvt_pk_bf16_f32 v35, v38, v39\n"
"v_mfma_f32_16x16x32_bf16 a[224:227], v[196:199], v[144:147], a[224:227]\n"
"buffer_store_dwordx4 v[32:35], v236, s[36:39], s74 offen offset:2048 sc1\n"
"v_mfma_f32_16x16x32_bf16 a[228:231], v[196:199], v[148:151], a[228:231]\n"
"v_accvgpr_read_b32 v44, a212\n"
"v_accvgpr_read_b32 v45, a213\n"
"v_mfma_f32_16x16x32_bf16 a[228:231], v[192:195], v[132:135], a[228:231]\n"
"v_accvgpr_read_b32 v46, a214\n"
"v_accvgpr_read_b32 v47, a215\n"
"v_mfma_f32_16x16x32_bf16 a[232:235], v[192:195], v[136:139], a[232:235]\n"
"v_cvt_pk_bf16_f32 v42, v44, v45\n"
"v_cvt_pk_bf16_f32 v43, v46, v47\n"
"v_mfma_f32_16x16x32_bf16 a[232:235], v[196:199], v[152:155], a[232:235]\n"
"buffer_store_dwordx4 v[40:43], v236, s[36:39], s74 offen offset:3072 sc1\n"
"v_mfma_f32_16x16x32_bf16 a[236:239], v[196:199], v[156:159], a[236:239]\n"
"ds_read_b64_tr_b16 v[196:197], v229 offset:56320\n"
"v_accvgpr_read_b32 v52, a216\n"
"v_mfma_f32_16x16x32_bf16 a[236:239], v[192:195], v[140:143], a[236:239]\n"
"s_waitcnt lgkmcnt(5)\n"
"ds_read_b64_tr_b16 v[198:199], v229 offset:58880\n"
"ds_read_b64_tr_b16 v[192:193], v229 offset:51200\n"
"v_mfma_f32_16x16x32_bf16 a[224:227], v[200:203], v[160:163], a[224:227]\n"
"ds_read_b64_tr_b16 v[194:195], v229 offset:53760\n"
"v_accvgpr_read_b32 v53, a217\n"
"v_mfma_f32_16x16x32_bf16 a[224:227], v[204:207], v[176:179], a[224:227]\n"
"v_accvgpr_read_b32 v54, a218\n"
"v_accvgpr_read_b32 v55, a219\n"
"v_mfma_f32_16x16x32_bf16 a[228:231], v[204:207], v[180:183], a[228:231]\n"
"v_cvt_pk_bf16_f32 v50, v52, v53\n"
"v_cvt_pk_bf16_f32 v51, v54, v55\n"
"v_mfma_f32_16x16x32_bf16 a[228:231], v[200:203], v[164:167], a[228:231]\n"
"buffer_store_dwordx4 v[48:51], v236, s[36:39], s77 offen offset:2048 sc1\n"
"v_mfma_f32_16x16x32_bf16 a[232:235], v[200:203], v[168:171], a[232:235]\n"
"v_accvgpr_read_b32 v60, a220\n"
"v_accvgpr_read_b32 v61, a221\n"
"v_mfma_f32_16x16x32_bf16 a[232:235], v[204:207], v[184:187], a[232:235]\n"
"v_accvgpr_read_b32 v62, a222\n"
"v_accvgpr_read_b32 v63, a223\n"
"v_mfma_f32_16x16x32_bf16 a[236:239], v[204:207], v[188:191], a[236:239]\n"
"ds_read_b64_tr_b16 v[204:205], v227 offset:15360\n"
"v_cvt_pk_bf16_f32 v58, v60, v61\n"
"v_mfma_f32_16x16x32_bf16 a[236:239], v[200:203], v[172:175], a[236:239]\n"
"s_waitcnt lgkmcnt(5)\n"
"ds_read_b64_tr_b16 v[206:207], v227 offset:17920\n"
"ds_read_b64_tr_b16 v[200:201], v227 offset:10240\n"
"v_mfma_f32_16x16x32_bf16 a[224:227], v[208:211], v[0:3], a[224:227]\n"
"ds_read_b64_tr_b16 v[202:203], v227 offset:12800\n"
"v_cvt_pk_bf16_f32 v59, v62, v63\n"
"v_mfma_f32_16x16x32_bf16 a[224:227], v[212:215], v[16:19], a[224:227]\n"
"buffer_store_dwordx4 v[56:59], v236, s[36:39], s77 offen offset:3072 sc1\n"
"v_mfma_f32_16x16x32_bf16 a[228:231], v[212:215], v[20:23], a[228:231]\n"
"v_mfma_f32_16x16x32_bf16 a[228:231], v[208:211], v[4:7], a[228:231]\n"
"v_mfma_f32_16x16x32_bf16 a[232:235], v[208:211], v[8:11], a[232:235]\n"
"v_accvgpr_read_b32 v32, a224\n"
"v_accvgpr_read_b32 v33, a225\n"
"v_mfma_f32_16x16x32_bf16 a[232:235], v[212:215], v[24:27], a[232:235]\n"
"v_accvgpr_read_b32 v34, a226\n"
"v_accvgpr_read_b32 v35, a227\n"
"v_mfma_f32_16x16x32_bf16 a[236:239], v[212:215], v[28:31], a[236:239]\n"
"ds_read_b64_tr_b16 v[212:213], v227 offset:35840\n"
"v_cvt_pk_bf16_f32 v32, v32, v33\n"
"v_mfma_f32_16x16x32_bf16 a[236:239], v[208:211], v[12:15], a[236:239]\n"
"s_waitcnt lgkmcnt(5)\n"
"ds_read_b64_tr_b16 v[214:215], v227 offset:38400\n"
"ds_read_b64_tr_b16 v[208:209], v227 offset:30720\n"
"v_mfma_f32_16x16x32_bf16 a[240:243], v[192:195], v[128:131], a[240:243]\n"
"ds_read_b64_tr_b16 v[210:211], v227 offset:33280\n"
"v_cvt_pk_bf16_f32 v33, v34, v35\n"
"v_mfma_f32_16x16x32_bf16 a[240:243], v[196:199], v[144:147], a[240:243]\n"
"v_accvgpr_read_b32 v40, a228\n"
"v_accvgpr_read_b32 v41, a229\n"
"v_mfma_f32_16x16x32_bf16 a[244:247], v[196:199], v[148:151], a[244:247]\n"
"v_accvgpr_read_b32 v42, a230\n"
"v_accvgpr_read_b32 v43, a231\n"
"v_mfma_f32_16x16x32_bf16 a[244:247], v[192:195], v[132:135], a[244:247]\n"
"v_cvt_pk_bf16_f32 v40, v40, v41\n"
"v_cvt_pk_bf16_f32 v41, v42, v43\n"
"v_mfma_f32_16x16x32_bf16 a[248:251], v[192:195], v[136:139], a[248:251]\n"
"v_accvgpr_read_b32 v48, a232\n"
"v_accvgpr_read_b32 v49, a233\n"
"v_mfma_f32_16x16x32_bf16 a[248:251], v[196:199], v[152:155], a[248:251]\n"
"v_accvgpr_read_b32 v50, a234\n"
"v_accvgpr_read_b32 v51, a235\n"
"v_mfma_f32_16x16x32_bf16 a[252:255], v[196:199], v[156:159], a[252:255]\n"
"ds_read_b64_tr_b16 v[196:197], v229 offset:56352\n"
"v_cvt_pk_bf16_f32 v48, v48, v49\n"
"v_mfma_f32_16x16x32_bf16 a[252:255], v[192:195], v[140:143], a[252:255]\n"
"s_waitcnt lgkmcnt(5)\n"
"ds_read_b64_tr_b16 v[198:199], v229 offset:58912\n"
"ds_read_b64_tr_b16 v[192:193], v229 offset:51232\n"
"v_mfma_f32_16x16x32_bf16 a[240:243], v[200:203], v[160:163], a[240:243]\n"
"ds_read_b64_tr_b16 v[194:195], v229 offset:53792\n"
"v_cvt_pk_bf16_f32 v49, v50, v51\n"
"v_mfma_f32_16x16x32_bf16 a[240:243], v[204:207], v[176:179], a[240:243]\n"
"v_accvgpr_read_b32 v56, a236\n"
"v_accvgpr_read_b32 v57, a237\n"
"v_mfma_f32_16x16x32_bf16 a[244:247], v[204:207], v[180:183], a[244:247]\n"
"v_accvgpr_read_b32 v58, a238\n"
"v_accvgpr_read_b32 v59, a239\n"
"v_mfma_f32_16x16x32_bf16 a[244:247], v[200:203], v[164:167], a[244:247]\n"
"v_cvt_pk_bf16_f32 v56, v56, v57\n"
"v_cvt_pk_bf16_f32 v57, v58, v59\n"
"v_mfma_f32_16x16x32_bf16 a[248:251], v[200:203], v[168:171], a[248:251]\n"
"v_mfma_f32_16x16x32_bf16 a[248:251], v[204:207], v[184:187], a[248:251]\n"
"v_mfma_f32_16x16x32_bf16 a[252:255], v[204:207], v[188:191], a[252:255]\n"
"ds_read_b64_tr_b16 v[204:205], v227 offset:15392\n"
"v_mfma_f32_16x16x32_bf16 a[252:255], v[200:203], v[172:175], a[252:255]\n"
"s_waitcnt lgkmcnt(5)\n"
"ds_read_b64_tr_b16 v[206:207], v227 offset:17952\n"
"ds_read_b64_tr_b16 v[200:201], v227 offset:10272\n"
"v_mfma_f32_16x16x32_bf16 a[240:243], v[208:211], v[0:3], a[240:243]\n"
"ds_read_b64_tr_b16 v[202:203], v227 offset:12832\n"
"v_mfma_f32_16x16x32_bf16 a[240:243], v[212:215], v[16:19], a[240:243]\n"
"v_mfma_f32_16x16x32_bf16 a[244:247], v[212:215], v[20:23], a[244:247]\n"
"v_mfma_f32_16x16x32_bf16 a[244:247], v[208:211], v[4:7], a[244:247]\n"
"v_mfma_f32_16x16x32_bf16 a[248:251], v[208:211], v[8:11], a[248:251]\n"
"v_accvgpr_read_b32 v36, a240\n"
"v_accvgpr_read_b32 v37, a241\n"
"v_mfma_f32_16x16x32_bf16 a[248:251], v[212:215], v[24:27], a[248:251]\n"
"v_accvgpr_read_b32 v38, a242\n"
"v_accvgpr_read_b32 v39, a243\n"
"v_mfma_f32_16x16x32_bf16 a[252:255], v[212:215], v[28:31], a[252:255]\n"
"ds_read_b64_tr_b16 v[212:213], v227 offset:35872\n"
"v_cvt_pk_bf16_f32 v34, v36, v37\n"
"v_mfma_f32_16x16x32_bf16 a[252:255], v[208:211], v[12:15], a[252:255]\n"
"s_waitcnt lgkmcnt(5)\n"
"ds_read_b64_tr_b16 v[214:215], v227 offset:38432\n"
"ds_read_b64_tr_b16 v[208:209], v227 offset:30752\n"
"v_mfma_f32_16x16x32_bf16 v[64:67], v[192:195], v[128:131], v[64:67]\n"
"ds_read_b64_tr_b16 v[210:211], v227 offset:33312\n"
"v_cvt_pk_bf16_f32 v35, v38, v39\n"
"v_mfma_f32_16x16x32_bf16 v[64:67], v[196:199], v[144:147], v[64:67]\n"
"buffer_store_dwordx4 v[32:35], v236, s[36:39], s75 offen offset:0 sc1\n"
"v_mfma_f32_16x16x32_bf16 v[68:71], v[196:199], v[148:151], v[68:71]\n"
"v_accvgpr_read_b32 v44, a244\n"
"v_accvgpr_read_b32 v45, a245\n"
"v_mfma_f32_16x16x32_bf16 v[68:71], v[192:195], v[132:135], v[68:71]\n"
"v_accvgpr_read_b32 v46, a246\n"
"v_accvgpr_read_b32 v47, a247\n"
"v_mfma_f32_16x16x32_bf16 v[72:75], v[192:195], v[136:139], v[72:75]\n"
"v_cvt_pk_bf16_f32 v42, v44, v45\n"
"v_cvt_pk_bf16_f32 v43, v46, v47\n"
"v_mfma_f32_16x16x32_bf16 v[72:75], v[196:199], v[152:155], v[72:75]\n"
"buffer_store_dwordx4 v[40:43], v236, s[36:39], s75 offen offset:1024 sc1\n"
"v_mfma_f32_16x16x32_bf16 v[76:79], v[196:199], v[156:159], v[76:79]\n"
"ds_read_b64_tr_b16 v[196:197], v229 offset:56384\n"
"v_accvgpr_read_b32 v52, a248\n"
"v_mfma_f32_16x16x32_bf16 v[76:79], v[192:195], v[140:143], v[76:79]\n"
"s_waitcnt lgkmcnt(5)\n"
"ds_read_b64_tr_b16 v[198:199], v229 offset:58944\n"
"ds_read_b64_tr_b16 v[192:193], v229 offset:51264\n"
"v_mfma_f32_16x16x32_bf16 v[64:67], v[200:203], v[160:163], v[64:67]\n"
"ds_read_b64_tr_b16 v[194:195], v229 offset:53824\n"
"v_accvgpr_read_b32 v53, a249\n"
"v_mfma_f32_16x16x32_bf16 v[64:67], v[204:207], v[176:179], v[64:67]\n"
"v_accvgpr_read_b32 v54, a250\n"
"Ltp14%=:\n"
"s_cbranch_execz Ltrl0x3%=\n"
"v_accvgpr_read_b32 v55, a251\n"
"v_mfma_f32_16x16x32_bf16 v[68:71], v[204:207], v[180:183], v[68:71]\n"
"v_cvt_pk_bf16_f32 v50, v52, v53\n"
"v_cvt_pk_bf16_f32 v51, v54, v55\n"
"v_mfma_f32_16x16x32_bf16 v[68:71], v[200:203], v[164:167], v[68:71]\n"
"buffer_store_dwordx4 v[48:51], v236, s[36:39], s78 offen offset:0 sc1\n"
"v_mfma_f32_16x16x32_bf16 v[72:75], v[200:203], v[168:171], v[72:75]\n"
"v_accvgpr_read_b32 v60, a252\n"
"v_accvgpr_read_b32 v61, a253\n"
"v_mfma_f32_16x16x32_bf16 v[72:75], v[204:207], v[184:187], v[72:75]\n"
"v_accvgpr_read_b32 v62, a254\n"
"v_accvgpr_read_b32 v63, a255\n"
"v_mfma_f32_16x16x32_bf16 v[76:79], v[204:207], v[188:191], v[76:79]\n"
"ds_read_b64_tr_b16 v[204:205], v227 offset:15424\n"
"v_cvt_pk_bf16_f32 v58, v60, v61\n"
"v_mfma_f32_16x16x32_bf16 v[76:79], v[200:203], v[172:175], v[76:79]\n"
"s_waitcnt lgkmcnt(5)\n"
"ds_read_b64_tr_b16 v[206:207], v227 offset:17984\n"
"ds_read_b64_tr_b16 v[200:201], v227 offset:10304\n"
"v_mfma_f32_16x16x32_bf16 v[64:67], v[208:211], v[0:3], v[64:67]\n"
"ds_read_b64_tr_b16 v[202:203], v227 offset:12864\n"
"v_cvt_pk_bf16_f32 v59, v62, v63\n"
"v_mfma_f32_16x16x32_bf16 v[64:67], v[212:215], v[16:19], v[64:67]\n"
"buffer_store_dwordx4 v[56:59], v236, s[36:39], s78 offen offset:1024 sc1\n"
"v_mfma_f32_16x16x32_bf16 v[68:71], v[212:215], v[20:23], v[68:71]\n"
"v_mfma_f32_16x16x32_bf16 v[68:71], v[208:211], v[4:7], v[68:71]\n"
"v_mfma_f32_16x16x32_bf16 v[72:75], v[208:211], v[8:11], v[72:75]\n"
"v_cvt_pk_bf16_f32 v64, v64, v65\n"
"v_cvt_pk_bf16_f32 v65, v66, v67\n"
"v_mfma_f32_16x16x32_bf16 v[72:75], v[212:215], v[24:27], v[72:75]\n"
"v_mfma_f32_16x16x32_bf16 v[76:79], v[212:215], v[28:31], v[76:79]\n"
"ds_read_b64_tr_b16 v[212:213], v227 offset:35904\n"
"v_cvt_pk_bf16_f32 v68, v68, v69\n"
"v_mfma_f32_16x16x32_bf16 v[76:79], v[208:211], v[12:15], v[76:79]\n"
"s_waitcnt lgkmcnt(5)\n"
"ds_read_b64_tr_b16 v[214:215], v227 offset:38464\n"
"ds_read_b64_tr_b16 v[208:209], v227 offset:30784\n"
"v_mfma_f32_16x16x32_bf16 v[80:83], v[192:195], v[128:131], v[80:83]\n"
"ds_read_b64_tr_b16 v[210:211], v227 offset:33344\n"
"v_cvt_pk_bf16_f32 v69, v70, v71\n"
"v_mfma_f32_16x16x32_bf16 v[80:83], v[196:199], v[144:147], v[80:83]\n"
"v_cvt_pk_bf16_f32 v72, v72, v73\n"
"v_cvt_pk_bf16_f32 v73, v74, v75\n"
"v_mfma_f32_16x16x32_bf16 v[84:87], v[196:199], v[148:151], v[84:87]\n"
"v_cvt_pk_bf16_f32 v76, v76, v77\n"
"v_cvt_pk_bf16_f32 v77, v78, v79\n"
"v_mfma_f32_16x16x32_bf16 v[84:87], v[192:195], v[132:135], v[84:87]\n"
"v_mfma_f32_16x16x32_bf16 v[88:91], v[192:195], v[136:139], v[88:91]\n"
"v_mfma_f32_16x16x32_bf16 v[88:91], v[196:199], v[152:155], v[88:91]\n"
"v_mfma_f32_16x16x32_bf16 v[92:95], v[196:199], v[156:159], v[92:95]\n"
"ds_read_b64_tr_b16 v[196:197], v229 offset:56416\n"
"v_mfma_f32_16x16x32_bf16 v[92:95], v[192:195], v[140:143], v[92:95]\n"
"s_waitcnt lgkmcnt(5)\n"
"ds_read_b64_tr_b16 v[198:199], v229 offset:58976\n"
"ds_read_b64_tr_b16 v[192:193], v229 offset:51296\n"
"v_mfma_f32_16x16x32_bf16 v[80:83], v[200:203], v[160:163], v[80:83]\n"
"ds_read_b64_tr_b16 v[194:195], v229 offset:53856\n"
"v_mfma_f32_16x16x32_bf16 v[80:83], v[204:207], v[176:179], v[80:83]\n"
"v_mfma_f32_16x16x32_bf16 v[84:87], v[204:207], v[180:183], v[84:87]\n"
"v_mfma_f32_16x16x32_bf16 v[84:87], v[200:203], v[164:167], v[84:87]\n"
"v_mfma_f32_16x16x32_bf16 v[88:91], v[200:203], v[168:171], v[88:91]\n"
"v_mfma_f32_16x16x32_bf16 v[88:91], v[204:207], v[184:187], v[88:91]\n"
"v_mfma_f32_16x16x32_bf16 v[92:95], v[204:207], v[188:191], v[92:95]\n"
"ds_read_b64_tr_b16 v[204:205], v227 offset:15456\n"
"v_mfma_f32_16x16x32_bf16 v[92:95], v[200:203], v[172:175], v[92:95]\n"
"s_waitcnt lgkmcnt(5)\n"
"ds_read_b64_tr_b16 v[206:207], v227 offset:18016\n"
"ds_read_b64_tr_b16 v[200:201], v227 offset:10336\n"
"v_mfma_f32_16x16x32_bf16 v[80:83], v[208:211], v[0:3], v[80:83]\n"
"ds_read_b64_tr_b16 v[202:203], v227 offset:12896\n"
"v_mfma_f32_16x16x32_bf16 v[80:83], v[212:215], v[16:19], v[80:83]\n"
"v_mfma_f32_16x16x32_bf16 v[84:87], v[212:215], v[20:23], v[84:87]\n"
"s_lshl_b32 s82, s24, 2\n"
"v_mfma_f32_16x16x32_bf16 v[84:87], v[208:211], v[4:7], v[84:87]\n"
"v_mfma_f32_16x16x32_bf16 v[88:91], v[208:211], v[8:11], v[88:91]\n"
"s_add_u32 s82, s82, s61\n"
"v_cvt_pk_bf16_f32 v66, v80, v81\n"
"v_cvt_pk_bf16_f32 v67, v82, v83\n"
"v_mfma_f32_16x16x32_bf16 v[88:91], v[212:215], v[24:27], v[88:91]\n"
"v_mfma_f32_16x16x32_bf16 v[92:95], v[212:215], v[28:31], v[92:95]\n"
"ds_read_b64_tr_b16 v[212:213], v227 offset:35936\n"
"v_cvt_pk_bf16_f32 v70, v84, v85\n"
"v_mfma_f32_16x16x32_bf16 v[92:95], v[208:211], v[12:15], v[92:95]\n"
"s_waitcnt lgkmcnt(5)\n"
"ds_read_b64_tr_b16 v[214:215], v227 offset:38496\n"
"ds_read_b64_tr_b16 v[208:209], v227 offset:30816\n"
"v_mfma_f32_16x16x32_bf16 v[96:99], v[192:195], v[128:131], v[96:99]\n"
"ds_read_b64_tr_b16 v[210:211], v227 offset:33376\n"
"v_cvt_pk_bf16_f32 v71, v86, v87\n"
"v_mfma_f32_16x16x32_bf16 v[96:99], v[196:199], v[144:147], v[96:99]\n"
"v_cvt_pk_bf16_f32 v74, v88, v89\n"
"v_cvt_pk_bf16_f32 v75, v90, v91\n"
"v_mfma_f32_16x16x32_bf16 v[100:103], v[196:199], v[148:151], v[100:103]\n"
"s_waitcnt vmcnt(0) lgkmcnt(0)\n"
"s_barrier\n"
"buffer_store_dword v253, v239, s[96:99], s82 offen offset:0 sc1\n"
"v_mfma_f32_16x16x32_bf16 v[100:103], v[192:195], v[132:135], v[100:103]\n"
"buffer_store_dwordx4 v[64:67], v236, s[36:39], s75 offen offset:2048 sc1\n"
"v_mfma_f32_16x16x32_bf16 v[104:107], v[192:195], v[136:139], v[104:107]\n"
"buffer_store_dwordx4 v[68:71], v236, s[36:39], s75 offen offset:3072 sc1\n"
"v_mfma_f32_16x16x32_bf16 v[104:107], v[196:199], v[152:155], v[104:107]\n"
"buffer_store_dwordx4 v[72:75], v236, s[36:39], s78 offen offset:2048 sc1\n"
"v_mfma_f32_16x16x32_bf16 v[108:111], v[196:199], v[156:159], v[108:111]\n"
"ds_read_b64_tr_b16 v[196:197], v229 offset:56448\n"
"v_cvt_pk_bf16_f32 v78, v92, v93\n"
"v_mfma_f32_16x16x32_bf16 v[108:111], v[192:195], v[140:143], v[108:111]\n"
"ds_read_b64_tr_b16 v[198:199], v229 offset:59008\n"
"ds_read_b64_tr_b16 v[192:193], v229 offset:51328\n"
"v_mfma_f32_16x16x32_bf16 v[96:99], v[200:203], v[160:163], v[96:99]\n"
"ds_read_b64_tr_b16 v[194:195], v229 offset:53888\n"
"v_cvt_pk_bf16_f32 v79, v94, v95\n"
"v_mfma_f32_16x16x32_bf16 v[96:99], v[204:207], v[176:179], v[96:99]\n"
"buffer_store_dwordx4 v[76:79], v236, s[36:39], s78 offen offset:3072 sc1\n"
"v_mfma_f32_16x16x32_bf16 v[100:103], v[204:207], v[180:183], v[100:103]\n"
"v_mfma_f32_16x16x32_bf16 v[100:103], v[200:203], v[164:167], v[100:103]\n"
"v_mfma_f32_16x16x32_bf16 v[104:107], v[200:203], v[168:171], v[104:107]\n"
"v_mfma_f32_16x16x32_bf16 v[104:107], v[204:207], v[184:187], v[104:107]\n"
"v_mfma_f32_16x16x32_bf16 v[108:111], v[204:207], v[188:191], v[108:111]\n"
"ds_read_b64_tr_b16 v[204:205], v227 offset:15488\n"
"v_mfma_f32_16x16x32_bf16 v[108:111], v[200:203], v[172:175], v[108:111]\n"
"ds_read_b64_tr_b16 v[206:207], v227 offset:18048\n"
"ds_read_b64_tr_b16 v[200:201], v227 offset:10368\n"
"v_mfma_f32_16x16x32_bf16 v[96:99], v[208:211], v[0:3], v[96:99]\n"
"ds_read_b64_tr_b16 v[202:203], v227 offset:12928\n"
"v_mfma_f32_16x16x32_bf16 v[96:99], v[212:215], v[16:19], v[96:99]\n"
"v_mfma_f32_16x16x32_bf16 v[100:103], v[212:215], v[20:23], v[100:103]\n"
"v_mfma_f32_16x16x32_bf16 v[100:103], v[208:211], v[4:7], v[100:103]\n"
"v_mfma_f32_16x16x32_bf16 v[104:107], v[208:211], v[8:11], v[104:107]\n"
"v_cvt_pk_bf16_f32 v96, v96, v97\n"
"v_cvt_pk_bf16_f32 v97, v98, v99\n"
"v_mfma_f32_16x16x32_bf16 v[104:107], v[212:215], v[24:27], v[104:107]\n"
"v_mfma_f32_16x16x32_bf16 v[108:111], v[212:215], v[28:31], v[108:111]\n"
"ds_read_b64_tr_b16 v[212:213], v227 offset:35968\n"
"v_cvt_pk_bf16_f32 v100, v100, v101\n"
"v_mfma_f32_16x16x32_bf16 v[108:111], v[208:211], v[12:15], v[108:111]\n"
"s_waitcnt lgkmcnt(5)\n"
"ds_read_b64_tr_b16 v[214:215], v227 offset:38528\n"
"ds_read_b64_tr_b16 v[208:209], v227 offset:30848\n"
"v_mfma_f32_16x16x32_bf16 v[112:115], v[192:195], v[128:131], v[112:115]\n"
"ds_read_b64_tr_b16 v[210:211], v227 offset:33408\n"
"v_cvt_pk_bf16_f32 v101, v102, v103\n"
"v_mfma_f32_16x16x32_bf16 v[112:115], v[196:199], v[144:147], v[112:115]\n"
"v_cvt_pk_bf16_f32 v104, v104, v105\n"
"v_cvt_pk_bf16_f32 v105, v106, v107\n"
"v_mfma_f32_16x16x32_bf16 v[116:119], v[196:199], v[148:151], v[116:119]\n"
"v_cvt_pk_bf16_f32 v108, v108, v109\n"
"v_cvt_pk_bf16_f32 v109, v110, v111\n"
"v_mfma_f32_16x16x32_bf16 v[116:119], v[192:195], v[132:135], v[116:119]\n"
"v_mfma_f32_16x16x32_bf16 v[120:123], v[192:195], v[136:139], v[120:123]\n"
"v_mfma_f32_16x16x32_bf16 v[120:123], v[196:199], v[152:155], v[120:123]\n"
"v_mfma_f32_16x16x32_bf16 v[124:127], v[196:199], v[156:159], v[124:127]\n"
"v_mfma_f32_16x16x32_bf16 v[124:127], v[192:195], v[140:143], v[124:127]\n"
"s_waitcnt lgkmcnt(4)\n"
"v_mfma_f32_16x16x32_bf16 v[112:115], v[200:203], v[160:163], v[112:115]\n"
"v_mfma_f32_16x16x32_bf16 v[112:115], v[204:207], v[176:179], v[112:115]\n"
"v_mfma_f32_16x16x32_bf16 v[116:119], v[204:207], v[180:183], v[116:119]\n"
"v_mfma_f32_16x16x32_bf16 v[116:119], v[200:203], v[164:167], v[116:119]\n"
"v_mfma_f32_16x16x32_bf16 v[120:123], v[200:203], v[168:171], v[120:123]\n"
"v_mfma_f32_16x16x32_bf16 v[120:123], v[204:207], v[184:187], v[120:123]\n"
"v_mfma_f32_16x16x32_bf16 v[124:127], v[204:207], v[188:191], v[124:127]\n"
"v_mfma_f32_16x16x32_bf16 v[124:127], v[200:203], v[172:175], v[124:127]\n"
"s_waitcnt lgkmcnt(0)\n"
"v_mfma_f32_16x16x32_bf16 v[112:115], v[208:211], v[0:3], v[112:115]\n"
"v_mfma_f32_16x16x32_bf16 v[112:115], v[212:215], v[16:19], v[112:115]\n"
"v_mfma_f32_16x16x32_bf16 v[116:119], v[212:215], v[20:23], v[116:119]\n"
"v_mfma_f32_16x16x32_bf16 v[116:119], v[208:211], v[4:7], v[116:119]\n"
"v_mfma_f32_16x16x32_bf16 v[120:123], v[208:211], v[8:11], v[120:123]\n"
"v_cvt_pk_bf16_f32 v98, v112, v113\n"
"v_cvt_pk_bf16_f32 v99, v114, v115\n"
"v_mfma_f32_16x16x32_bf16 v[120:123], v[212:215], v[24:27], v[120:123]\n"
"buffer_store_dwordx4 v[96:99], v236, s[36:39], s76 offen offset:0 sc1\n"
"v_mfma_f32_16x16x32_bf16 v[124:127], v[212:215], v[28:31], v[124:127]\n"
"v_cvt_pk_bf16_f32 v102, v116, v117\n"
"v_cvt_pk_bf16_f32 v103, v118, v119\n"
"v_mfma_f32_16x16x32_bf16 v[124:127], v[208:211], v[12:15], v[124:127]\n"
"buffer_store_dwordx4 v[100:103], v236, s[36:39], s76 offen offset:1024 sc1\n"
"v_cvt_pk_bf16_f32 v106, v120, v121\n"
"v_cvt_pk_bf16_f32 v107, v122, v123\n"
"buffer_store_dwordx4 v[104:107], v236, s[36:39], s79 offen offset:0 sc1\n"
"s_nop 4\n"
"v_cvt_pk_bf16_f32 v110, v124, v125\n"
"v_cvt_pk_bf16_f32 v111, v126, v127\n"
"buffer_store_dwordx4 v[108:111], v236, s[36:39], s79 offen offset:1024 sc1\n"
"buffer_load_dword v255, v240, s[96:99], s61 offen offset:0 sc1\n"
"s_bitcmp1_b32 s74, 29\n"
"s_cbranch_scc0 Lkoe0%=\n"
"ds_write_b128 v220, a[0:3] offset:40960\n"
"ds_write_b128 v220, a[16:19] offset:51200\n"
"ds_write_b128 v220, a[4:7] offset:41984\n"
"ds_write_b128 v220, a[20:23] offset:52224\n"
"ds_write_b128 v220, a[192:195] offset:43008\n"
"ds_write_b128 v220, a[208:211] offset:53248\n"
"ds_write_b128 v220, a[196:199] offset:44032\n"
"ds_write_b128 v220, a[212:215] offset:54272\n"
"ds_write_b128 v220, a[224:227] offset:45056\n"
"ds_write_b128 v220, a[240:243] offset:55296\n"
"ds_write_b128 v220, a[228:231] offset:46080\n"
"ds_write_b128 v220, a[244:247] offset:56320\n"
"ds_write_b128 v220, v[64:67] offset:47104\n"
"ds_write_b128 v220, v[68:71] offset:48128\n"
"ds_write_b128 v220, v[96:99] offset:49152\n"
"ds_write_b128 v220, v[100:103] offset:50176\n"
"Lkoe0%=:\n"
"s_bitcmp1_b32 s77, 29\n"
"s_cbranch_scc0 Lkoe1%=\n"
"ds_write_b128 v220, a[8:11] offset:40960\n"
"ds_write_b128 v220, a[24:27] offset:51200\n"
"ds_write_b128 v220, a[12:15] offset:41984\n"
"ds_write_b128 v220, a[28:31] offset:52224\n"
"ds_write_b128 v220, a[200:203] offset:43008\n"
"ds_write_b128 v220, a[216:219] offset:53248\n"
"ds_write_b128 v220, a[204:207] offset:44032\n"
"ds_write_b128 v220, a[220:223] offset:54272\n"
"ds_write_b128 v220, a[232:235] offset:45056\n"
"ds_write_b128 v220, a[248:251] offset:55296\n"
"ds_write_b128 v220, a[236:239] offset:46080\n"
"ds_write_b128 v220, a[252:255] offset:56320\n"
"ds_write_b128 v220, v[72:75] offset:47104\n"
"ds_write_b128 v220, v[76:79] offset:48128\n"
"ds_write_b128 v220, v[104:107] offset:49152\n"
"ds_write_b128 v220, v[108:111] offset:50176\n"
"Lkoe1%=:\n"
"Lkre%=:\n"
"s_and_b32 s58, %[w], 1\n"
"s_lshl_b32 s80, s58, 11\n"
"s_sub_u32 s80, 0x1000, s80\n"
"s_lshl_b32 s81, s58, 6\n"
"s_sub_u32 s81, 0x80, s81\n"
"v_mbcnt_lo_u32_b32 v217, -1, 0\n"
"v_mbcnt_hi_u32_b32 v217, -1, v217\n"
"v_bfe_u32 v218, v217, 3, 1\n"
"v_add_u32 v251, s80, v237\n"
"v_add_u32 v216, s81, v238\n"
"s_mul_i32 s71, s24, 0x14000\n"
"s_sub_u32 s71, 0xa000, s71\n"
"v_add_u32 v208, s71, v237\n"
"v_add_u32 v209, s71, v251\n"
"v_mov_b32 v219, 0x7ff00000\n"
"v_cmp_eq_u32 vcc, s58, v218\n"
"s_nop 1\n"
"v_cndmask_b32 v251, v219, v251, vcc\n"
"v_cndmask_b32 v216, v219, v216, vcc\n"
"s_lshl_b32 s81, 0x100, s24\n"
"s_andn2_b32 s81, 0xff00, s81\n"
"s_mov_b32 s80, 0xffff0000\n"
"s_add_u32 s8, s24, 1\n"
"s_and_b32 s8, s8, 7\n"
"s_mul_i32 s8, s8, 0x2800\n"
"s_add_u32 s9, s24, 2\n"
"s_and_b32 s9, s9, 7\n"
"s_mul_i32 s9, s9, 0x2800\n"
"s_add_u32 s10, s24, 3\n"
"s_and_b32 s10, s10, 7\n"
"s_mul_i32 s10, s10, 0x2800\n"
"s_add_u32 s11, s24, 4\n"
"s_and_b32 s11, s11, 7\n"
"s_mul_i32 s11, s11, 0x2800\n"
"s_add_u32 s12, s24, 5\n"
"s_and_b32 s12, s12, 7\n"
"s_mul_i32 s12, s12, 0x2800\n"
"s_add_u32 s13, s24, 6\n"
"s_and_b32 s13, s13, 7\n"
"s_mul_i32 s13, s13, 0x2800\n"
"s_add_u32 s14, s24, 7\n"
"s_and_b32 s14, s14, 7\n"
"s_mul_i32 s14, s14, 0x2800\n"
"v_add_u32 v210, 0x1800, v237\n"
"s_waitcnt vmcnt(0) lgkmcnt(0)\n"
"s_barrier\n"
"buffer_store_dword v253, v239, s[96:99], s82 offen offset:32 sc1\n"
"ds_read_b128 v[0:3], v208 offset:0\n"
"ds_read_b128 v[8:11], v209 offset:0\n"
"ds_read_b128 v[4:7], v208 offset:10240\n"
"ds_read_b128 v[12:15], v209 offset:10240\n"
"ds_read_b128 v[96:99], v208 offset:6144\n"
"v_cmp_ne_u32 vcc, v255, v253\n"
"s_nop 1\n"
"s_and_b32 s66, vcc_lo, 0xff\n"
"s_cbranch_scc1 Lspa%=\n"
"Lrta%=:\n"
"buffer_load_dwordx4 v[36:39], v237, s[36:39], s8 offen offset:0 sc1\n"
"buffer_load_dwordx4 v[40:43], v237, s[36:39], s9 offen offset:0 sc1\n"
"buffer_load_dwordx4 v[44:47], v237, s[36:39], s10 offen offset:0 sc1\n"
"buffer_load_dwordx4 v[48:51], v237, s[36:39], s11 offen offset:0 sc1\n"
"buffer_load_dwordx4 v[52:55], v237, s[36:39], s12 offen offset:0 sc1\n"
"buffer_load_dwordx4 v[56:59], v237, s[36:39], s13 offen offset:0 sc1\n"
"buffer_load_dwordx4 v[60:63], v237, s[36:39], s14 offen offset:0 sc1\n"
"buffer_load_dwordx4 v[68:71], v251, s[36:39], s8 offen offset:0 sc1\n"
"buffer_load_dwordx4 v[72:75], v251, s[36:39], s9 offen offset:0 sc1\n"
"buffer_load_dwordx4 v[76:79], v251, s[36:39], s10 offen offset:0 sc1\n"
"buffer_load_dwordx4 v[80:83], v251, s[36:39], s11 offen offset:0 sc1\n"
"buffer_load_dwordx4 v[84:87], v251, s[36:39], s12 offen offset:0 sc1\n"
"buffer_load_dwordx4 v[88:91], v251, s[36:39], s13 offen offset:0 sc1\n"
"buffer_load_dwordx4 v[92:95], v251, s[36:39], s14 offen offset:0 sc1\n"
"s_and_b32 s66, vcc_lo, s81\n"
"s_cbranch_scc0 Lpd%=\n"
"s_cmp_lg_u32 %[w], 0\n"
"s_cbranch_scc1 Lpd%=\n"
"s_mov_b32 s56, 0x400000\n"
"buffer_load_dword v200, v240, s[96:99], s61 offen sc1\n"
"s_sleep 2\n"
"buffer_load_dword v201, v240, s[96:99], s61 offen sc1\n"
"s_sleep 2\n"
"Lpl%=:\n"
"buffer_load_dword v202, v240, s[96:99], s61 offen sc1\n"
"s_sleep 2\n"
"s_waitcnt vmcnt(2)\n"
"v_cmp_ne_u32 vcc, v200, v253\n"
"s_nop 1\n"
"s_and_b32 s66, vcc_lo, s81\n"
"s_cbranch_scc0 Lpd%=\n"
"buffer_load_dword v200, v240, s[96:99], s61 offen sc1\n"
"s_sleep 2\n"
"s_waitcnt vmcnt(2)\n"
"v_cmp_ne_u32 vcc, v201, v253\n"
"s_nop 1\n"
"s_and_b32 s66, vcc_lo, s81\n"
"s_cbranch_scc0 Lpd%=\n"
"buffer_load_dword v201, v240, s[96:99], s61 offen sc1\n"
"s_sleep 2\n"
"s_waitcnt vmcnt(2)\n"
"v_cmp_ne_u32 vcc, v202, v253\n"
"s_nop 1\n"
"s_and_b32 s66, vcc_lo, s81\n"
"s_cbranch_scc0 Lpd%=\n"
"s_sub_u32 s56, s56, 1\n"
"s_cbranch_scc0 Lpl%=\n"
"Lpd%=:\n"
"s_barrier\n"
"buffer_load_dwordx4 v[100:103], v210, s[36:39], s8 offen offset:0 sc1\n"
"buffer_load_dwordx4 v[104:107], v210, s[36:39], s9 offen offset:0 sc1\n"
"buffer_load_dwordx4 v[108:111], v210, s[36:39], s10 offen offset:0 sc1\n"
"buffer_load_dwordx4 v[112:115], v210, s[36:39], s11 offen offset:0 sc1\n"
"buffer_load_dwordx4 v[116:119], v210, s[36:39], s12 offen offset:0 sc1\n"
"buffer_load_dwordx4 v[120:123], v210, s[36:39], s13 offen offset:0 sc1\n"
"buffer_load_dwordx4 v[124:127], v210, s[36:39], s14 offen offset:0 sc1\n"
"s_cmp_eq_u32 s82, 0x500\n"
"s_cselect_b32 s58, 0, 0x3ff80000\n"
"buffer_store_dword v253, v239, s[96:99], s58 offen offset:0 sc1\n"
"s_waitcnt lgkmcnt(2)\n"
"s_waitcnt vmcnt(21)\n"
"v_lshlrev_b32 v136, 16, v36\n"
"v_and_b32 v137, s80, v36\n"
"v_lshlrev_b32 v138, 16, v37\n"
"v_and_b32 v139, s80, v37\n"
"v_lshlrev_b32 v140, 16, v38\n"
"v_and_b32 v141, s80, v38\n"
"v_lshlrev_b32 v142, 16, v39\n"
"v_and_b32 v143, s80, v39\n"
"v_pk_add_f32 v[0:1], v[0:1], v[136:137]\n"
"v_pk_add_f32 v[2:3], v[2:3], v[138:139]\n"
"v_pk_add_f32 v[4:5], v[4:5], v[140:141]\n"
"v_pk_add_f32 v[6:7], v[6:7], v[142:143]\n"
"s_waitcnt vmcnt(20)\n"
"v_lshlrev_b32 v128, 16, v40\n"
"v_and_b32 v129, s80, v40\n"
"v_lshlrev_b32 v130, 16, v41\n"
"v_and_b32 v131, s80, v41\n"
"v_lshlrev_b32 v132, 16, v42\n"
"v_and_b32 v133, s80, v42\n"
"v_lshlrev_b32 v134, 16, v43\n"
"v_and_b32 v135, s80, v43\n"
"v_pk_add_f32 v[0:1], v[0:1], v[128:129]\n"
"v_pk_add_f32 v[2:3], v[2:3], v[130:131]\n"
"v_pk_add_f32 v[4:5], v[4:5], v[132:133]\n"
"v_pk_add_f32 v[6:7], v[6:7], v[134:135]\n"
"s_waitcnt vmcnt(19)\n"
"v_lshlrev_b32 v136, 16, v44\n"
"v_and_b32 v137, s80, v44\n"
"v_lshlrev_b32 v138, 16, v45\n"
"v_and_b32 v139, s80, v45\n"
"v_lshlrev_b32 v140, 16, v46\n"
"v_and_b32 v141, s80, v46\n"
"v_lshlrev_b32 v142, 16, v47\n"
"v_and_b32 v143, s80, v47\n"
"v_pk_add_f32 v[0:1], v[0:1], v[136:137]\n"
"v_pk_add_f32 v[2:3], v[2:3], v[138:139]\n"
"v_pk_add_f32 v[4:5], v[4:5], v[140:141]\n"
"v_pk_add_f32 v[6:7], v[6:7], v[142:143]\n"
"s_waitcnt vmcnt(18)\n"
"v_lshlrev_b32 v128, 16, v48\n"
"v_and_b32 v129, s80, v48\n"
"v_lshlrev_b32 v130, 16, v49\n"
"v_and_b32 v131, s80, v49\n"
"v_lshlrev_b32 v132, 16, v50\n"
"v_and_b32 v133, s80, v50\n"
"v_lshlrev_b32 v134, 16, v51\n"
"v_and_b32 v135, s80, v51\n"
"v_pk_add_f32 v[0:1], v[0:1], v[128:129]\n"
"v_pk_add_f32 v[2:3], v[2:3], v[130:131]\n"
"v_pk_add_f32 v[4:5], v[4:5], v[132:133]\n"
"v_pk_add_f32 v[6:7], v[6:7], v[134:135]\n"
"s_waitcnt vmcnt(17)\n"
"v_lshlrev_b32 v136, 16, v52\n"
"v_and_b32 v137, s80, v52\n"
"v_lshlrev_b32 v138, 16, v53\n"
"v_and_b32 v139, s80, v53\n"
"v_lshlrev_b32 v140, 16, v54\n"
"v_and_b32 v141, s80, v54\n"
"v_lshlrev_b32 v142, 16, v55\n"
"v_and_b32 v143, s80, v55\n"
"v_pk_add_f32 v[0:1], v[0:1], v[136:137]\n"
"v_pk_add_f32 v[2:3], v[2:3], v[138:139]\n"
"v_pk_add_f32 v[4:5], v[4:5], v[140:141]\n"
"v_pk_add_f32 v[6:7], v[6:7], v[142:143]\n"
"s_waitcnt vmcnt(16)\n"
"v_lshlrev_b32 v128, 16, v56\n"
"v_and_b32 v129, s80, v56\n"
"v_lshlrev_b32 v130, 16, v57\n"
"v_and_b32 v131, s80, v57\n"
"v_lshlrev_b32 v132, 16, v58\n"
"v_and_b32 v133, s80, v58\n"
"v_lshlrev_b32 v134, 16, v59\n"
"v_and_b32 v135, s80, v59\n"
"v_pk_add_f32 v[0:1], v[0:1], v[128:129]\n"
"v_pk_add_f32 v[2:3], v[2:3], v[130:131]\n"
"v_pk_add_f32 v[4:5], v[4:5], v[132:133]\n"
"v_pk_add_f32 v[6:7], v[6:7], v[134:135]\n"
"s_waitcnt vmcnt(15)\n"
"v_lshlrev_b32 v136, 16, v60\n"
"v_and_b32 v137, s80, v60\n"
"v_lshlrev_b32 v138, 16, v61\n"
"v_and_b32 v139, s80, v61\n"
"v_lshlrev_b32 v140, 16, v62\n"
"v_and_b32 v141, s80, v62\n"
"v_lshlrev_b32 v142, 16, v63\n"
"v_and_b32 v143, s80, v63\n"
"v_pk_add_f32 v[0:1], v[0:1], v[136:137]\n"
"v_pk_add_f32 v[2:3], v[2:3], v[138:139]\n"
"v_pk_add_f32 v[4:5], v[4:5], v[140:141]\n"
"v_pk_add_f32 v[6:7], v[6:7], v[142:143]\n"
"v_cvt_pk_bf16_f32 v0, v0, v1\n"
"v_cvt_pk_bf16_f32 v1, v2, v3\n"
"v_cvt_pk_bf16_f32 v2, v4, v5\n"
"v_cvt_pk_bf16_f32 v3, v6, v7\n"
"s_nop 0\n"
"v_permlane16_swap_b32 v0, v2\n"
"v_permlane16_swap_b32 v1, v3\n"
"buffer_store_dwordx4 v[0:3], v238, s[44:47], 0 offen offset:0 sc1\n"
"s_waitcnt lgkmcnt(1)\n"
"s_waitcnt vmcnt(15)\n"
"v_lshlrev_b32 v136, 16, v68\n"
"v_and_b32 v137, s80, v68\n"
"v_lshlrev_b32 v138, 16, v69\n"
"v_and_b32 v139, s80, v69\n"
"v_lshlrev_b32 v140, 16, v70\n"
"v_and_b32 v141, s80, v70\n"
"v_lshlrev_b32 v142, 16, v71\n"
"v_and_b32 v143, s80, v71\n"
"v_pk_add_f32 v[8:9], v[8:9], v[136:137]\n"
"v_pk_add_f32 v[10:11], v[10:11], v[138:139]\n"
"v_pk_add_f32 v[12:13], v[12:13], v[140:141]\n"
"v_pk_add_f32 v[14:15], v[14:15], v[142:143]\n"
"s_waitcnt vmcnt(14)\n"
"v_lshlrev_b32 v128, 16, v72\n"
"v_and_b32 v129, s80, v72\n"
"v_lshlrev_b32 v130, 16, v73\n"
"v_and_b32 v131, s80, v73\n"
"v_lshlrev_b32 v132, 16, v74\n"
"v_and_b32 v133, s80, v74\n"
"v_lshlrev_b32 v134, 16, v75\n"
"v_and_b32 v135, s80, v75\n"
"v_pk_add_f32 v[8:9], v[8:9], v[128:129]\n"
"v_pk_add_f32 v[10:11], v[10:11], v[130:131]\n"
"v_pk_add_f32 v[12:13], v[12:13], v[132:133]\n"
"v_pk_add_f32 v[14:15], v[14:15], v[134:135]\n"
"s_waitcnt vmcnt(13)\n"
"v_lshlrev_b32 v136, 16, v76\n"
"v_and_b32 v137, s80, v76\n"
"v_lshlrev_b32 v138, 16, v77\n"
"v_and_b32 v139, s80, v77\n"
"v_lshlrev_b32 v140, 16, v78\n"
"v_and_b32 v141, s80, v78\n"
"v_lshlrev_b32 v142, 16, v79\n"
"v_and_b32 v143, s80, v79\n"
"v_pk_add_f32 v[8:9], v[8:9], v[136:137]\n"
"v_pk_add_f32 v[10:11], v[10:11], v[138:139]\n"
"v_pk_add_f32 v[12:13], v[12:13], v[140:141]\n"
"v_pk_add_f32 v[14:15], v[14:15], v[142:143]\n"
"s_waitcnt vmcnt(12)\n"
"v_lshlrev_b32 v128, 16, v80\n"
"v_and_b32 v129, s80, v80\n"
"v_lshlrev_b32 v130, 16, v81\n"
"v_and_b32 v131, s80, v81\n"
"v_lshlrev_b32 v132, 16, v82\n"
"v_and_b32 v133, s80, v82\n"
"v_lshlrev_b32 v134, 16, v83\n"
"v_and_b32 v135, s80, v83\n"
"v_pk_add_f32 v[8:9], v[8:9], v[128:129]\n"
"v_pk_add_f32 v[10:11], v[10:11], v[130:131]\n"
"v_pk_add_f32 v[12:13], v[12:13], v[132:133]\n"
"v_pk_add_f32 v[14:15], v[14:15], v[134:135]\n"
"s_waitcnt vmcnt(11)\n"
"v_lshlrev_b32 v136, 16, v84\n"
"v_and_b32 v137, s80, v84\n"
"v_lshlrev_b32 v138, 16, v85\n"
"v_and_b32 v139, s80, v85\n"
"v_lshlrev_b32 v140, 16, v86\n"
"v_and_b32 v141, s80, v86\n"
"v_lshlrev_b32 v142, 16, v87\n"
"v_and_b32 v143, s80, v87\n"
"v_pk_add_f32 v[8:9], v[8:9], v[136:137]\n"
"v_pk_add_f32 v[10:11], v[10:11], v[138:139]\n"
"v_pk_add_f32 v[12:13], v[12:13], v[140:141]\n"
"v_pk_add_f32 v[14:15], v[14:15], v[142:143]\n"
"s_waitcnt vmcnt(10)\n"
"v_lshlrev_b32 v128, 16, v88\n"
"v_and_b32 v129, s80, v88\n"
"v_lshlrev_b32 v130, 16, v89\n"
"v_and_b32 v131, s80, v89\n"
"v_lshlrev_b32 v132, 16, v90\n"
"v_and_b32 v133, s80, v90\n"
"v_lshlrev_b32 v134, 16, v91\n"
"v_and_b32 v135, s80, v91\n"
"v_pk_add_f32 v[8:9], v[8:9], v[128:129]\n"
"v_pk_add_f32 v[10:11], v[10:11], v[130:131]\n"
"v_pk_add_f32 v[12:13], v[12:13], v[132:133]\n"
"v_pk_add_f32 v[14:15], v[14:15], v[134:135]\n"
"s_waitcnt vmcnt(9)\n"
"v_lshlrev_b32 v136, 16, v92\n"
"v_and_b32 v137, s80, v92\n"
"v_lshlrev_b32 v138, 16, v93\n"
"v_and_b32 v139, s80, v93\n"
"v_lshlrev_b32 v140, 16, v94\n"
"v_and_b32 v141, s80, v94\n"
"v_lshlrev_b32 v142, 16, v95\n"
"v_and_b32 v143, s80, v95\n"
"v_pk_add_f32 v[8:9], v[8:9], v[136:137]\n"
"v_pk_add_f32 v[10:11], v[10:11], v[138:139]\n"
"v_pk_add_f32 v[12:13], v[12:13], v[140:141]\n"
"v_pk_add_f32 v[14:15], v[14:15], v[142:143]\n"
"v_cvt_pk_bf16_f32 v8, v8, v9\n"
"v_cvt_pk_bf16_f32 v9, v10, v11\n"
"v_cvt_pk_bf16_f32 v10, v12, v13\n"
"v_cvt_pk_bf16_f32 v11, v14, v15\n"
"s_nop 0\n"
"v_permlane16_swap_b32 v8, v10\n"
"v_permlane16_swap_b32 v9, v11\n"
"buffer_store_dwordx4 v[8:11], v216, s[44:47], 0 offen offset:0 sc1\n"
"s_waitcnt lgkmcnt(0)\n"
"v_lshlrev_b32 v16, 16, v96\n"
"v_and_b32 v17, s80, v96\n"
"v_lshlrev_b32 v18, 16, v97\n"
"v_and_b32 v19, s80, v97\n"
"v_lshlrev_b32 v20, 16, v98\n"
"v_and_b32 v21, s80, v98\n"
"v_lshlrev_b32 v22, 16, v99\n"
"v_and_b32 v23, s80, v99\n"
"s_waitcnt vmcnt(9)\n"
"v_lshlrev_b32 v136, 16, v100\n"
"v_and_b32 v137, s80, v100\n"
"v_lshlrev_b32 v138, 16, v101\n"
"v_and_b32 v139, s80, v101\n"
"v_lshlrev_b32 v140, 16, v102\n"
"v_and_b32 v141, s80, v102\n"
"v_lshlrev_b32 v142, 16, v103\n"
"v_and_b32 v143, s80, v103\n"
"v_pk_add_f32 v[16:17], v[16:17], v[136:137]\n"
"v_pk_add_f32 v[18:19], v[18:19], v[138:139]\n"
"v_pk_add_f32 v[20:21], v[20:21], v[140:141]\n"
"v_pk_add_f32 v[22:23], v[22:23], v[142:143]\n"
"s_waitcnt vmcnt(8)\n"
"v_lshlrev_b32 v128, 16, v104\n"
"v_and_b32 v129, s80, v104\n"
"v_lshlrev_b32 v130, 16, v105\n"
"v_and_b32 v131, s80, v105\n"
"v_lshlrev_b32 v132, 16, v106\n"
"v_and_b32 v133, s80, v106\n"
"v_lshlrev_b32 v134, 16, v107\n"
"v_and_b32 v135, s80, v107\n"
"v_pk_add_f32 v[16:17], v[16:17], v[128:129]\n"
"v_pk_add_f32 v[18:19], v[18:19], v[130:131]\n"
"v_pk_add_f32 v[20:21], v[20:21], v[132:133]\n"
"v_pk_add_f32 v[22:23], v[22:23], v[134:135]\n"
"s_waitcnt vmcnt(7)\n"
"v_lshlrev_b32 v136, 16, v108\n"
"v_and_b32 v137, s80, v108\n"
"v_lshlrev_b32 v138, 16, v109\n"
"v_and_b32 v139, s80, v109\n"
"v_lshlrev_b32 v140, 16, v110\n"
"v_and_b32 v141, s80, v110\n"
"v_lshlrev_b32 v142, 16, v111\n"
"v_and_b32 v143, s80, v111\n"
"v_pk_add_f32 v[16:17], v[16:17], v[136:137]\n"
"v_pk_add_f32 v[18:19], v[18:19], v[138:139]\n"
"v_pk_add_f32 v[20:21], v[20:21], v[140:141]\n"
"v_pk_add_f32 v[22:23], v[22:23], v[142:143]\n"
"s_waitcnt vmcnt(6)\n"
"v_lshlrev_b32 v128, 16, v112\n"
"v_and_b32 v129, s80, v112\n"
"v_lshlrev_b32 v130, 16, v113\n"
"v_and_b32 v131, s80, v113\n"
"v_lshlrev_b32 v132, 16, v114\n"
"v_and_b32 v133, s80, v114\n"
"v_lshlrev_b32 v134, 16, v115\n"
"v_and_b32 v135, s80, v115\n"
"v_pk_add_f32 v[16:17], v[16:17], v[128:129]\n"
"v_pk_add_f32 v[18:19], v[18:19], v[130:131]\n"
"v_pk_add_f32 v[20:21], v[20:21], v[132:133]\n"
"v_pk_add_f32 v[22:23], v[22:23], v[134:135]\n"
"s_waitcnt vmcnt(5)\n"
"v_lshlrev_b32 v136, 16, v116\n"
"v_and_b32 v137, s80, v116\n"
"v_lshlrev_b32 v138, 16, v117\n"
"v_and_b32 v139, s80, v117\n"
"v_lshlrev_b32 v140, 16, v118\n"
"v_and_b32 v141, s80, v118\n"
"v_lshlrev_b32 v142, 16, v119\n"
"v_and_b32 v143, s80, v119\n"
"v_pk_add_f32 v[16:17], v[16:17], v[136:137]\n"
"v_pk_add_f32 v[18:19], v[18:19], v[138:139]\n"
"v_pk_add_f32 v[20:21], v[20:21], v[140:141]\n"
"v_pk_add_f32 v[22:23], v[22:23], v[142:143]\n"
"s_waitcnt vmcnt(4)\n"
"v_lshlrev_b32 v128, 16, v120\n"
"v_and_b32 v129, s80, v120\n"
"v_lshlrev_b32 v130, 16, v121\n"
"v_and_b32 v131, s80, v121\n"
"v_lshlrev_b32 v132, 16, v122\n"
"v_and_b32 v133, s80, v122\n"
"v_lshlrev_b32 v134, 16, v123\n"
"v_and_b32 v135, s80, v123\n"
"v_pk_add_f32 v[16:17], v[16:17], v[128:129]\n"
"v_pk_add_f32 v[18:19], v[18:19], v[130:131]\n"
"v_pk_add_f32 v[20:21], v[20:21], v[132:133]\n"
"v_pk_add_f32 v[22:23], v[22:23], v[134:135]\n"
"s_waitcnt vmcnt(3)\n"
"v_lshlrev_b32 v136, 16, v124\n"
"v_and_b32 v137, s80, v124\n"
"v_lshlrev_b32 v138, 16, v125\n"
"v_and_b32 v139, s80, v125\n"
"v_lshlrev_b32 v140, 16, v126\n"
"v_and_b32 v141, s80, v126\n"
"v_lshlrev_b32 v142, 16, v127\n"
"v_and_b32 v143, s80, v127\n"
"v_pk_add_f32 v[16:17], v[16:17], v[136:137]\n"
"v_pk_add_f32 v[18:19], v[18:19], v[138:139]\n"
"v_pk_add_f32 v[20:21], v[20:21], v[140:141]\n"
"v_pk_add_f32 v[22:23], v[22:23], v[142:143]\n"
"v_cvt_pk_bf16_f32 v16, v16, v17\n"
"v_cvt_pk_bf16_f32 v17, v18, v19\n"
"v_cvt_pk_bf16_f32 v18, v20, v21\n"
"v_cvt_pk_bf16_f32 v19, v22, v23\n"
"s_nop 0\n"
"v_permlane16_swap_b32 v16, v18\n"
"v_permlane16_swap_b32 v17, v19\n"
"buffer_store_dwordx4 v[16:19], v238, s[44:47], 0 offen offset:192 sc1\n"
"s_branch Lend%=\n"
"Lspx1%=:\n"
"s_mov_b32 s56, 0x400000\n"
"Lsqx1%=:\n"
"s_sleep 1\n"
"buffer_load_dword v255, v240, s[96:99], s60 offen offset:0 sc1\n"
"s_waitcnt vmcnt(0)\n"
"v_cmp_ne_u32 vcc, v255, v253\n"
"s_sub_u32 s56, s56, 1\n"
"s_cbranch_scc1 Lrtx1%=\n"
"s_nop 1\n"
"s_lshr_b32 s66, vcc_lo, 0\n"
"s_bitcmp1_b32 s66, s95\n"
"s_cbranch_scc1 Lsqx1%=\n"
"s_branch Lrtx1%=\n"
"Lspx2%=:\n"
"s_mov_b32 s56, 0x400000\n"
"Lsqx2%=:\n"
"s_sleep 1\n"
"buffer_load_dword v255, v240, s[96:99], s60 offen offset:0 sc1\n"
"s_waitcnt vmcnt(0)\n"
"v_cmp_ne_u32 vcc, v255, v253\n"
"s_sub_u32 s56, s56, 1\n"
"s_cbranch_scc1 Lrtx2%=\n"
"s_nop 1\n"
"s_lshr_b32 s66, vcc_lo, 0\n"
"s_bitcmp1_b32 s66, s67\n"
"s_cbranch_scc1 Lsqx2%=\n"
"s_branch Lrtx2%=\n"
"Lspx3%=:\n"
"s_mov_b32 s56, 0x400000\n"
"Lsqx3%=:\n"
"s_sleep 1\n"
"buffer_load_dword v255, v240, s[96:99], s60 offen offset:0 sc1\n"
"s_waitcnt vmcnt(0)\n"
"v_cmp_ne_u32 vcc, v255, v253\n"
"s_sub_u32 s56, s56, 1\n"
"s_cbranch_scc1 Lrtx3%=\n"
"s_nop 1\n"
"s_lshr_b32 s66, vcc_lo, 0\n"
"s_bitcmp1_b32 s66, s69\n"
"s_cbranch_scc1 Lsqx3%=\n"
"s_branch Lrtx3%=\n"
"Lspy1%=:\n"
"s_mov_b32 s56, 0x400000\n"
"Lsqy1%=:\n"
"s_sleep 1\n"
"buffer_load_dword v255, v240, s[96:99], s60 offen offset:0 sc1\n"
"s_waitcnt vmcnt(0)\n"
"v_cmp_ne_u32 vcc, v255, v253\n"
"s_sub_u32 s56, s56, 1\n"
"s_cbranch_scc1 Lrty1%=\n"
"s_nop 1\n"
"s_lshr_b32 s66, vcc_lo, 4\n"
"s_bitcmp1_b32 s66, s95\n"
"s_cbranch_scc1 Lsqy1%=\n"
"s_branch Lrty1%=\n"
"Lspy2%=:\n"
"s_mov_b32 s56, 0x400000\n"
"Lsqy2%=:\n"
"s_sleep 1\n"
"buffer_load_dword v255, v240, s[96:99], s60 offen offset:0 sc1\n"
"s_waitcnt vmcnt(0)\n"
"v_cmp_ne_u32 vcc, v255, v253\n"
"s_sub_u32 s56, s56, 1\n"
"s_cbranch_scc1 Lrty2%=\n"
"s_nop 1\n"
"s_lshr_b32 s66, vcc_lo, 4\n"
"s_bitcmp1_b32 s66, s67\n"
"s_cbranch_scc1 Lsqy2%=\n"
"s_branch Lrty2%=\n"
"Lspy3%=:\n"
"s_mov_b32 s56, 0x400000\n"
"Lsqy3%=:\n"
"s_sleep 1\n"
"buffer_load_dword v255, v240, s[96:99], s60 offen offset:0 sc1\n"
"s_waitcnt vmcnt(0)\n"
"v_cmp_ne_u32 vcc, v255, v253\n"
"s_sub_u32 s56, s56, 1\n"
"s_cbranch_scc1 Lrty3%=\n"
"s_nop 1\n"
"s_lshr_b32 s66, vcc_lo, 4\n"
"s_bitcmp1_b32 s66, s69\n"
"s_cbranch_scc1 Lsqy3%=\n"
"s_branch Lrty3%=\n"
"Lspa%=:\n"
"s_mov_b32 s56, 0x400000\n"
"Lsqa%=:\n"
"s_sleep 1\n"
"buffer_load_dword v255, v240, s[96:99], s61 offen offset:0 sc1\n"
"s_waitcnt vmcnt(0)\n"
"v_cmp_ne_u32 vcc, v255, v253\n"
"s_sub_u32 s56, s56, 1\n"
"s_cbranch_scc1 Lrta%=\n"
"s_nop 1\n"
"s_and_b32 s66, vcc_lo, 0xff\n"
"s_cbranch_scc1 Lsqa%=\n"
"s_branch Lrta%=\n"
"Lmax%=:\n"
"s_waitcnt vmcnt(0) lgkmcnt(0)\n"
"s_nop 7\n"
"s_barrier\n"
"s_mov_b32 s64, s73\n"
"s_mov_b32 s56, 15\n"
"v_mov_b32 v164, 0xff800000\n"
"v_mov_b32 v165, 0xff800000\n"
"v_mov_b32 v166, 0xff800000\n"
"v_mov_b32 v167, 0xff800000\n"
"Lmaxl%=:\n"
"s_add_u32 m0, s73, 0\n"
"s_nop 0\n"
"buffer_load_dwordx4 v216, s[44:47], s64 offen offset:0 lds\n"
"buffer_load_dwordx4 v217, s[44:47], s64 offen offset:1024 lds\n"
"buffer_load_dwordx4 v218, s[44:47], s64 offen offset:2048 lds\n"
"buffer_load_dwordx4 v219, s[44:47], s64 offen offset:3072 lds\n"
"s_add_u32 m0, s73, 0x1000\n"
"s_nop 0\n"
"buffer_load_dwordx4 v220, s[44:47], s64 offen offset:0 lds\n"
"buffer_load_dwordx4 v221, s[44:47], s64 offen offset:1024 lds\n"
"s_add_u32 s64, s64, 0x6000\n"
"s_waitcnt vmcnt(0) lgkmcnt(0)\n"
"s_barrier\n"
"ds_read_b128 v[192:195], v230 offset:0\n"
"s_waitcnt lgkmcnt(0)\n"
"v_mfma_f32_16x16x32_bf16 v[0:3], v[192:195], a[192:195], 0\n"
"v_mfma_f32_16x16x32_bf16 v[8:11], v[192:195], a[196:199], 0\n"
"v_mfma_f32_16x16x32_bf16 v[16:19], v[192:195], a[200:203], 0\n"
"v_mfma_f32_16x16x32_bf16 v[24:27], v[192:195], a[204:207], 0\n"
"ds_read_b128 v[196:199], v231 offset:0\n"
"s_waitcnt lgkmcnt(0)\n"
"v_mfma_f32_16x16x32_bf16 v[0:3], v[196:199], a[208:211], v[0:3]\n"
"v_mfma_f32_16x16x32_bf16 v[8:11], v[196:199], a[212:215], v[8:11]\n"
"v_mfma_f32_16x16x32_bf16 v[16:19], v[196:199], a[216:219], v[16:19]\n"
"v_mfma_f32_16x16x32_bf16 v[24:27], v[196:199], a[220:223], v[24:27]\n"
"ds_read_b128 v[200:203], v230 offset:128\n"
"s_waitcnt lgkmcnt(0)\n"
"v_mfma_f32_16x16x32_bf16 v[0:3], v[200:203], a[224:227], v[0:3]\n"
"v_mfma_f32_16x16x32_bf16 v[8:11], v[200:203], a[228:231], v[8:11]\n"
"v_mfma_f32_16x16x32_bf16 v[16:19], v[200:203], a[232:235], v[16:19]\n"
"v_mfma_f32_16x16x32_bf16 v[24:27], v[200:203], a[236:239], v[24:27]\n"
"ds_read_b128 v[204:207], v231 offset:128\n"
"s_waitcnt lgkmcnt(0)\n"
"v_mfma_f32_16x16x32_bf16 v[0:3], v[204:207], a[240:243], v[0:3]\n"
"v_mfma_f32_16x16x32_bf16 v[8:11], v[204:207], a[244:247], v[8:11]\n"
"v_mfma_f32_16x16x32_bf16 v[16:19], v[204:207], a[248:251], v[16:19]\n"
"v_mfma_f32_16x16x32_bf16 v[24:27], v[204:207], a[252:255], v[24:27]\n"
"ds_read_b128 v[208:211], v230 offset:256\n"
"s_waitcnt lgkmcnt(0)\n"
"v_mfma_f32_16x16x32_bf16 v[0:3], v[208:211], v[128:131], v[0:3]\n"
"v_mfma_f32_16x16x32_bf16 v[8:11], v[208:211], v[136:139], v[8:11]\n"
"v_mfma_f32_16x16x32_bf16 v[16:19], v[208:211], v[144:147], v[16:19]\n"
"v_mfma_f32_16x16x32_bf16 v[24:27], v[208:211], v[152:155], v[24:27]\n"
"ds_read_b128 v[212:215], v231 offset:256\n"
"s_waitcnt lgkmcnt(0)\n"
"v_mfma_f32_16x16x32_bf16 v[0:3], v[212:215], v[160:163], v[0:3]\n"
"v_mfma_f32_16x16x32_bf16 v[8:11], v[212:215], v[168:171], v[8:11]\n"
"v_mfma_f32_16x16x32_bf16 v[16:19], v[212:215], v[176:179], v[16:19]\n"
"v_mfma_f32_16x16x32_bf16 v[24:27], v[212:215], v[184:187], v[24:27]\n"
"ds_read_b128 v[192:195], v230 offset:6144\n"
"s_waitcnt lgkmcnt(0)\n"
"v_mfma_f32_16x16x32_bf16 v[4:7], v[192:195], a[192:195], 0\n"
"v_mfma_f32_16x16x32_bf16 v[12:15], v[192:195], a[196:199], 0\n"
"v_mfma_f32_16x16x32_bf16 v[20:23], v[192:195], a[200:203], 0\n"
"v_mfma_f32_16x16x32_bf16 v[28:31], v[192:195], a[204:207], 0\n"
"ds_read_b128 v[196:199], v231 offset:6144\n"
"s_waitcnt lgkmcnt(0)\n"
"v_mfma_f32_16x16x32_bf16 v[4:7], v[196:199], a[208:211], v[4:7]\n"
"v_mfma_f32_16x16x32_bf16 v[12:15], v[196:199], a[212:215], v[12:15]\n"
"v_mfma_f32_16x16x32_bf16 v[20:23], v[196:199], a[216:219], v[20:23]\n"
"v_mfma_f32_16x16x32_bf16 v[28:31], v[196:199], a[220:223], v[28:31]\n"
"ds_read_b128 v[200:203], v230 offset:6272\n"
"s_waitcnt lgkmcnt(0)\n"
"v_mfma_f32_16x16x32_bf16 v[4:7], v[200:203], a[224:227], v[4:7]\n"
"v_mfma_f32_16x16x32_bf16 v[12:15], v[200:203], a[228:231], v[12:15]\n"
"v_mfma_f32_16x16x32_bf16 v[20:23], v[200:203], a[232:235], v[20:23]\n"
"v_mfma_f32_16x16x32_bf16 v[28:31], v[200:203], a[236:239], v[28:31]\n"
"ds_read_b128 v[204:207], v231 offset:6272\n"
"s_waitcnt lgkmcnt(0)\n"
"v_mfma_f32_16x16x32_bf16 v[4:7], v[204:207], a[240:243], v[4:7]\n"
"v_mfma_f32_16x16x32_bf16 v[12:15], v[204:207], a[244:247], v[12:15]\n"
"v_mfma_f32_16x16x32_bf16 v[20:23], v[204:207], a[248:251], v[20:23]\n"
"v_mfma_f32_16x16x32_bf16 v[28:31], v[204:207], a[252:255], v[28:31]\n"
"ds_read_b128 v[208:211], v230 offset:6400\n"
"s_waitcnt lgkmcnt(0)\n"
"v_mfma_f32_16x16x32_bf16 v[4:7], v[208:211], v[128:131], v[4:7]\n"
"v_mfma_f32_16x16x32_bf16 v[12:15], v[208:211], v[136:139], v[12:15]\n"
"v_mfma_f32_16x16x32_bf16 v[20:23], v[208:211], v[144:147], v[20:23]\n"
"v_mfma_f32_16x16x32_bf16 v[28:31], v[208:211], v[152:155], v[28:31]\n"
"ds_read_b128 v[212:215], v231 offset:6400\n"
"s_waitcnt lgkmcnt(0)\n"
"v_mfma_f32_16x16x32_bf16 v[4:7], v[212:215], v[160:163], v[4:7]\n"
"v_mfma_f32_16x16x32_bf16 v[12:15], v[212:215], v[168:171], v[12:15]\n"
"v_mfma_f32_16x16x32_bf16 v[20:23], v[212:215], v[176:179], v[20:23]\n"
"v_mfma_f32_16x16x32_bf16 v[28:31], v[212:215], v[184:187], v[28:31]\n"
"ds_read_b128 v[192:195], v230 offset:12288\n"
"s_waitcnt lgkmcnt(0)\n"
"v_mfma_f32_16x16x32_bf16 v[32:35], v[192:195], a[192:195], 0\n"
"v_mfma_f32_16x16x32_bf16 v[40:43], v[192:195], a[196:199], 0\n"
"v_mfma_f32_16x16x32_bf16 v[48:51], v[192:195], a[200:203], 0\n"
"v_mfma_f32_16x16x32_bf16 v[56:59], v[192:195], a[204:207], 0\n"
"ds_read_b128 v[196:199], v231 offset:12288\n"
"s_waitcnt lgkmcnt(0)\n"
"v_mfma_f32_16x16x32_bf16 v[32:35], v[196:199], a[208:211], v[32:35]\n"
"v_mfma_f32_16x16x32_bf16 v[40:43], v[196:199], a[212:215], v[40:43]\n"
"v_mfma_f32_16x16x32_bf16 v[48:51], v[196:199], a[216:219], v[48:51]\n"
"v_mfma_f32_16x16x32_bf16 v[56:59], v[196:199], a[220:223], v[56:59]\n"
"ds_read_b128 v[200:203], v230 offset:12416\n"
"s_waitcnt lgkmcnt(0)\n"
"v_mfma_f32_16x16x32_bf16 v[32:35], v[200:203], a[224:227], v[32:35]\n"
"v_mfma_f32_16x16x32_bf16 v[40:43], v[200:203], a[228:231], v[40:43]\n"
"v_mfma_f32_16x16x32_bf16 v[48:51], v[200:203], a[232:235], v[48:51]\n"
"v_mfma_f32_16x16x32_bf16 v[56:59], v[200:203], a[236:239], v[56:59]\n"
"ds_read_b128 v[204:207], v231 offset:12416\n"
"s_waitcnt lgkmcnt(0)\n"
"v_mfma_f32_16x16x32_bf16 v[32:35], v[204:207], a[240:243], v[32:35]\n"
"v_mfma_f32_16x16x32_bf16 v[40:43], v[204:207], a[244:247], v[40:43]\n"
"v_mfma_f32_16x16x32_bf16 v[48:51], v[204:207], a[248:251], v[48:51]\n"
"v_mfma_f32_16x16x32_bf16 v[56:59], v[204:207], a[252:255], v[56:59]\n"
"ds_read_b128 v[208:211], v230 offset:12544\n"
"s_waitcnt lgkmcnt(0)\n"
"v_mfma_f32_16x16x32_bf16 v[32:35], v[208:211], v[128:131], v[32:35]\n"
"v_mfma_f32_16x16x32_bf16 v[40:43], v[208:211], v[136:139], v[40:43]\n"
"v_mfma_f32_16x16x32_bf16 v[48:51], v[208:211], v[144:147], v[48:51]\n"
"v_mfma_f32_16x16x32_bf16 v[56:59], v[208:211], v[152:155], v[56:59]\n"
"ds_read_b128 v[212:215], v231 offset:12544\n"
"s_waitcnt lgkmcnt(0)\n"
"v_mfma_f32_16x16x32_bf16 v[32:35], v[212:215], v[160:163], v[32:35]\n"
"v_mfma_f32_16x16x32_bf16 v[40:43], v[212:215], v[168:171], v[40:43]\n"
"v_mfma_f32_16x16x32_bf16 v[48:51], v[212:215], v[176:179], v[48:51]\n"
"v_mfma_f32_16x16x32_bf16 v[56:59], v[212:215], v[184:187], v[56:59]\n"
"ds_read_b128 v[192:195], v230 offset:18432\n"
"s_waitcnt lgkmcnt(0)\n"
"v_mfma_f32_16x16x32_bf16 v[36:39], v[192:195], a[192:195], 0\n"
"v_mfma_f32_16x16x32_bf16 v[44:47], v[192:195], a[196:199], 0\n"
"v_mfma_f32_16x16x32_bf16 v[52:55], v[192:195], a[200:203], 0\n"
"v_mfma_f32_16x16x32_bf16 v[60:63], v[192:195], a[204:207], 0\n"
"ds_read_b128 v[196:199], v231 offset:18432\n"
"s_waitcnt lgkmcnt(0)\n"
"v_mfma_f32_16x16x32_bf16 v[36:39], v[196:199], a[208:211], v[36:39]\n"
"v_mfma_f32_16x16x32_bf16 v[44:47], v[196:199], a[212:215], v[44:47]\n"
"v_mfma_f32_16x16x32_bf16 v[52:55], v[196:199], a[216:219], v[52:55]\n"
"v_mfma_f32_16x16x32_bf16 v[60:63], v[196:199], a[220:223], v[60:63]\n"
"ds_read_b128 v[200:203], v230 offset:18560\n"
"s_waitcnt lgkmcnt(0)\n"
"v_mfma_f32_16x16x32_bf16 v[36:39], v[200:203], a[224:227], v[36:39]\n"
"v_mfma_f32_16x16x32_bf16 v[44:47], v[200:203], a[228:231], v[44:47]\n"
"v_mfma_f32_16x16x32_bf16 v[52:55], v[200:203], a[232:235], v[52:55]\n"
"v_mfma_f32_16x16x32_bf16 v[60:63], v[200:203], a[236:239], v[60:63]\n"
"ds_read_b128 v[204:207], v231 offset:18560\n"
"s_waitcnt lgkmcnt(0)\n"
"v_mfma_f32_16x16x32_bf16 v[36:39], v[204:207], a[240:243], v[36:39]\n"
"v_mfma_f32_16x16x32_bf16 v[44:47], v[204:207], a[244:247], v[44:47]\n"
"v_mfma_f32_16x16x32_bf16 v[52:55], v[204:207], a[248:251], v[52:55]\n"
"v_mfma_f32_16x16x32_bf16 v[60:63], v[204:207], a[252:255], v[60:63]\n"
"ds_read_b128 v[208:211], v230 offset:18688\n"
"s_waitcnt lgkmcnt(0)\n"
"v_mfma_f32_16x16x32_bf16 v[36:39], v[208:211], v[128:131], v[36:39]\n"
"v_mfma_f32_16x16x32_bf16 v[44:47], v[208:211], v[136:139], v[44:47]\n"
"v_mfma_f32_16x16x32_bf16 v[52:55], v[208:211], v[144:147], v[52:55]\n"
"v_mfma_f32_16x16x32_bf16 v[60:63], v[208:211], v[152:155], v[60:63]\n"
"ds_read_b128 v[212:215], v231 offset:18688\n"
"s_waitcnt lgkmcnt(0)\n"
"v_mfma_f32_16x16x32_bf16 v[36:39], v[212:215], v[160:163], v[36:39]\n"
"v_mfma_f32_16x16x32_bf16 v[44:47], v[212:215], v[168:171], v[44:47]\n"
"v_mfma_f32_16x16x32_bf16 v[52:55], v[212:215], v[176:179], v[52:55]\n"
"v_mfma_f32_16x16x32_bf16 v[60:63], v[212:215], v[184:187], v[60:63]\n"
"s_nop 7\n"
"s_nop 7\n"
"s_barrier\n"
"v_max3_f32 v164, v164, v0, v1\n"
"v_max3_f32 v164, v164, v2, v3\n"
"v_max3_f32 v165, v165, v8, v9\n"
"v_max3_f32 v165, v165, v10, v11\n"
"v_max3_f32 v166, v166, v16, v17\n"
"v_max3_f32 v166, v166, v18, v19\n"
"v_max3_f32 v167, v167, v24, v25\n"
"v_max3_f32 v167, v167, v26, v27\n"
"v_max3_f32 v164, v164, v4, v5\n"
"v_max3_f32 v164, v164, v6, v7\n"
"v_max3_f32 v165, v165, v12, v13\n"
"v_max3_f32 v165, v165, v14, v15\n"
"v_max3_f32 v166, v166, v20, v21\n"
"v_max3_f32 v166, v166, v22, v23\n"
"v_max3_f32 v167, v167, v28, v29\n"
"v_max3_f32 v167, v167, v30, v31\n"
"v_max3_f32 v164, v164, v32, v33\n"
"v_max3_f32 v164, v164, v34, v35\n"
"v_max3_f32 v165, v165, v40, v41\n"
"v_max3_f32 v165, v165, v42, v43\n"
"v_max3_f32 v166, v166, v48, v49\n"
"v_max3_f32 v166, v166, v50, v51\n"
"v_max3_f32 v167, v167, v56, v57\n"
"v_max3_f32 v167, v167, v58, v59\n"
"v_max3_f32 v164, v164, v36, v37\n"
"v_max3_f32 v164, v164, v38, v39\n"
"v_max3_f32 v165, v165, v44, v45\n"
"v_max3_f32 v165, v165, v46, v47\n"
"v_max3_f32 v166, v166, v52, v53\n"
"v_max3_f32 v166, v166, v54, v55\n"
"v_max3_f32 v167, v167, v60, v61\n"
"v_max3_f32 v167, v167, v62, v63\n"
"s_sub_u32 s56, s56, 1\n"
"s_cbranch_scc0 Lmaxl%=\n"
"s_nop 1\n"
"v_permlane32_swap_b32 v164, v165\n"
"s_nop 1\n"
"v_permlane32_swap_b32 v166, v167\n"
"s_nop 1\n"
"v_max_f32 v164, v164, v165\n"
"s_nop 1\n"
"v_max_f32 v166, v166, v167\n"
"s_nop 1\n"
"v_permlane16_swap_b32 v164, v166\n"
"s_nop 1\n"
"v_max_f32 v164, v164, v166\n"
"s_nop 1\n"
"v_mov_b32 v251, v164\n"
"s_mov_b32 s63, 2\n"
"s_branch Lstart%=\n"
"Lfail%=:\n"
"s_waitcnt vmcnt(0) lgkmcnt(0)\n"
"s_nop 7\n"
"s_nop 7\n"
"s_barrier\n"
"s_mov_b32 s63, 1\n"
"s_branch Lstart%=\n"
"Lwa%=:\n"
"v_mfma_f32_16x16x32_bf16 a[0:3], v[192:195], v[64:67], a[0:3]\n"
"ds_read_b64_tr_b16 v[210:211], v234 offset:30720\n"
"v_mfma_f32_16x16x32_bf16 a[0:3], v[196:199], v[96:99], a[0:3]\n"
"s_add_u32 m0, s73, 0\n"
"v_exp_f32 v0, v0\n"
"v_mfma_f32_16x16x32_bf16 a[4:7], v[196:199], v[104:107], a[4:7]\n"
"buffer_load_dwordx4 v216, s[44:47], s64 offen offset:0 lds\n"
"v_mfma_f32_16x16x32_bf16 a[4:7], v[192:195], v[72:75], a[4:7]\n"
"v_exp_f32 v1, v1\n"
"v_mfma_f32_16x16x32_bf16 a[8:11], v[192:195], v[80:83], a[8:11]\n"
"v_exp_f32 v2, v2\n"
"v_mfma_f32_16x16x32_bf16 a[8:11], v[196:199], v[112:115], a[8:11]\n"
"v_exp_f32 v3, v3\n"
"v_mfma_f32_16x16x32_bf16 a[12:15], v[196:199], v[120:123], a[12:15]\n"
"ds_read_b64_tr_b16 v[196:197], v235 offset:36864\n"
"v_mfma_f32_16x16x32_bf16 a[12:15], v[192:195], v[88:91], a[12:15]\n"
"s_waitcnt lgkmcnt(5)\n"
"ds_read_b64_tr_b16 v[198:199], v235 offset:43008\n"
"ds_read_b64_tr_b16 v[192:193], v235 offset:24576\n"
"v_mfma_f32_16x16x32_bf16 a[16:19], v[200:203], v[64:67], a[16:19]\n"
"ds_read_b64_tr_b16 v[194:195], v235 offset:30720\n"
"v_mfma_f32_16x16x32_bf16 a[16:19], v[204:207], v[96:99], a[16:19]\n"
"v_exp_f32 v4, v4\n"
"v_mfma_f32_16x16x32_bf16 a[20:23], v[204:207], v[104:107], a[20:23]\n"
"v_exp_f32 v5, v5\n"
"v_mfma_f32_16x16x32_bf16 a[20:23], v[200:203], v[72:75], a[20:23]\n"
"v_exp_f32 v6, v6\n"
"v_mfma_f32_16x16x32_bf16 a[24:27], v[200:203], v[80:83], a[24:27]\n"
"buffer_load_dwordx4 v217, s[44:47], s64 offen offset:1024 lds\n"
"v_mfma_f32_16x16x32_bf16 a[24:27], v[204:207], v[112:115], a[24:27]\n"
"v_exp_f32 v7, v7\n"
"v_mfma_f32_16x16x32_bf16 a[28:31], v[204:207], v[120:123], a[28:31]\n"
"ds_read_b64_tr_b16 v[204:205], v232 offset:36992\n"
"v_add_f32 v242, v242, v0\n"
"v_mfma_f32_16x16x32_bf16 a[28:31], v[200:203], v[88:91], a[28:31]\n"
"s_waitcnt lgkmcnt(5)\n"
"ds_read_b64_tr_b16 v[206:207], v232 offset:43136\n"
"ds_read_b64_tr_b16 v[200:201], v232 offset:24704\n"
"v_mfma_f32_16x16x32_bf16 a[32:35], v[208:211], v[64:67], a[32:35]\n"
"ds_read_b64_tr_b16 v[202:203], v232 offset:30848\n"
"v_add_f32 v243, v243, v1\n"
"v_mfma_f32_16x16x32_bf16 a[32:35], v[212:215], v[96:99], a[32:35]\n"
"v_cvt_pk_bf16_f32 v0, v0, v1\n"
"v_add_f32 v242, v242, v2\n"
"v_mfma_f32_16x16x32_bf16 a[36:39], v[212:215], v[104:107], a[36:39]\n"
"v_add_f32 v243, v243, v3\n"
"v_cvt_pk_bf16_f32 v1, v2, v3\n"
"v_mfma_f32_16x16x32_bf16 a[36:39], v[208:211], v[72:75], a[36:39]\n"
"v_add_f32 v242, v242, v4\n"
"v_add_f32 v243, v243, v5\n"
"v_mfma_f32_16x16x32_bf16 a[40:43], v[208:211], v[80:83], a[40:43]\n"
"v_cvt_pk_bf16_f32 v2, v4, v5\n"
"v_add_f32 v242, v242, v6\n"
"v_mfma_f32_16x16x32_bf16 a[40:43], v[212:215], v[112:115], a[40:43]\n"
"v_add_f32 v243, v243, v7\n"
"v_mfma_f32_16x16x32_bf16 a[44:47], v[212:215], v[120:123], a[44:47]\n"
"ds_read_b64_tr_b16 v[212:213], v233 offset:36992\n"
"v_cvt_pk_bf16_f32 v3, v6, v7\n"
"v_mfma_f32_16x16x32_bf16 a[44:47], v[208:211], v[88:91], a[44:47]\n"
"s_waitcnt lgkmcnt(5)\n"
"ds_read_b64_tr_b16 v[214:215], v233 offset:43136\n"
"ds_read_b64_tr_b16 v[208:209], v233 offset:24704\n"
"v_mfma_f32_16x16x32_bf16 a[48:51], v[192:195], v[64:67], a[48:51]\n"
"ds_read_b64_tr_b16 v[210:211], v233 offset:30848\n"
"v_mfma_f32_16x16x32_bf16 a[48:51], v[196:199], v[96:99], a[48:51]\n"
"v_exp_f32 v8, v8\n"
"v_mfma_f32_16x16x32_bf16 a[52:55], v[196:199], v[104:107], a[52:55]\n"
"buffer_load_dwordx4 v218, s[44:47], s64 offen offset:2048 lds\n"
"v_mfma_f32_16x16x32_bf16 a[52:55], v[192:195], v[72:75], a[52:55]\n"
"v_exp_f32 v9, v9\n"
"v_mfma_f32_16x16x32_bf16 a[56:59], v[192:195], v[80:83], a[56:59]\n"
"v_exp_f32 v10, v10\n"
"v_mfma_f32_16x16x32_bf16 a[56:59], v[196:199], v[112:115], a[56:59]\n"
"v_exp_f32 v11, v11\n"
"v_mfma_f32_16x16x32_bf16 a[60:63], v[196:199], v[120:123], a[60:63]\n"
"ds_read_b64_tr_b16 v[196:197], v234 offset:36992\n"
"v_mfma_f32_16x16x32_bf16 a[60:63], v[192:195], v[88:91], a[60:63]\n"
"s_waitcnt lgkmcnt(5)\n"
"ds_read_b64_tr_b16 v[198:199], v234 offset:43136\n"
"ds_read_b64_tr_b16 v[192:193], v234 offset:24704\n"
"v_mfma_f32_16x16x32_bf16 a[64:67], v[200:203], v[64:67], a[64:67]\n"
"ds_read_b64_tr_b16 v[194:195], v234 offset:30848\n"
"v_mfma_f32_16x16x32_bf16 a[64:67], v[204:207], v[96:99], a[64:67]\n"
"v_exp_f32 v12, v12\n"
"v_mfma_f32_16x16x32_bf16 a[68:71], v[204:207], v[104:107], a[68:71]\n"
"v_exp_f32 v13, v13\n"
"v_mfma_f32_16x16x32_bf16 a[68:71], v[200:203], v[72:75], a[68:71]\n"
"v_exp_f32 v14, v14\n"
"v_mfma_f32_16x16x32_bf16 a[72:75], v[200:203], v[80:83], a[72:75]\n"
"buffer_load_dwordx4 v219, s[44:47], s64 offen offset:3072 lds\n"
"v_mfma_f32_16x16x32_bf16 a[72:75], v[204:207], v[112:115], a[72:75]\n"
"v_exp_f32 v15, v15\n"
"v_mfma_f32_16x16x32_bf16 a[76:79], v[204:207], v[120:123], a[76:79]\n"
"ds_read_b64_tr_b16 v[204:205], v235 offset:36992\n"
"v_add_f32 v244, v244, v8\n"
"v_mfma_f32_16x16x32_bf16 a[76:79], v[200:203], v[88:91], a[76:79]\n"
"s_waitcnt lgkmcnt(5)\n"
"ds_read_b64_tr_b16 v[206:207], v235 offset:43136\n"
"ds_read_b64_tr_b16 v[200:201], v235 offset:24704\n"
"v_mfma_f32_16x16x32_bf16 a[80:83], v[208:211], v[64:67], a[80:83]\n"
"ds_read_b64_tr_b16 v[202:203], v235 offset:30848\n"
"v_add_f32 v245, v245, v9\n"
"v_mfma_f32_16x16x32_bf16 a[80:83], v[212:215], v[96:99], a[80:83]\n"
"v_cvt_pk_bf16_f32 v8, v8, v9\n"
"v_add_f32 v244, v244, v10\n"
"v_mfma_f32_16x16x32_bf16 a[84:87], v[212:215], v[104:107], a[84:87]\n"
"v_add_f32 v245, v245, v11\n"
"v_cvt_pk_bf16_f32 v9, v10, v11\n"
"v_mfma_f32_16x16x32_bf16 a[84:87], v[208:211], v[72:75], a[84:87]\n"
"v_add_f32 v244, v244, v12\n"
"v_add_f32 v245, v245, v13\n"
"v_mfma_f32_16x16x32_bf16 a[88:91], v[208:211], v[80:83], a[88:91]\n"
"v_cvt_pk_bf16_f32 v10, v12, v13\n"
"v_add_f32 v244, v244, v14\n"
"v_mfma_f32_16x16x32_bf16 a[88:91], v[212:215], v[112:115], a[88:91]\n"
"v_add_f32 v245, v245, v15\n"
"v_cvt_pk_bf16_f32 v11, v14, v15\n"
"v_mfma_f32_16x16x32_bf16 a[92:95], v[212:215], v[120:123], a[92:95]\n"
"ds_read_b64_tr_b16 v[212:213], v232 offset:37120\n"
"v_mfma_f32_16x16x32_bf16 a[92:95], v[208:211], v[88:91], a[92:95]\n"
"s_waitcnt lgkmcnt(5)\n"
"ds_read_b64_tr_b16 v[214:215], v232 offset:43264\n"
"ds_read_b64_tr_b16 v[208:209], v232 offset:24832\n"
"v_mfma_f32_16x16x32_bf16 a[96:99], v[192:195], v[64:67], a[96:99]\n"
"ds_read_b64_tr_b16 v[210:211], v232 offset:30976\n"
"v_mfma_f32_16x16x32_bf16 a[96:99], v[196:199], v[96:99], a[96:99]\n"
"s_add_u32 m0, s73, 0x1000\n"
"v_exp_f32 v16, v16\n"
"v_mfma_f32_16x16x32_bf16 a[100:103], v[196:199], v[104:107], a[100:103]\n"
"buffer_load_dwordx4 v220, s[44:47], s64 offen offset:0 lds\n"
"v_mfma_f32_16x16x32_bf16 a[100:103], v[192:195], v[72:75], a[100:103]\n"
"v_exp_f32 v17, v17\n"
"v_mfma_f32_16x16x32_bf16 a[104:107], v[192:195], v[80:83], a[104:107]\n"
"v_exp_f32 v18, v18\n"
"v_mfma_f32_16x16x32_bf16 a[104:107], v[196:199], v[112:115], a[104:107]\n"
"v_exp_f32 v19, v19\n"
"v_mfma_f32_16x16x32_bf16 a[108:111], v[196:199], v[120:123], a[108:111]\n"
"ds_read_b64_tr_b16 v[196:197], v233 offset:37120\n"
"v_mfma_f32_16x16x32_bf16 a[108:111], v[192:195], v[88:91], a[108:111]\n"
"s_waitcnt lgkmcnt(5)\n"
"ds_read_b64_tr_b16 v[198:199], v233 offset:43264\n"
"ds_read_b64_tr_b16 v[192:193], v233 offset:24832\n"
"v_mfma_f32_16x16x32_bf16 a[112:115], v[200:203], v[64:67], a[112:115]\n"
"ds_read_b64_tr_b16 v[194:195], v233 offset:30976\n"
"v_mfma_f32_16x16x32_bf16 a[112:115], v[204:207], v[96:99], a[112:115]\n"
"v_exp_f32 v20, v20\n"
"v_mfma_f32_16x16x32_bf16 a[116:119], v[204:207], v[104:107], a[116:119]\n"
"v_exp_f32 v21, v21\n"
"v_mfma_f32_16x16x32_bf16 a[116:119], v[200:203], v[72:75], a[116:119]\n"
"v_exp_f32 v22, v22\n"
"v_mfma_f32_16x16x32_bf16 a[120:123], v[200:203], v[80:83], a[120:123]\n"
"buffer_load_dwordx4 v221, s[44:47], s64 offen offset:1024 lds\n"
"v_mfma_f32_16x16x32_bf16 a[120:123], v[204:207], v[112:115], a[120:123]\n"
"v_exp_f32 v23, v23\n"
"v_mfma_f32_16x16x32_bf16 a[124:127], v[204:207], v[120:123], a[124:127]\n"
"ds_read_b64_tr_b16 v[204:205], v234 offset:37120\n"
"v_add_f32 v246, v246, v16\n"
"v_mfma_f32_16x16x32_bf16 a[124:127], v[200:203], v[88:91], a[124:127]\n"
"s_waitcnt lgkmcnt(5)\n"
"ds_read_b64_tr_b16 v[206:207], v234 offset:43264\n"
"ds_read_b64_tr_b16 v[200:201], v234 offset:24832\n"
"v_mfma_f32_16x16x32_bf16 a[128:131], v[208:211], v[64:67], a[128:131]\n"
"s_add_u32 s64, s64, 0x6000\n"
"ds_read_b64_tr_b16 v[202:203], v234 offset:30976\n"
"v_add_f32 v247, v247, v17\n"
"v_mfma_f32_16x16x32_bf16 a[128:131], v[212:215], v[96:99], a[128:131]\n"
"v_cvt_pk_bf16_f32 v16, v16, v17\n"
"v_add_f32 v246, v246, v18\n"
"v_mfma_f32_16x16x32_bf16 a[132:135], v[212:215], v[104:107], a[132:135]\n"
"s_cmp_eq_u32 s56, 3\n"
"s_cbranch_scc1 Ltil0x3%=\n"
"Lwtsl0x3%=:\n"
"v_add_f32 v247, v247, v19\n"
"v_cvt_pk_bf16_f32 v17, v18, v19\n"
"v_mfma_f32_16x16x32_bf16 a[132:135], v[208:211], v[72:75], a[132:135]\n"
"v_add_f32 v246, v246, v20\n"
"v_add_f32 v247, v247, v21\n"
"v_mfma_f32_16x16x32_bf16 a[136:139], v[208:211], v[80:83], a[136:139]\n"
"v_cvt_pk_bf16_f32 v18, v20, v21\n"
"v_add_f32 v246, v246, v22\n"
"v_mfma_f32_16x16x32_bf16 a[136:139], v[212:215], v[112:115], a[136:139]\n"
"s_memrealtime s[82:83]\n"
"v_exp_f32 v24, v24\n"
"v_mfma_f32_16x16x32_bf16 a[140:143], v[212:215], v[120:123], a[140:143]\n"
"ds_read_b64_tr_b16 v[212:213], v235 offset:37120\n"
"v_add_f32 v247, v247, v23\n"
"v_mfma_f32_16x16x32_bf16 a[140:143], v[208:211], v[88:91], a[140:143]\n"
"s_waitcnt lgkmcnt(5)\n"
"ds_read_b64_tr_b16 v[214:215], v235 offset:43264\n"
"ds_read_b64_tr_b16 v[208:209], v235 offset:24832\n"
"v_mfma_f32_16x16x32_bf16 a[144:147], v[192:195], v[64:67], a[144:147]\n"
"ds_read_b64_tr_b16 v[210:211], v235 offset:30976\n"
"v_cvt_pk_bf16_f32 v19, v22, v23\n"
"v_mfma_f32_16x16x32_bf16 a[144:147], v[196:199], v[96:99], a[144:147]\n"
"v_exp_f32 v25, v25\n"
"v_mfma_f32_16x16x32_bf16 a[148:151], v[196:199], v[104:107], a[148:151]\n"
"v_exp_f32 v26, v26\n"
"v_mfma_f32_16x16x32_bf16 a[148:151], v[192:195], v[72:75], a[148:151]\n"
"v_exp_f32 v27, v27\n"
"v_mfma_f32_16x16x32_bf16 a[152:155], v[192:195], v[80:83], a[152:155]\n"
"v_exp_f32 v28, v28\n"
"v_mfma_f32_16x16x32_bf16 a[152:155], v[196:199], v[112:115], a[152:155]\n"
"v_mfma_f32_16x16x32_bf16 a[156:159], v[196:199], v[120:123], a[156:159]\n"
"s_waitcnt vmcnt(12) lgkmcnt(0)\n"
"s_sub_u32 s83, s94, s82\n"
"s_cmp_gt_i32 s83, 0\n"
"s_cbranch_scc0 Lwrpc2%=\n"
"s_min_i32 s83, s83, 0xc8\n"
"Lwqpc2%=:\n"
"s_sleep 1\n"
"s_sub_u32 s83, s83, 4\n"
"s_cmp_gt_i32 s83, 0\n"
"s_cbranch_scc1 Lwqpc2%=\n"
"Lwrpc2%=:\n"
"s_barrier\n"
"ds_read_b128 v[196:199], v231 offset:24576\n"
"v_mfma_f32_16x16x32_bf16 a[156:159], v[192:195], v[88:91], a[156:159]\n"
"s_sub_u32 s6, 120, s71\n"
"s_max_i32 s6, s6, 4\n"
"ds_read_b128 v[192:195], v230 offset:24576\n"
"v_mfma_f32_16x16x32_bf16 a[160:163], v[200:203], v[64:67], a[160:163]\n"
"s_sub_u32 s82, s82, 16\n"
"v_exp_f32 v29, v29\n"
"v_mfma_f32_16x16x32_bf16 a[160:163], v[204:207], v[96:99], a[160:163]\n"
"s_sub_u32 s7, s71, s29\n"
"s_add_u32 s7, s7, 10\n"
"v_exp_f32 v30, v30\n"
"v_mfma_f32_16x16x32_bf16 a[164:167], v[204:207], v[104:107], a[164:167]\n"
"s_sub_u32 s28, s82, s94\n"
"s_cmp_gt_i32 s28, s6\n"
"s_cselect_b32 s28, s7, -5\n"
"v_exp_f32 v31, v31\n"
"v_mfma_f32_16x16x32_bf16 a[164:167], v[200:203], v[72:75], a[164:167]\n"
"s_add_u32 s71, s71, s28\n"
"s_min_i32 s71, s71, 152\n"
"s_max_i32 s71, s71, s29\n"
"v_add_f32 v248, v248, v24\n"
"v_add_f32 v249, v249, v25\n"
"v_mfma_f32_16x16x32_bf16 a[168:171], v[200:203], v[80:83], a[168:171]\n"
"s_max_i32 s94, s94, s82\n"
"v_cvt_pk_bf16_f32 v24, v24, v25\n"
"v_mfma_f32_16x16x32_bf16 a[168:171], v[204:207], v[112:115], a[168:171]\n"
"s_add_u32 s94, s94, s71\n"
"v_add_f32 v248, v248, v26\n"
"v_mfma_f32_16x16x32_bf16 a[172:175], v[204:207], v[120:123], a[172:175]\n"
"ds_read_b128 v[204:207], v231 offset:24704\n"
"v_add_f32 v249, v249, v27\n"
"v_mfma_f32_16x16x32_bf16 a[172:175], v[200:203], v[88:91], a[172:175]\n"
"s_sub_u32 s28, s71, 36\n"
"s_max_i32 s29, s29, s28\n"
"ds_read_b128 v[200:203], v230 offset:24704\n"
"v_mfma_f32_16x16x32_bf16 a[176:179], v[208:211], v[64:67], a[176:179]\n"
"v_cvt_pk_bf16_f32 v25, v26, v27\n"
"v_add_f32 v248, v248, v28\n"
"v_mfma_f32_16x16x32_bf16 a[176:179], v[212:215], v[96:99], a[176:179]\n"
"v_add_f32 v249, v249, v29\n"
"v_cvt_pk_bf16_f32 v26, v28, v29\n"
"v_mfma_f32_16x16x32_bf16 a[180:183], v[212:215], v[104:107], a[180:183]\n"
"v_add_f32 v248, v248, v30\n"
"v_mfma_f32_16x16x32_bf16 a[180:183], v[208:211], v[72:75], a[180:183]\n"
"v_add_f32 v249, v249, v31\n"
"v_mfma_f32_16x16x32_bf16 a[184:187], v[208:211], v[80:83], a[184:187]\n"
"v_cvt_pk_bf16_f32 v27, v30, v31\n"
"v_mfma_f32_16x16x32_bf16 a[184:187], v[212:215], v[112:115], a[184:187]\n"
"v_exp_f32 v32, v32\n"
"v_mfma_f32_16x16x32_bf16 a[188:191], v[212:215], v[120:123], a[188:191]\n"
"ds_read_b128 v[212:215], v231 offset:24832\n"
"v_mfma_f32_16x16x32_bf16 a[188:191], v[208:211], v[88:91], a[188:191]\n"
"s_waitcnt lgkmcnt(3)\n"
"ds_read_b128 v[208:211], v230 offset:24832\n"
"v_mfma_f32_16x16x32_bf16 v[64:67], v[192:195], a[192:195], v[132:135]\n"
"v_exp_f32 v33, v33\n"
"v_mfma_f32_16x16x32_bf16 v[64:67], v[196:199], a[208:211], v[64:67]\n"
"s_add_u32 m0, s73, 0x12000\n"
"v_exp_f32 v34, v34\n"
"v_mfma_f32_16x16x32_bf16 v[72:75], v[196:199], a[212:215], v[140:143]\n"
"buffer_load_dwordx4 v216, s[48:51], s65 offen offset:0 lds\n"
"v_mfma_f32_16x16x32_bf16 v[72:75], v[192:195], a[196:199], v[72:75]\n"
"v_exp_f32 v35, v35\n"
"v_mfma_f32_16x16x32_bf16 v[80:83], v[192:195], a[200:203], v[148:151]\n"
"v_exp_f32 v36, v36\n"
"v_mfma_f32_16x16x32_bf16 v[80:83], v[196:199], a[216:219], v[80:83]\n"
"v_exp_f32 v37, v37\n"
"v_mfma_f32_16x16x32_bf16 v[88:91], v[196:199], a[220:223], v[156:159]\n"
"ds_read_b128 v[196:199], v231 offset:30720\n"
"v_mfma_f32_16x16x32_bf16 v[88:91], v[192:195], a[204:207], v[88:91]\n"
"s_waitcnt lgkmcnt(3)\n"
"ds_read_b128 v[192:195], v230 offset:30720\n"
"v_mfma_f32_16x16x32_bf16 v[64:67], v[200:203], a[224:227], v[64:67]\n"
"v_exp_f32 v38, v38\n"
"v_mfma_f32_16x16x32_bf16 v[64:67], v[204:207], a[240:243], v[64:67]\n"
"v_exp_f32 v39, v39\n"
"v_mfma_f32_16x16x32_bf16 v[72:75], v[204:207], a[244:247], v[72:75]\n"
"v_add_f32 v242, v242, v32\n"
"v_mfma_f32_16x16x32_bf16 v[72:75], v[200:203], a[228:231], v[72:75]\n"
"v_add_f32 v243, v243, v33\n"
"v_cvt_pk_bf16_f32 v32, v32, v33\n"
"v_mfma_f32_16x16x32_bf16 v[80:83], v[200:203], a[232:235], v[80:83]\n"
"buffer_load_dwordx4 v217, s[48:51], s65 offen offset:1024 lds\n"
"v_mfma_f32_16x16x32_bf16 v[80:83], v[204:207], a[248:251], v[80:83]\n"
"v_add_f32 v242, v242, v34\n"
"v_add_f32 v243, v243, v35\n"
"v_mfma_f32_16x16x32_bf16 v[88:91], v[204:207], a[252:255], v[88:91]\n"
"ds_read_b128 v[204:207], v231 offset:30848\n"
"v_cvt_pk_bf16_f32 v33, v34, v35\n"
"v_mfma_f32_16x16x32_bf16 v[88:91], v[200:203], a[236:239], v[88:91]\n"
"s_waitcnt lgkmcnt(3)\n"
"ds_read_b128 v[200:203], v230 offset:30848\n"
"v_add_f32 v242, v242, v36\n"
"v_mfma_f32_16x16x32_bf16 v[64:67], v[208:211], v[128:131], v[64:67]\n"
"v_add_f32 v243, v243, v37\n"
"v_cvt_pk_bf16_f32 v34, v36, v37\n"
"v_mfma_f32_16x16x32_bf16 v[64:67], v[212:215], v[160:163], v[64:67]\n"
"v_add_f32 v242, v242, v38\n"
"v_mfma_f32_16x16x32_bf16 v[72:75], v[212:215], v[168:171], v[72:75]\n"
"v_add_f32 v243, v243, v39\n"
"v_mfma_f32_16x16x32_bf16 v[72:75], v[208:211], v[136:139], v[72:75]\n"
"v_cvt_pk_bf16_f32 v35, v38, v39\n"
"v_mfma_f32_16x16x32_bf16 v[80:83], v[208:211], v[144:147], v[80:83]\n"
"v_exp_f32 v40, v40\n"
"v_mfma_f32_16x16x32_bf16 v[80:83], v[212:215], v[176:179], v[80:83]\n"
"v_exp_f32 v41, v41\n"
"v_mfma_f32_16x16x32_bf16 v[88:91], v[212:215], v[184:187], v[88:91]\n"
"ds_read_b128 v[212:215], v231 offset:30976\n"
"v_mfma_f32_16x16x32_bf16 v[88:91], v[208:211], v[152:155], v[88:91]\n"
"s_waitcnt lgkmcnt(3)\n"
"ds_read_b128 v[208:211], v230 offset:30976\n"
"v_mfma_f32_16x16x32_bf16 v[68:71], v[192:195], a[192:195], v[132:135]\n"
"v_exp_f32 v42, v42\n"
"v_mfma_f32_16x16x32_bf16 v[68:71], v[196:199], a[208:211], v[68:71]\n"
"v_exp_f32 v43, v43\n"
"v_mfma_f32_16x16x32_bf16 v[76:79], v[196:199], a[212:215], v[140:143]\n"
"buffer_load_dwordx4 v218, s[48:51], s65 offen offset:2048 lds\n"
"v_mfma_f32_16x16x32_bf16 v[76:79], v[192:195], a[196:199], v[76:79]\n"
"v_exp_f32 v44, v44\n"
"v_mfma_f32_16x16x32_bf16 v[84:87], v[192:195], a[200:203], v[148:151]\n"
"v_exp_f32 v45, v45\n"
"v_mfma_f32_16x16x32_bf16 v[84:87], v[196:199], a[216:219], v[84:87]\n"
"v_mfma_f32_16x16x32_bf16 v[92:95], v[196:199], a[220:223], v[156:159]\n"
"ds_read_b128 v[196:199], v231 offset:36864\n"
"v_mfma_f32_16x16x32_bf16 v[92:95], v[192:195], a[204:207], v[92:95]\n"
"s_waitcnt lgkmcnt(3)\n"
"ds_read_b128 v[192:195], v230 offset:36864\n"
"v_mfma_f32_16x16x32_bf16 v[68:71], v[200:203], a[224:227], v[68:71]\n"
"v_exp_f32 v46, v46\n"
"v_mfma_f32_16x16x32_bf16 v[68:71], v[204:207], a[240:243], v[68:71]\n"
"v_exp_f32 v47, v47\n"
"v_mfma_f32_16x16x32_bf16 v[76:79], v[204:207], a[244:247], v[76:79]\n"
"v_add_f32 v244, v244, v40\n"
"v_add_f32 v245, v245, v41\n"
"v_mfma_f32_16x16x32_bf16 v[76:79], v[200:203], a[228:231], v[76:79]\n"
"v_cvt_pk_bf16_f32 v40, v40, v41\n"
"v_mfma_f32_16x16x32_bf16 v[84:87], v[200:203], a[232:235], v[84:87]\n"
"buffer_load_dwordx4 v219, s[48:51], s65 offen offset:3072 lds\n"
"v_mfma_f32_16x16x32_bf16 v[84:87], v[204:207], a[248:251], v[84:87]\n"
"v_add_f32 v244, v244, v42\n"
"v_add_f32 v245, v245, v43\n"
"v_mfma_f32_16x16x32_bf16 v[92:95], v[204:207], a[252:255], v[92:95]\n"
"ds_read_b128 v[204:207], v231 offset:36992\n"
"v_cvt_pk_bf16_f32 v41, v42, v43\n"
"v_mfma_f32_16x16x32_bf16 v[92:95], v[200:203], a[236:239], v[92:95]\n"
"s_waitcnt lgkmcnt(3)\n"
"ds_read_b128 v[200:203], v230 offset:36992\n"
"v_add_f32 v244, v244, v44\n"
"v_mfma_f32_16x16x32_bf16 v[68:71], v[208:211], v[128:131], v[68:71]\n"
"v_add_f32 v245, v245, v45\n"
"v_cvt_pk_bf16_f32 v42, v44, v45\n"
"v_mfma_f32_16x16x32_bf16 v[68:71], v[212:215], v[160:163], v[68:71]\n"
"v_add_f32 v244, v244, v46\n"
"v_add_f32 v245, v245, v47\n"
"v_mfma_f32_16x16x32_bf16 v[76:79], v[212:215], v[168:171], v[76:79]\n"
"v_cvt_pk_bf16_f32 v43, v46, v47\n"
"v_mfma_f32_16x16x32_bf16 v[76:79], v[208:211], v[136:139], v[76:79]\n"
"v_exp_f32 v48, v48\n"
"v_mfma_f32_16x16x32_bf16 v[84:87], v[208:211], v[144:147], v[84:87]\n"
"v_mfma_f32_16x16x32_bf16 v[84:87], v[212:215], v[176:179], v[84:87]\n"
"v_exp_f32 v49, v49\n"
"v_mfma_f32_16x16x32_bf16 v[92:95], v[212:215], v[184:187], v[92:95]\n"
"ds_read_b128 v[212:215], v231 offset:37120\n"
"v_mfma_f32_16x16x32_bf16 v[92:95], v[208:211], v[152:155], v[92:95]\n"
"s_waitcnt lgkmcnt(3)\n"
"ds_read_b128 v[208:211], v230 offset:37120\n"
"v_mfma_f32_16x16x32_bf16 v[96:99], v[192:195], a[192:195], v[132:135]\n"
"v_exp_f32 v50, v50\n"
"v_mfma_f32_16x16x32_bf16 v[96:99], v[196:199], a[208:211], v[96:99]\n"
"s_add_u32 m0, s73, 0x13000\n"
"v_exp_f32 v51, v51\n"
"v_mfma_f32_16x16x32_bf16 v[104:107], v[196:199], a[212:215], v[140:143]\n"
"buffer_load_dwordx4 v220, s[48:51], s65 offen offset:0 lds\n"
"v_mfma_f32_16x16x32_bf16 v[104:107], v[192:195], a[196:199], v[104:107]\n"
"v_exp_f32 v52, v52\n"
"v_mfma_f32_16x16x32_bf16 v[112:115], v[192:195], a[200:203], v[148:151]\n"
"v_exp_f32 v53, v53\n"
"v_mfma_f32_16x16x32_bf16 v[112:115], v[196:199], a[216:219], v[112:115]\n"
"v_exp_f32 v54, v54\n"
"v_mfma_f32_16x16x32_bf16 v[120:123], v[196:199], a[220:223], v[156:159]\n"
"ds_read_b128 v[196:199], v231 offset:43008\n"
"v_mfma_f32_16x16x32_bf16 v[120:123], v[192:195], a[204:207], v[120:123]\n"
"s_waitcnt lgkmcnt(3)\n"
"ds_read_b128 v[192:195], v230 offset:43008\n"
"v_mfma_f32_16x16x32_bf16 v[96:99], v[200:203], a[224:227], v[96:99]\n"
"v_exp_f32 v55, v55\n"
"v_mfma_f32_16x16x32_bf16 v[96:99], v[204:207], a[240:243], v[96:99]\n"
"v_add_f32 v246, v246, v48\n"
"v_add_f32 v247, v247, v49\n"
"v_mfma_f32_16x16x32_bf16 v[104:107], v[204:207], a[244:247], v[104:107]\n"
"v_cvt_pk_bf16_f32 v48, v48, v49\n"
"v_mfma_f32_16x16x32_bf16 v[104:107], v[200:203], a[228:231], v[104:107]\n"
"v_add_f32 v246, v246, v50\n"
"v_mfma_f32_16x16x32_bf16 v[112:115], v[200:203], a[232:235], v[112:115]\n"
"buffer_load_dwordx4 v221, s[48:51], s65 offen offset:1024 lds\n"
"v_mfma_f32_16x16x32_bf16 v[112:115], v[204:207], a[248:251], v[112:115]\n"
"v_add_f32 v247, v247, v51\n"
"v_cvt_pk_bf16_f32 v49, v50, v51\n"
"v_mfma_f32_16x16x32_bf16 v[120:123], v[204:207], a[252:255], v[120:123]\n"
"ds_read_b128 v[204:207], v231 offset:43136\n"
"v_add_f32 v246, v246, v52\n"
"v_mfma_f32_16x16x32_bf16 v[120:123], v[200:203], a[236:239], v[120:123]\n"
"s_waitcnt lgkmcnt(3)\n"
"ds_read_b128 v[200:203], v230 offset:43136\n"
"v_add_f32 v247, v247, v53\n"
"v_mfma_f32_16x16x32_bf16 v[96:99], v[208:211], v[128:131], v[96:99]\n"
"s_add_u32 s65, s65, 0x6000\n"
"v_cvt_pk_bf16_f32 v50, v52, v53\n"
"v_add_f32 v246, v246, v54\n"
"v_mfma_f32_16x16x32_bf16 v[96:99], v[212:215], v[160:163], v[96:99]\n"
"v_add_f32 v247, v247, v55\n"
"v_cvt_pk_bf16_f32 v51, v54, v55\n"
"v_mfma_f32_16x16x32_bf16 v[104:107], v[212:215], v[168:171], v[104:107]\n"
"v_exp_f32 v56, v56\n"
"v_mfma_f32_16x16x32_bf16 v[104:107], v[208:211], v[136:139], v[104:107]\n"
"v_mfma_f32_16x16x32_bf16 v[112:115], v[208:211], v[144:147], v[112:115]\n"
"v_exp_f32 v57, v57\n"
"v_mfma_f32_16x16x32_bf16 v[112:115], v[212:215], v[176:179], v[112:115]\n"
"s_memrealtime s[82:83]\n"
"v_mfma_f32_16x16x32_bf16 v[120:123], v[212:215], v[184:187], v[120:123]\n"
"ds_read_b128 v[212:215], v231 offset:43264\n"
"v_mfma_f32_16x16x32_bf16 v[120:123], v[208:211], v[152:155], v[120:123]\n"
"s_waitcnt lgkmcnt(3)\n"
"ds_read_b128 v[208:211], v230 offset:43264\n"
"v_mfma_f32_16x16x32_bf16 v[100:103], v[192:195], a[192:195], v[132:135]\n"
"v_exp_f32 v58, v58\n"
"v_mfma_f32_16x16x32_bf16 v[100:103], v[196:199], a[208:211], v[100:103]\n"
"v_exp_f32 v59, v59\n"
"v_mfma_f32_16x16x32_bf16 v[108:111], v[196:199], a[212:215], v[140:143]\n"
"v_exp_f32 v60, v60\n"
"v_mfma_f32_16x16x32_bf16 v[108:111], v[192:195], a[196:199], v[108:111]\n"
"v_exp_f32 v61, v61\n"
"v_mfma_f32_16x16x32_bf16 v[116:119], v[192:195], a[200:203], v[148:151]\n"
"v_exp_f32 v62, v62\n"
"v_mfma_f32_16x16x32_bf16 v[116:119], v[196:199], a[216:219], v[116:119]\n"
"v_mfma_f32_16x16x32_bf16 v[124:127], v[196:199], a[220:223], v[156:159]\n"
"s_waitcnt vmcnt(12) lgkmcnt(0)\n"
"s_sub_u32 s83, s94, s82\n"
"s_cmp_gt_i32 s83, 0\n"
"s_cbranch_scc0 Lwrpc3%=\n"
"s_min_i32 s83, s83, 0xc8\n"
"Lwqpc3%=:\n"
"s_sleep 1\n"
"s_sub_u32 s83, s83, 4\n"
"s_cmp_gt_i32 s83, 0\n"
"s_cbranch_scc1 Lwqpc3%=\n"
"Lwrpc3%=:\n"
"s_barrier\n"
"ds_read_b64_tr_b16 v[196:197], v232 offset:12288\n"
"v_mfma_f32_16x16x32_bf16 v[124:127], v[192:195], a[204:207], v[124:127]\n"
"s_sub_u32 s6, 120, s71\n"
"s_max_i32 s6, s6, 4\n"
"ds_read_b64_tr_b16 v[198:199], v232 offset:18432\n"
"ds_read_b64_tr_b16 v[192:193], v232 offset:0\n"
"v_mfma_f32_16x16x32_bf16 v[100:103], v[200:203], a[224:227], v[100:103]\n"
"s_sub_u32 s82, s82, 16\n"
"ds_read_b64_tr_b16 v[194:195], v232 offset:6144\n"
"v_add_f32 v248, v248, v56\n"
"v_mfma_f32_16x16x32_bf16 v[100:103], v[204:207], a[240:243], v[100:103]\n"
"s_sub_u32 s7, s71, s29\n"
"s_add_u32 s7, s7, 10\n"
"v_exp_f32 v63, v63\n"
"v_mfma_f32_16x16x32_bf16 v[108:111], v[204:207], a[244:247], v[108:111]\n"
"s_sub_u32 s28, s82, s94\n"
"s_cmp_gt_i32 s28, s6\n"
"s_cselect_b32 s28, s7, -5\n"
"v_add_f32 v249, v249, v57\n"
"v_cvt_pk_bf16_f32 v56, v56, v57\n"
"v_mfma_f32_16x16x32_bf16 v[108:111], v[200:203], a[228:231], v[108:111]\n"
"s_add_u32 s71, s71, s28\n"
"s_min_i32 s71, s71, 152\n"
"s_max_i32 s71, s71, s29\n"
"v_add_f32 v248, v248, v58\n"
"v_add_f32 v249, v249, v59\n"
"v_mfma_f32_16x16x32_bf16 v[116:119], v[200:203], a[232:235], v[116:119]\n"
"s_max_i32 s94, s94, s82\n"
"v_cvt_pk_bf16_f32 v57, v58, v59\n"
"v_mfma_f32_16x16x32_bf16 v[116:119], v[204:207], a[248:251], v[116:119]\n"
"s_add_u32 s94, s94, s71\n"
"v_add_f32 v248, v248, v60\n"
"v_add_f32 v249, v249, v61\n"
"v_mfma_f32_16x16x32_bf16 v[124:127], v[204:207], a[252:255], v[124:127]\n"
"ds_read_b64_tr_b16 v[204:205], v233 offset:12288\n"
"v_cvt_pk_bf16_f32 v58, v60, v61\n"
"v_mfma_f32_16x16x32_bf16 v[124:127], v[200:203], a[236:239], v[124:127]\n"
"s_sub_u32 s28, s71, 36\n"
"s_max_i32 s29, s29, s28\n"
"ds_read_b64_tr_b16 v[206:207], v233 offset:18432\n"
"ds_read_b64_tr_b16 v[200:201], v233 offset:0\n"
"v_mfma_f32_16x16x32_bf16 v[100:103], v[208:211], v[128:131], v[100:103]\n"
"ds_read_b64_tr_b16 v[202:203], v233 offset:6144\n"
"v_add_f32 v248, v248, v62\n"
"v_mfma_f32_16x16x32_bf16 v[100:103], v[212:215], v[160:163], v[100:103]\n"
"v_add_f32 v249, v249, v63\n"
"v_cvt_pk_bf16_f32 v59, v62, v63\n"
"v_mfma_f32_16x16x32_bf16 v[108:111], v[212:215], v[168:171], v[108:111]\n"
"v_mfma_f32_16x16x32_bf16 v[108:111], v[208:211], v[136:139], v[108:111]\n"
"v_mfma_f32_16x16x32_bf16 v[116:119], v[208:211], v[144:147], v[116:119]\n"
"v_mfma_f32_16x16x32_bf16 v[116:119], v[212:215], v[176:179], v[116:119]\n"
"v_mfma_f32_16x16x32_bf16 v[124:127], v[212:215], v[184:187], v[124:127]\n"
"ds_read_b64_tr_b16 v[212:213], v234 offset:12288\n"
"v_mfma_f32_16x16x32_bf16 v[124:127], v[208:211], v[152:155], v[124:127]\n"
"s_waitcnt lgkmcnt(5)\n"
"ds_read_b64_tr_b16 v[214:215], v234 offset:18432\n"
"ds_read_b64_tr_b16 v[208:209], v234 offset:0\n"
"v_mfma_f32_16x16x32_bf16 a[0:3], v[192:195], v[0:3], a[0:3]\n"
"ds_read_b64_tr_b16 v[210:211], v234 offset:6144\n"
"v_mfma_f32_16x16x32_bf16 a[0:3], v[196:199], v[32:35], a[0:3]\n"
"s_add_u32 m0, s73, 0x6000\n"
"v_exp_f32 v64, v64\n"
"v_mfma_f32_16x16x32_bf16 a[4:7], v[196:199], v[40:43], a[4:7]\n"
"buffer_load_dwordx4 v216, s[44:47], s64 offen offset:0 lds\n"
"v_mfma_f32_16x16x32_bf16 a[4:7], v[192:195], v[8:11], a[4:7]\n"
"v_exp_f32 v65, v65\n"
"v_mfma_f32_16x16x32_bf16 a[8:11], v[192:195], v[16:19], a[8:11]\n"
"v_exp_f32 v66, v66\n"
"v_mfma_f32_16x16x32_bf16 a[8:11], v[196:199], v[48:51], a[8:11]\n"
"v_exp_f32 v67, v67\n"
"v_mfma_f32_16x16x32_bf16 a[12:15], v[196:199], v[56:59], a[12:15]\n"
"ds_read_b64_tr_b16 v[196:197], v235 offset:12288\n"
"v_mfma_f32_16x16x32_bf16 a[12:15], v[192:195], v[24:27], a[12:15]\n"
"s_waitcnt lgkmcnt(5)\n"
"ds_read_b64_tr_b16 v[198:199], v235 offset:18432\n"
"ds_read_b64_tr_b16 v[192:193], v235 offset:0\n"
"v_mfma_f32_16x16x32_bf16 a[16:19], v[200:203], v[0:3], a[16:19]\n"
"ds_read_b64_tr_b16 v[194:195], v235 offset:6144\n"
"v_mfma_f32_16x16x32_bf16 a[16:19], v[204:207], v[32:35], a[16:19]\n"
"v_exp_f32 v68, v68\n"
"v_mfma_f32_16x16x32_bf16 a[20:23], v[204:207], v[40:43], a[20:23]\n"
"v_exp_f32 v69, v69\n"
"v_mfma_f32_16x16x32_bf16 a[20:23], v[200:203], v[8:11], a[20:23]\n"
"v_exp_f32 v70, v70\n"
"v_mfma_f32_16x16x32_bf16 a[24:27], v[200:203], v[16:19], a[24:27]\n"
"buffer_load_dwordx4 v217, s[44:47], s64 offen offset:1024 lds\n"
"v_mfma_f32_16x16x32_bf16 a[24:27], v[204:207], v[48:51], a[24:27]\n"
"v_exp_f32 v71, v71\n"
"v_mfma_f32_16x16x32_bf16 a[28:31], v[204:207], v[56:59], a[28:31]\n"
"ds_read_b64_tr_b16 v[204:205], v232 offset:12416\n"
"v_add_f32 v242, v242, v64\n"
"v_mfma_f32_16x16x32_bf16 a[28:31], v[200:203], v[24:27], a[28:31]\n"
"s_waitcnt lgkmcnt(5)\n"
"ds_read_b64_tr_b16 v[206:207], v232 offset:18560\n"
"ds_read_b64_tr_b16 v[200:201], v232 offset:128\n"
"v_mfma_f32_16x16x32_bf16 a[32:35], v[208:211], v[0:3], a[32:35]\n"
"ds_read_b64_tr_b16 v[202:203], v232 offset:6272\n"
"v_add_f32 v243, v243, v65\n"
"v_mfma_f32_16x16x32_bf16 a[32:35], v[212:215], v[32:35], a[32:35]\n"
"v_cvt_pk_bf16_f32 v64, v64, v65\n"
"v_add_f32 v242, v242, v66\n"
"v_mfma_f32_16x16x32_bf16 a[36:39], v[212:215], v[40:43], a[36:39]\n"
"v_add_f32 v243, v243, v67\n"
"v_cvt_pk_bf16_f32 v65, v66, v67\n"
"v_mfma_f32_16x16x32_bf16 a[36:39], v[208:211], v[8:11], a[36:39]\n"
"v_add_f32 v242, v242, v68\n"
"v_add_f32 v243, v243, v69\n"
"v_mfma_f32_16x16x32_bf16 a[40:43], v[208:211], v[16:19], a[40:43]\n"
"v_cvt_pk_bf16_f32 v66, v68, v69\n"
"v_add_f32 v242, v242, v70\n"
"v_mfma_f32_16x16x32_bf16 a[40:43], v[212:215], v[48:51], a[40:43]\n"
"v_add_f32 v243, v243, v71\n"
"v_mfma_f32_16x16x32_bf16 a[44:47], v[212:215], v[56:59], a[44:47]\n"
"ds_read_b64_tr_b16 v[212:213], v233 offset:12416\n"
"v_cvt_pk_bf16_f32 v67, v70, v71\n"
"v_mfma_f32_16x16x32_bf16 a[44:47], v[208:211], v[24:27], a[44:47]\n"
"s_waitcnt lgkmcnt(5)\n"
"ds_read_b64_tr_b16 v[214:215], v233 offset:18560\n"
"ds_read_b64_tr_b16 v[208:209], v233 offset:128\n"
"v_mfma_f32_16x16x32_bf16 a[48:51], v[192:195], v[0:3], a[48:51]\n"
"ds_read_b64_tr_b16 v[210:211], v233 offset:6272\n"
"v_mfma_f32_16x16x32_bf16 a[48:51], v[196:199], v[32:35], a[48:51]\n"
"v_exp_f32 v72, v72\n"
"v_mfma_f32_16x16x32_bf16 a[52:55], v[196:199], v[40:43], a[52:55]\n"
"buffer_load_dwordx4 v218, s[44:47], s64 offen offset:2048 lds\n"
"v_mfma_f32_16x16x32_bf16 a[52:55], v[192:195], v[8:11], a[52:55]\n"
"v_exp_f32 v73, v73\n"
"v_mfma_f32_16x16x32_bf16 a[56:59], v[192:195], v[16:19], a[56:59]\n"
"v_exp_f32 v74, v74\n"
"v_mfma_f32_16x16x32_bf16 a[56:59], v[196:199], v[48:51], a[56:59]\n"
"v_exp_f32 v75, v75\n"
"v_mfma_f32_16x16x32_bf16 a[60:63], v[196:199], v[56:59], a[60:63]\n"
"ds_read_b64_tr_b16 v[196:197], v234 offset:12416\n"
"v_mfma_f32_16x16x32_bf16 a[60:63], v[192:195], v[24:27], a[60:63]\n"
"s_waitcnt lgkmcnt(5)\n"
"ds_read_b64_tr_b16 v[198:199], v234 offset:18560\n"
"ds_read_b64_tr_b16 v[192:193], v234 offset:128\n"
"v_mfma_f32_16x16x32_bf16 a[64:67], v[200:203], v[0:3], a[64:67]\n"
"ds_read_b64_tr_b16 v[194:195], v234 offset:6272\n"
"v_mfma_f32_16x16x32_bf16 a[64:67], v[204:207], v[32:35], a[64:67]\n"
"v_exp_f32 v76, v76\n"
"v_mfma_f32_16x16x32_bf16 a[68:71], v[204:207], v[40:43], a[68:71]\n"
"v_exp_f32 v77, v77\n"
"v_mfma_f32_16x16x32_bf16 a[68:71], v[200:203], v[8:11], a[68:71]\n"
"v_exp_f32 v78, v78\n"
"v_mfma_f32_16x16x32_bf16 a[72:75], v[200:203], v[16:19], a[72:75]\n"
"buffer_load_dwordx4 v219, s[44:47], s64 offen offset:3072 lds\n"
"v_mfma_f32_16x16x32_bf16 a[72:75], v[204:207], v[48:51], a[72:75]\n"
"v_exp_f32 v79, v79\n"
"v_mfma_f32_16x16x32_bf16 a[76:79], v[204:207], v[56:59], a[76:79]\n"
"ds_read_b64_tr_b16 v[204:205], v235 offset:12416\n"
"v_add_f32 v244, v244, v72\n"
"v_mfma_f32_16x16x32_bf16 a[76:79], v[200:203], v[24:27], a[76:79]\n"
"s_waitcnt lgkmcnt(5)\n"
"ds_read_b64_tr_b16 v[206:207], v235 offset:18560\n"
"ds_read_b64_tr_b16 v[200:201], v235 offset:128\n"
"v_mfma_f32_16x16x32_bf16 a[80:83], v[208:211], v[0:3], a[80:83]\n"
"ds_read_b64_tr_b16 v[202:203], v235 offset:6272\n"
"v_add_f32 v245, v245, v73\n"
"v_mfma_f32_16x16x32_bf16 a[80:83], v[212:215], v[32:35], a[80:83]\n"
"v_cvt_pk_bf16_f32 v72, v72, v73\n"
"v_add_f32 v244, v244, v74\n"
"v_mfma_f32_16x16x32_bf16 a[84:87], v[212:215], v[40:43], a[84:87]\n"
"v_add_f32 v245, v245, v75\n"
"v_cvt_pk_bf16_f32 v73, v74, v75\n"
"v_mfma_f32_16x16x32_bf16 a[84:87], v[208:211], v[8:11], a[84:87]\n"
"v_add_f32 v244, v244, v76\n"
"v_add_f32 v245, v245, v77\n"
"v_mfma_f32_16x16x32_bf16 a[88:91], v[208:211], v[16:19], a[88:91]\n"
"v_cvt_pk_bf16_f32 v74, v76, v77\n"
"v_add_f32 v244, v244, v78\n"
"v_mfma_f32_16x16x32_bf16 a[88:91], v[212:215], v[48:51], a[88:91]\n"
"v_add_f32 v245, v245, v79\n"
"v_cvt_pk_bf16_f32 v75, v78, v79\n"
"v_mfma_f32_16x16x32_bf16 a[92:95], v[212:215], v[56:59], a[92:95]\n"
"ds_read_b64_tr_b16 v[212:213], v232 offset:12544\n"
"v_mfma_f32_16x16x32_bf16 a[92:95], v[208:211], v[24:27], a[92:95]\n"
"s_waitcnt lgkmcnt(5)\n"
"ds_read_b64_tr_b16 v[214:215], v232 offset:18688\n"
"ds_read_b64_tr_b16 v[208:209], v232 offset:256\n"
"v_mfma_f32_16x16x32_bf16 a[96:99], v[192:195], v[0:3], a[96:99]\n"
"ds_read_b64_tr_b16 v[210:211], v232 offset:6400\n"
"v_mfma_f32_16x16x32_bf16 a[96:99], v[196:199], v[32:35], a[96:99]\n"
"s_add_u32 m0, s73, 0x7000\n"
"v_exp_f32 v80, v80\n"
"v_mfma_f32_16x16x32_bf16 a[100:103], v[196:199], v[40:43], a[100:103]\n"
"buffer_load_dwordx4 v220, s[44:47], s64 offen offset:0 lds\n"
"v_mfma_f32_16x16x32_bf16 a[100:103], v[192:195], v[8:11], a[100:103]\n"
"v_exp_f32 v81, v81\n"
"v_mfma_f32_16x16x32_bf16 a[104:107], v[192:195], v[16:19], a[104:107]\n"
"v_exp_f32 v82, v82\n"
"v_mfma_f32_16x16x32_bf16 a[104:107], v[196:199], v[48:51], a[104:107]\n"
"v_exp_f32 v83, v83\n"
"v_mfma_f32_16x16x32_bf16 a[108:111], v[196:199], v[56:59], a[108:111]\n"
"ds_read_b64_tr_b16 v[196:197], v233 offset:12544\n"
"v_mfma_f32_16x16x32_bf16 a[108:111], v[192:195], v[24:27], a[108:111]\n"
"s_waitcnt lgkmcnt(5)\n"
"ds_read_b64_tr_b16 v[198:199], v233 offset:18688\n"
"ds_read_b64_tr_b16 v[192:193], v233 offset:256\n"
"v_mfma_f32_16x16x32_bf16 a[112:115], v[200:203], v[0:3], a[112:115]\n"
"ds_read_b64_tr_b16 v[194:195], v233 offset:6400\n"
"v_mfma_f32_16x16x32_bf16 a[112:115], v[204:207], v[32:35], a[112:115]\n"
"v_exp_f32 v84, v84\n"
"v_mfma_f32_16x16x32_bf16 a[116:119], v[204:207], v[40:43], a[116:119]\n"
"v_exp_f32 v85, v85\n"
"v_mfma_f32_16x16x32_bf16 a[116:119], v[200:203], v[8:11], a[116:119]\n"
"v_exp_f32 v86, v86\n"
"v_mfma_f32_16x16x32_bf16 a[120:123], v[200:203], v[16:19], a[120:123]\n"
"buffer_load_dwordx4 v221, s[44:47], s64 offen offset:1024 lds\n"
"v_mfma_f32_16x16x32_bf16 a[120:123], v[204:207], v[48:51], a[120:123]\n"
"v_exp_f32 v87, v87\n"
"v_mfma_f32_16x16x32_bf16 a[124:127], v[204:207], v[56:59], a[124:127]\n"
"ds_read_b64_tr_b16 v[204:205], v234 offset:12544\n"
"v_add_f32 v246, v246, v80\n"
"v_mfma_f32_16x16x32_bf16 a[124:127], v[200:203], v[24:27], a[124:127]\n"
"s_waitcnt lgkmcnt(5)\n"
"ds_read_b64_tr_b16 v[206:207], v234 offset:18688\n"
"ds_read_b64_tr_b16 v[200:201], v234 offset:256\n"
"v_mfma_f32_16x16x32_bf16 a[128:131], v[208:211], v[0:3], a[128:131]\n"
"s_add_u32 s64, s64, 0x6000\n"
"ds_read_b64_tr_b16 v[202:203], v234 offset:6400\n"
"v_add_f32 v247, v247, v81\n"
"v_mfma_f32_16x16x32_bf16 a[128:131], v[212:215], v[32:35], a[128:131]\n"
"v_cvt_pk_bf16_f32 v80, v80, v81\n"
"v_add_f32 v246, v246, v82\n"
"v_mfma_f32_16x16x32_bf16 a[132:135], v[212:215], v[40:43], a[132:135]\n"
"v_add_f32 v247, v247, v83\n"
"v_cvt_pk_bf16_f32 v81, v82, v83\n"
"v_mfma_f32_16x16x32_bf16 a[132:135], v[208:211], v[8:11], a[132:135]\n"
"v_add_f32 v246, v246, v84\n"
"v_add_f32 v247, v247, v85\n"
"v_mfma_f32_16x16x32_bf16 a[136:139], v[208:211], v[16:19], a[136:139]\n"
"v_cvt_pk_bf16_f32 v82, v84, v85\n"
"v_add_f32 v246, v246, v86\n"
"v_mfma_f32_16x16x32_bf16 a[136:139], v[212:215], v[48:51], a[136:139]\n"
"s_memrealtime s[82:83]\n"
"v_exp_f32 v88, v88\n"
"v_mfma_f32_16x16x32_bf16 a[140:143], v[212:215], v[56:59], a[140:143]\n"
"ds_read_b64_tr_b16 v[212:213], v235 offset:12544\n"
"v_add_f32 v247, v247, v87\n"
"v_mfma_f32_16x16x32_bf16 a[140:143], v[208:211], v[24:27], a[140:143]\n"
"s_waitcnt lgkmcnt(5)\n"
"ds_read_b64_tr_b16 v[214:215], v235 offset:18688\n"
"ds_read_b64_tr_b16 v[208:209], v235 offset:256\n"
"v_mfma_f32_16x16x32_bf16 a[144:147], v[192:195], v[0:3], a[144:147]\n"
"ds_read_b64_tr_b16 v[210:211], v235 offset:6400\n"
"v_cvt_pk_bf16_f32 v83, v86, v87\n"
"v_mfma_f32_16x16x32_bf16 a[144:147], v[196:199], v[32:35], a[144:147]\n"
"v_exp_f32 v89, v89\n"
"v_mfma_f32_16x16x32_bf16 a[148:151], v[196:199], v[40:43], a[148:151]\n"
"v_exp_f32 v90, v90\n"
"v_mfma_f32_16x16x32_bf16 a[148:151], v[192:195], v[8:11], a[148:151]\n"
"v_exp_f32 v91, v91\n"
"v_mfma_f32_16x16x32_bf16 a[152:155], v[192:195], v[16:19], a[152:155]\n"
"v_exp_f32 v92, v92\n"
"v_mfma_f32_16x16x32_bf16 a[152:155], v[196:199], v[48:51], a[152:155]\n"
"v_mfma_f32_16x16x32_bf16 a[156:159], v[196:199], v[56:59], a[156:159]\n"
"s_waitcnt vmcnt(12) lgkmcnt(0)\n"
"s_sub_u32 s83, s94, s82\n"
"s_cmp_gt_i32 s83, 0\n"
"s_cbranch_scc0 Lwrpc4%=\n"
"s_min_i32 s83, s83, 0xc8\n"
"Lwqpc4%=:\n"
"s_sleep 1\n"
"s_sub_u32 s83, s83, 4\n"
"s_cmp_gt_i32 s83, 0\n"
"s_cbranch_scc1 Lwqpc4%=\n"
"Lwrpc4%=:\n"
"s_barrier\n"
"ds_read_b128 v[196:199], v231 offset:0\n"
"v_mfma_f32_16x16x32_bf16 a[156:159], v[192:195], v[24:27], a[156:159]\n"
"s_sub_u32 s6, 120, s71\n"
"s_max_i32 s6, s6, 4\n"
"ds_read_b128 v[192:195], v230 offset:0\n"
"v_mfma_f32_16x16x32_bf16 a[160:163], v[200:203], v[0:3], a[160:163]\n"
"s_sub_u32 s82, s82, 16\n"
"v_exp_f32 v93, v93\n"
"v_mfma_f32_16x16x32_bf16 a[160:163], v[204:207], v[32:35], a[160:163]\n"
"s_sub_u32 s7, s71, s29\n"
"s_add_u32 s7, s7, 10\n"
"v_exp_f32 v94, v94\n"
"v_mfma_f32_16x16x32_bf16 a[164:167], v[204:207], v[40:43], a[164:167]\n"
"s_sub_u32 s28, s82, s94\n"
"s_cmp_gt_i32 s28, s6\n"
"s_cselect_b32 s28, s7, -5\n"
"v_exp_f32 v95, v95\n"
"v_mfma_f32_16x16x32_bf16 a[164:167], v[200:203], v[8:11], a[164:167]\n"
"s_add_u32 s71, s71, s28\n"
"s_min_i32 s71, s71, 152\n"
"s_max_i32 s71, s71, s29\n"
"v_add_f32 v248, v248, v88\n"
"v_add_f32 v249, v249, v89\n"
"v_mfma_f32_16x16x32_bf16 a[168:171], v[200:203], v[16:19], a[168:171]\n"
"s_max_i32 s94, s94, s82\n"
"v_cvt_pk_bf16_f32 v88, v88, v89\n"
"v_mfma_f32_16x16x32_bf16 a[168:171], v[204:207], v[48:51], a[168:171]\n"
"s_add_u32 s94, s94, s71\n"
"v_add_f32 v248, v248, v90\n"
"v_mfma_f32_16x16x32_bf16 a[172:175], v[204:207], v[56:59], a[172:175]\n"
"ds_read_b128 v[204:207], v231 offset:128\n"
"v_add_f32 v249, v249, v91\n"
"v_mfma_f32_16x16x32_bf16 a[172:175], v[200:203], v[24:27], a[172:175]\n"
"s_sub_u32 s28, s71, 36\n"
"s_max_i32 s29, s29, s28\n"
"ds_read_b128 v[200:203], v230 offset:128\n"
"v_mfma_f32_16x16x32_bf16 a[176:179], v[208:211], v[0:3], a[176:179]\n"
"v_cvt_pk_bf16_f32 v89, v90, v91\n"
"v_add_f32 v248, v248, v92\n"
"v_mfma_f32_16x16x32_bf16 a[176:179], v[212:215], v[32:35], a[176:179]\n"
"v_add_f32 v249, v249, v93\n"
"v_cvt_pk_bf16_f32 v90, v92, v93\n"
"v_mfma_f32_16x16x32_bf16 a[180:183], v[212:215], v[40:43], a[180:183]\n"
"v_add_f32 v248, v248, v94\n"
"v_mfma_f32_16x16x32_bf16 a[180:183], v[208:211], v[8:11], a[180:183]\n"
"v_add_f32 v249, v249, v95\n"
"v_mfma_f32_16x16x32_bf16 a[184:187], v[208:211], v[16:19], a[184:187]\n"
"v_cvt_pk_bf16_f32 v91, v94, v95\n"
"v_mfma_f32_16x16x32_bf16 a[184:187], v[212:215], v[48:51], a[184:187]\n"
"v_exp_f32 v96, v96\n"
"v_mfma_f32_16x16x32_bf16 a[188:191], v[212:215], v[56:59], a[188:191]\n"
"ds_read_b128 v[212:215], v231 offset:256\n"
"v_mfma_f32_16x16x32_bf16 a[188:191], v[208:211], v[24:27], a[188:191]\n"
"s_waitcnt lgkmcnt(3)\n"
"ds_read_b128 v[208:211], v230 offset:256\n"
"v_mfma_f32_16x16x32_bf16 v[0:3], v[192:195], a[192:195], v[132:135]\n"
"v_exp_f32 v97, v97\n"
"v_mfma_f32_16x16x32_bf16 v[0:3], v[196:199], a[208:211], v[0:3]\n"
"s_add_u32 m0, s73, 0xc000\n"
"v_exp_f32 v98, v98\n"
"v_mfma_f32_16x16x32_bf16 v[8:11], v[196:199], a[212:215], v[140:143]\n"
"buffer_load_dwordx4 v216, s[48:51], s65 offen offset:0 lds\n"
"v_mfma_f32_16x16x32_bf16 v[8:11], v[192:195], a[196:199], v[8:11]\n"
"v_exp_f32 v99, v99\n"
"v_mfma_f32_16x16x32_bf16 v[16:19], v[192:195], a[200:203], v[148:151]\n"
"v_exp_f32 v100, v100\n"
"v_mfma_f32_16x16x32_bf16 v[16:19], v[196:199], a[216:219], v[16:19]\n"
"v_exp_f32 v101, v101\n"
"v_mfma_f32_16x16x32_bf16 v[24:27], v[196:199], a[220:223], v[156:159]\n"
"ds_read_b128 v[196:199], v231 offset:6144\n"
"v_mfma_f32_16x16x32_bf16 v[24:27], v[192:195], a[204:207], v[24:27]\n"
"s_waitcnt lgkmcnt(3)\n"
"ds_read_b128 v[192:195], v230 offset:6144\n"
"v_mfma_f32_16x16x32_bf16 v[0:3], v[200:203], a[224:227], v[0:3]\n"
"v_exp_f32 v102, v102\n"
"v_mfma_f32_16x16x32_bf16 v[0:3], v[204:207], a[240:243], v[0:3]\n"
"v_exp_f32 v103, v103\n"
"v_mfma_f32_16x16x32_bf16 v[8:11], v[204:207], a[244:247], v[8:11]\n"
"v_add_f32 v242, v242, v96\n"
"v_mfma_f32_16x16x32_bf16 v[8:11], v[200:203], a[228:231], v[8:11]\n"
"v_add_f32 v243, v243, v97\n"
"v_cvt_pk_bf16_f32 v96, v96, v97\n"
"v_mfma_f32_16x16x32_bf16 v[16:19], v[200:203], a[232:235], v[16:19]\n"
"buffer_load_dwordx4 v217, s[48:51], s65 offen offset:1024 lds\n"
"v_mfma_f32_16x16x32_bf16 v[16:19], v[204:207], a[248:251], v[16:19]\n"
"v_add_f32 v242, v242, v98\n"
"v_add_f32 v243, v243, v99\n"
"v_mfma_f32_16x16x32_bf16 v[24:27], v[204:207], a[252:255], v[24:27]\n"
"ds_read_b128 v[204:207], v231 offset:6272\n"
"v_cvt_pk_bf16_f32 v97, v98, v99\n"
"v_mfma_f32_16x16x32_bf16 v[24:27], v[200:203], a[236:239], v[24:27]\n"
"s_waitcnt lgkmcnt(3)\n"
"ds_read_b128 v[200:203], v230 offset:6272\n"
"v_add_f32 v242, v242, v100\n"
"v_mfma_f32_16x16x32_bf16 v[0:3], v[208:211], v[128:131], v[0:3]\n"
"v_add_f32 v243, v243, v101\n"
"v_cvt_pk_bf16_f32 v98, v100, v101\n"
"v_mfma_f32_16x16x32_bf16 v[0:3], v[212:215], v[160:163], v[0:3]\n"
"v_add_f32 v242, v242, v102\n"
"v_mfma_f32_16x16x32_bf16 v[8:11], v[212:215], v[168:171], v[8:11]\n"
"v_add_f32 v243, v243, v103\n"
"v_mfma_f32_16x16x32_bf16 v[8:11], v[208:211], v[136:139], v[8:11]\n"
"v_cvt_pk_bf16_f32 v99, v102, v103\n"
"v_mfma_f32_16x16x32_bf16 v[16:19], v[208:211], v[144:147], v[16:19]\n"
"v_exp_f32 v104, v104\n"
"v_mfma_f32_16x16x32_bf16 v[16:19], v[212:215], v[176:179], v[16:19]\n"
"v_exp_f32 v105, v105\n"
"v_mfma_f32_16x16x32_bf16 v[24:27], v[212:215], v[184:187], v[24:27]\n"
"ds_read_b128 v[212:215], v231 offset:6400\n"
"v_mfma_f32_16x16x32_bf16 v[24:27], v[208:211], v[152:155], v[24:27]\n"
"s_waitcnt lgkmcnt(3)\n"
"ds_read_b128 v[208:211], v230 offset:6400\n"
"v_mfma_f32_16x16x32_bf16 v[4:7], v[192:195], a[192:195], v[132:135]\n"
"v_exp_f32 v106, v106\n"
"v_mfma_f32_16x16x32_bf16 v[4:7], v[196:199], a[208:211], v[4:7]\n"
"v_exp_f32 v107, v107\n"
"v_mfma_f32_16x16x32_bf16 v[12:15], v[196:199], a[212:215], v[140:143]\n"
"buffer_load_dwordx4 v218, s[48:51], s65 offen offset:2048 lds\n"
"v_mfma_f32_16x16x32_bf16 v[12:15], v[192:195], a[196:199], v[12:15]\n"
"v_exp_f32 v108, v108\n"
"v_mfma_f32_16x16x32_bf16 v[20:23], v[192:195], a[200:203], v[148:151]\n"
"v_exp_f32 v109, v109\n"
"v_mfma_f32_16x16x32_bf16 v[20:23], v[196:199], a[216:219], v[20:23]\n"
"v_mfma_f32_16x16x32_bf16 v[28:31], v[196:199], a[220:223], v[156:159]\n"
"ds_read_b128 v[196:199], v231 offset:12288\n"
"v_mfma_f32_16x16x32_bf16 v[28:31], v[192:195], a[204:207], v[28:31]\n"
"s_waitcnt lgkmcnt(3)\n"
"ds_read_b128 v[192:195], v230 offset:12288\n"
"v_mfma_f32_16x16x32_bf16 v[4:7], v[200:203], a[224:227], v[4:7]\n"
"v_exp_f32 v110, v110\n"
"v_mfma_f32_16x16x32_bf16 v[4:7], v[204:207], a[240:243], v[4:7]\n"
"v_exp_f32 v111, v111\n"
"v_mfma_f32_16x16x32_bf16 v[12:15], v[204:207], a[244:247], v[12:15]\n"
"v_add_f32 v244, v244, v104\n"
"v_add_f32 v245, v245, v105\n"
"v_mfma_f32_16x16x32_bf16 v[12:15], v[200:203], a[228:231], v[12:15]\n"
"v_cvt_pk_bf16_f32 v104, v104, v105\n"
"v_mfma_f32_16x16x32_bf16 v[20:23], v[200:203], a[232:235], v[20:23]\n"
"buffer_load_dwordx4 v219, s[48:51], s65 offen offset:3072 lds\n"
"v_mfma_f32_16x16x32_bf16 v[20:23], v[204:207], a[248:251], v[20:23]\n"
"v_add_f32 v244, v244, v106\n"
"v_add_f32 v245, v245, v107\n"
"v_mfma_f32_16x16x32_bf16 v[28:31], v[204:207], a[252:255], v[28:31]\n"
"ds_read_b128 v[204:207], v231 offset:12416\n"
"v_cvt_pk_bf16_f32 v105, v106, v107\n"
"v_mfma_f32_16x16x32_bf16 v[28:31], v[200:203], a[236:239], v[28:31]\n"
"s_waitcnt lgkmcnt(3)\n"
"ds_read_b128 v[200:203], v230 offset:12416\n"
"v_add_f32 v244, v244, v108\n"
"v_mfma_f32_16x16x32_bf16 v[4:7], v[208:211], v[128:131], v[4:7]\n"
"v_add_f32 v245, v245, v109\n"
"v_cvt_pk_bf16_f32 v106, v108, v109\n"
"v_mfma_f32_16x16x32_bf16 v[4:7], v[212:215], v[160:163], v[4:7]\n"
"v_add_f32 v244, v244, v110\n"
"v_add_f32 v245, v245, v111\n"
"v_mfma_f32_16x16x32_bf16 v[12:15], v[212:215], v[168:171], v[12:15]\n"
"v_cvt_pk_bf16_f32 v107, v110, v111\n"
"v_mfma_f32_16x16x32_bf16 v[12:15], v[208:211], v[136:139], v[12:15]\n"
"v_exp_f32 v112, v112\n"
"v_mfma_f32_16x16x32_bf16 v[20:23], v[208:211], v[144:147], v[20:23]\n"
"v_mfma_f32_16x16x32_bf16 v[20:23], v[212:215], v[176:179], v[20:23]\n"
"v_exp_f32 v113, v113\n"
"v_mfma_f32_16x16x32_bf16 v[28:31], v[212:215], v[184:187], v[28:31]\n"
"ds_read_b128 v[212:215], v231 offset:12544\n"
"v_mfma_f32_16x16x32_bf16 v[28:31], v[208:211], v[152:155], v[28:31]\n"
"s_waitcnt lgkmcnt(3)\n"
"ds_read_b128 v[208:211], v230 offset:12544\n"
"v_mfma_f32_16x16x32_bf16 v[32:35], v[192:195], a[192:195], v[132:135]\n"
"v_exp_f32 v114, v114\n"
"v_mfma_f32_16x16x32_bf16 v[32:35], v[196:199], a[208:211], v[32:35]\n"
"s_add_u32 m0, s73, 0xd000\n"
"v_exp_f32 v115, v115\n"
"v_mfma_f32_16x16x32_bf16 v[40:43], v[196:199], a[212:215], v[140:143]\n"
"buffer_load_dwordx4 v220, s[48:51], s65 offen offset:0 lds\n"
"v_mfma_f32_16x16x32_bf16 v[40:43], v[192:195], a[196:199], v[40:43]\n"
"v_exp_f32 v116, v116\n"
"v_mfma_f32_16x16x32_bf16 v[48:51], v[192:195], a[200:203], v[148:151]\n"
"v_exp_f32 v117, v117\n"
"v_mfma_f32_16x16x32_bf16 v[48:51], v[196:199], a[216:219], v[48:51]\n"
"v_exp_f32 v118, v118\n"
"v_mfma_f32_16x16x32_bf16 v[56:59], v[196:199], a[220:223], v[156:159]\n"
"ds_read_b128 v[196:199], v231 offset:18432\n"
"v_mfma_f32_16x16x32_bf16 v[56:59], v[192:195], a[204:207], v[56:59]\n"
"s_waitcnt lgkmcnt(3)\n"
"ds_read_b128 v[192:195], v230 offset:18432\n"
"v_mfma_f32_16x16x32_bf16 v[32:35], v[200:203], a[224:227], v[32:35]\n"
"v_exp_f32 v119, v119\n"
"v_mfma_f32_16x16x32_bf16 v[32:35], v[204:207], a[240:243], v[32:35]\n"
"v_add_f32 v246, v246, v112\n"
"v_add_f32 v247, v247, v113\n"
"v_mfma_f32_16x16x32_bf16 v[40:43], v[204:207], a[244:247], v[40:43]\n"
"v_cvt_pk_bf16_f32 v112, v112, v113\n"
"v_mfma_f32_16x16x32_bf16 v[40:43], v[200:203], a[228:231], v[40:43]\n"
"v_add_f32 v246, v246, v114\n"
"v_mfma_f32_16x16x32_bf16 v[48:51], v[200:203], a[232:235], v[48:51]\n"
"buffer_load_dwordx4 v221, s[48:51], s65 offen offset:1024 lds\n"
"v_mfma_f32_16x16x32_bf16 v[48:51], v[204:207], a[248:251], v[48:51]\n"
"v_add_f32 v247, v247, v115\n"
"v_cvt_pk_bf16_f32 v113, v114, v115\n"
"v_mfma_f32_16x16x32_bf16 v[56:59], v[204:207], a[252:255], v[56:59]\n"
"ds_read_b128 v[204:207], v231 offset:18560\n"
"v_add_f32 v246, v246, v116\n"
"v_mfma_f32_16x16x32_bf16 v[56:59], v[200:203], a[236:239], v[56:59]\n"
"s_waitcnt lgkmcnt(3)\n"
"ds_read_b128 v[200:203], v230 offset:18560\n"
"v_add_f32 v247, v247, v117\n"
"v_mfma_f32_16x16x32_bf16 v[32:35], v[208:211], v[128:131], v[32:35]\n"
"s_add_u32 s65, s65, 0x6000\n"
"v_cvt_pk_bf16_f32 v114, v116, v117\n"
"v_add_f32 v246, v246, v118\n"
"v_mfma_f32_16x16x32_bf16 v[32:35], v[212:215], v[160:163], v[32:35]\n"
"v_add_f32 v247, v247, v119\n"
"v_cvt_pk_bf16_f32 v115, v118, v119\n"
"v_mfma_f32_16x16x32_bf16 v[40:43], v[212:215], v[168:171], v[40:43]\n"
"v_exp_f32 v120, v120\n"
"v_mfma_f32_16x16x32_bf16 v[40:43], v[208:211], v[136:139], v[40:43]\n"
"v_mfma_f32_16x16x32_bf16 v[48:51], v[208:211], v[144:147], v[48:51]\n"
"v_exp_f32 v121, v121\n"
"v_mfma_f32_16x16x32_bf16 v[48:51], v[212:215], v[176:179], v[48:51]\n"
"s_memrealtime s[82:83]\n"
"v_mfma_f32_16x16x32_bf16 v[56:59], v[212:215], v[184:187], v[56:59]\n"
"ds_read_b128 v[212:215], v231 offset:18688\n"
"v_mfma_f32_16x16x32_bf16 v[56:59], v[208:211], v[152:155], v[56:59]\n"
"s_waitcnt lgkmcnt(3)\n"
"ds_read_b128 v[208:211], v230 offset:18688\n"
"v_mfma_f32_16x16x32_bf16 v[36:39], v[192:195], a[192:195], v[132:135]\n"
"v_exp_f32 v122, v122\n"
"v_mfma_f32_16x16x32_bf16 v[36:39], v[196:199], a[208:211], v[36:39]\n"
"v_exp_f32 v123, v123\n"
"v_mfma_f32_16x16x32_bf16 v[44:47], v[196:199], a[212:215], v[140:143]\n"
"v_exp_f32 v124, v124\n"
"v_mfma_f32_16x16x32_bf16 v[44:47], v[192:195], a[196:199], v[44:47]\n"
"v_exp_f32 v125, v125\n"
"v_mfma_f32_16x16x32_bf16 v[52:55], v[192:195], a[200:203], v[148:151]\n"
"v_exp_f32 v126, v126\n"
"v_mfma_f32_16x16x32_bf16 v[52:55], v[196:199], a[216:219], v[52:55]\n"
"v_mfma_f32_16x16x32_bf16 v[60:63], v[196:199], a[220:223], v[156:159]\n"
"s_waitcnt vmcnt(12) lgkmcnt(0)\n"
"s_sub_u32 s83, s94, s82\n"
"s_cmp_gt_i32 s83, 0\n"
"s_cbranch_scc0 Lwrpc5%=\n"
"s_min_i32 s83, s83, 0xc8\n"
"Lwqpc5%=:\n"
"s_sleep 1\n"
"s_sub_u32 s83, s83, 4\n"
"s_cmp_gt_i32 s83, 0\n"
"s_cbranch_scc1 Lwqpc5%=\n"
"Lwrpc5%=:\n"
"s_barrier\n"
"ds_read_b64_tr_b16 v[196:197], v232 offset:36864\n"
"v_mfma_f32_16x16x32_bf16 v[60:63], v[192:195], a[204:207], v[60:63]\n"
"s_sub_u32 s6, 120, s71\n"
"s_max_i32 s6, s6, 4\n"
"ds_read_b64_tr_b16 v[198:199], v232 offset:43008\n"
"ds_read_b64_tr_b16 v[192:193], v232 offset:24576\n"
"v_mfma_f32_16x16x32_bf16 v[36:39], v[200:203], a[224:227], v[36:39]\n"
"s_sub_u32 s82, s82, 16\n"
"ds_read_b64_tr_b16 v[194:195], v232 offset:30720\n"
"v_add_f32 v248, v248, v120\n"
"v_mfma_f32_16x16x32_bf16 v[36:39], v[204:207], a[240:243], v[36:39]\n"
"s_sub_u32 s7, s71, s29\n"
"s_add_u32 s7, s7, 10\n"
"v_exp_f32 v127, v127\n"
"v_mfma_f32_16x16x32_bf16 v[44:47], v[204:207], a[244:247], v[44:47]\n"
"s_sub_u32 s28, s82, s94\n"
"s_cmp_gt_i32 s28, s6\n"
"s_cselect_b32 s28, s7, -5\n"
"v_add_f32 v249, v249, v121\n"
"v_cvt_pk_bf16_f32 v120, v120, v121\n"
"v_mfma_f32_16x16x32_bf16 v[44:47], v[200:203], a[228:231], v[44:47]\n"
"s_add_u32 s71, s71, s28\n"
"s_min_i32 s71, s71, 152\n"
"s_max_i32 s71, s71, s29\n"
"v_add_f32 v248, v248, v122\n"
"v_add_f32 v249, v249, v123\n"
"v_mfma_f32_16x16x32_bf16 v[52:55], v[200:203], a[232:235], v[52:55]\n"
"s_max_i32 s94, s94, s82\n"
"v_cvt_pk_bf16_f32 v121, v122, v123\n"
"v_mfma_f32_16x16x32_bf16 v[52:55], v[204:207], a[248:251], v[52:55]\n"
"s_add_u32 s94, s94, s71\n"
"v_add_f32 v248, v248, v124\n"
"v_add_f32 v249, v249, v125\n"
"v_mfma_f32_16x16x32_bf16 v[60:63], v[204:207], a[252:255], v[60:63]\n"
"ds_read_b64_tr_b16 v[204:205], v233 offset:36864\n"
"v_cvt_pk_bf16_f32 v122, v124, v125\n"
"v_mfma_f32_16x16x32_bf16 v[60:63], v[200:203], a[236:239], v[60:63]\n"
"s_sub_u32 s28, s71, 36\n"
"s_max_i32 s29, s29, s28\n"
"ds_read_b64_tr_b16 v[206:207], v233 offset:43008\n"
"ds_read_b64_tr_b16 v[200:201], v233 offset:24576\n"
"v_mfma_f32_16x16x32_bf16 v[36:39], v[208:211], v[128:131], v[36:39]\n"
"ds_read_b64_tr_b16 v[202:203], v233 offset:30720\n"
"v_add_f32 v248, v248, v126\n"
"v_mfma_f32_16x16x32_bf16 v[36:39], v[212:215], v[160:163], v[36:39]\n"
"v_add_f32 v249, v249, v127\n"
"v_cvt_pk_bf16_f32 v123, v126, v127\n"
"v_mfma_f32_16x16x32_bf16 v[44:47], v[212:215], v[168:171], v[44:47]\n"
"v_mfma_f32_16x16x32_bf16 v[44:47], v[208:211], v[136:139], v[44:47]\n"
"v_mfma_f32_16x16x32_bf16 v[52:55], v[208:211], v[144:147], v[52:55]\n"
"v_mfma_f32_16x16x32_bf16 v[52:55], v[212:215], v[176:179], v[52:55]\n"
"v_mfma_f32_16x16x32_bf16 v[60:63], v[212:215], v[184:187], v[60:63]\n"
"s_sub_u32 s56, s56, 1\n"
"ds_read_b64_tr_b16 v[212:213], v234 offset:36864\n"
"v_mfma_f32_16x16x32_bf16 v[60:63], v[208:211], v[152:155], v[60:63]\n"
"s_waitcnt lgkmcnt(5)\n"
"ds_read_b64_tr_b16 v[214:215], v234 offset:43008\n"
"ds_read_b64_tr_b16 v[208:209], v234 offset:24576\n"
"s_cbranch_scc0 Lwa%=\n"
"s_branch Lwx%=\n"
"Lend%=:\n"
      :
      : [ka] "s"(__builtin_amdgcn_kernarg_segment_ptr()), [bid] "s"(bid), [w] "s"(w)
      : "v0","v1","v2","v3","v4","v5","v6","v7","v8","v9","v10","v11","v12","v13","v14","v15","v16","v17","v18","v19","v20","v21","v22","v23","v24","v25","v26","v27","v28","v29","v30","v31","v32","v33","v34","v35","v36","v37","v38","v39","v40","v41","v42","v43","v44","v45","v46","v47","v48","v49","v50","v51","v52","v53","v54","v55","v56","v57","v58","v59","v60","v61","v62","v63","v64","v65","v66","v67","v68","v69","v70","v71","v72","v73","v74","v75","v76","v77","v78","v79","v80","v81","v82","v83","v84","v85","v86","v87","v88","v89","v90","v91","v92","v93","v94","v95","v96","v97","v98","v99","v100","v101","v102","v103","v104","v105","v106","v107","v108","v109","v110","v111","v112","v113","v114","v115","v116","v117","v118","v119","v120","v121","v122","v123","v124","v125","v126","v127","v128","v129","v130","v131","v132","v133","v134","v135","v136","v137","v138","v139","v140","v141","v142","v143","v144","v145","v146","v147","v148","v149","v150","v151","v152","v153","v154","v155","v156","v157","v158","v159","v160","v161","v162","v163","v164","v165","v166","v167","v168","v169","v170","v171","v172","v173","v174","v175","v176","v177","v178","v179","v180","v181","v182","v183","v184","v185","v186","v187","v188","v189","v190","v191","v192","v193","v194","v195","v196","v197","v198","v199","v200","v201","v202","v203","v204","v205","v206","v207","v208","v209","v210","v211","v212","v213","v214","v215","v216","v217","v218","v219","v220","v221","v222","v223","v224","v225","v226","v227","v228","v229","v230","v231","v232","v233","v234","v235","v236","v237","v238","v239","v240","v241","v242","v243","v244","v245","v246","v247","v248","v249","v250","v251","v252","v253","v254","v255","a0","a1","a2","a3","a4","a5","a6","a7","a8","a9","a10","a11","a12","a13","a14","a15","a16","a17","a18","a19","a20","a21","a22","a23","a24","a25","a26","a27","a28","a29","a30","a31","a32","a33","a34","a35","a36","a37","a38","a39","a40","a41","a42","a43","a44","a45","a46","a47","a48","a49","a50","a51","a52","a53","a54","a55","a56","a57","a58","a59","a60","a61","a62","a63","a64","a65","a66","a67","a68","a69","a70","a71","a72","a73","a74","a75","a76","a77","a78","a79","a80","a81","a82","a83","a84","a85","a86","a87","a88","a89","a90","a91","a92","a93","a94","a95","a96","a97","a98","a99","a100","a101","a102","a103","a104","a105","a106","a107","a108","a109","a110","a111","a112","a113","a114","a115","a116","a117","a118","a119","a120","a121","a122","a123","a124","a125","a126","a127","a128","a129","a130","a131","a132","a133","a134","a135","a136","a137","a138","a139","a140","a141","a142","a143","a144","a145","a146","a147","a148","a149","a150","a151","a152","a153","a154","a155","a156","a157","a158","a159","a160","a161","a162","a163","a164","a165","a166","a167","a168","a169","a170","a171","a172","a173","a174","a175","a176","a177","a178","a179","a180","a181","a182","a183","a184","a185","a186","a187","a188","a189","a190","a191","a192","a193","a194","a195","a196","a197","a198","a199","a200","a201","a202","a203","a204","a205","a206","a207","a208","a209","a210","a211","a212","a213","a214","a215","a216","a217","a218","a219","a220","a221","a222","a223","a224","a225","a226","a227","a228","a229","a230","a231","a232","a233","a234","a235","a236","a237","a238","a239","a240","a241","a242","a243","a244","a245","a246","a247","a248","a249","a250","a251","a252","a253","a254","a255","s6","s7","s8","s9","s10","s11","s12","s13","s14","s15","s16","s17","s18","s19","s20","s21","s22","s23","s24","s25","s26","s27","s28","s29","s30","s31","s34","s35","s36","s37","s38","s39","s40","s41","s42","s43","s44","s45","s46","s47","s48","s49","s50","s51","s52","s53","s54","s55","s56","s57","s58","s59","s60","s61","s62","s63","s64","s65","s66","s67","s68","s69","s70","s71","s72","s73","s74","s75","s76","s77","s78","s79","s80","s81","s82","s83","s84","s85","s86","s87","s88","s89","s90","s91","s92","s93","s94","s95","s96","s97","s98","s99","vcc","scc","memory");
}

extern "C" void kernel_launch(void* const* d_in, const int* in_sizes, int n_in, void* d_out, int out_size,
                              void* d_ws, size_t ws_size, hipStream_t stream) {
  (void)hipFuncSetAttribute((const void*)mha_fused, hipFuncAttributeMaxDynamicSharedMemorySize, 163840);
  mha_fused<<<256, 256, 163840, stream>>>((const u16*)d_in[0], (const u16*)d_in[1], (const u16*)d_in[2],
                                          (const u16*)d_in[3], (const u16*)d_in[4], (char*)d_ws, (u16*)d_out);
}
```
